```python
import torch

def _softmax(x, axis=-1):
    return torch.softmax(x, dim=axis)
def _gelu(x):
    return torch.nn.functional.gelu(x, approximate="tanh")
def _silu(x):
    return torch.nn.functional.silu(x)
def _sigmoid(x):
    return torch.sigmoid(x)
def _layernorm(x, axis=-1, eps=1e-5):
    m = x.mean(dim=axis, keepdim=True)
    v = x.var(dim=axis, keepdim=True, unbiased=False)
    return (x - m) / torch.sqrt(v + eps)
def _scatter_add(tgt, idx, src, axis=0):
    return tgt.clone().index_add_(axis, idx, src)
def _segment_sum(src, seg, n):
    out = torch.zeros((n,) + src.shape[1:], dtype=src.dtype)
    return out.index_add_(0, seg, src)
def _pad(x, pw):
    flat = tuple(v for pair in reversed(pw) for v in pair)
    return torch.nn.functional.pad(x, flat)

kernel_name = 'interleave_layers__spectral_gfnet__mixer_token_mlp__prompt'

def setup_inputs():

    _w = (lambda a: (lambda t: t.to(torch.bfloat16) if t.is_floating_point() else t)(torch.as_tensor(a)))
    def _A_setup():
        B, S, D = 1, 2048, 448
        Df = D // 2 + 1
        return {
            "x": _w((lambda *s: torch.randn(*s, dtype=torch.bfloat16) * 0.05)(B, S, D)),
            "w_r": _w((lambda *s: torch.randn(*s, dtype=torch.bfloat16) * 1.6)(S, Df) * 0.02),
            "w_i": _w((lambda *s: torch.randn(*s, dtype=torch.bfloat16) * 1.6)(S, Df) * 0.02),
        }
    _A_in = _A_setup()

    def _B_setup():
        B, S, D, F = 1, 2048, 448, 1792
        return {
            "x": _w((lambda *s: torch.randn(*s, dtype=torch.bfloat16) * 0.05)(B, S, D)),
            "w1": _w((lambda *s: torch.randn(*s, dtype=torch.bfloat16) * 0.4)(S, F) * (S ** -0.5)),
            "b1": _w((lambda *s: torch.randn(*s, dtype=torch.bfloat16) * 3.2)(F) * 0.02),
            "w2": _w((lambda *s: torch.randn(*s, dtype=torch.bfloat16) * 0.8)(F, S) * (F ** -0.5)),
            "b2": _w((lambda *s: torch.randn(*s, dtype=torch.bfloat16) * 3.2)(S) * 0.02),
        }
    _B_in = _B_setup()

    return {
        "A_x": _A_in["x"],
        "A_w_r": _A_in["w_r"],
        "A_w_i": _A_in["w_i"],
        "B_x": _B_in["x"],
        "B_w1": _B_in["w1"],
        "B_b1": _B_in["b1"],
        "B_w2": _B_in["w2"],
        "B_b2": _B_in["b2"],
    }

def reference(A_x, A_w_r, A_w_i, B_x, B_w1, B_b1, B_w2, B_b2):
    def _A_ref(x, w_r, w_i):
        B, S, D = x.shape
        X = (lambda x, axes=(-2,-1): torch.fft.rfft2(x.float(), dim=axes))(x, axes=(-2, -1))
        W = w_r + 1j * w_i
        X = X * W
        y = (lambda x, s, axes=(-2,-1): torch.fft.irfft2(x, s=s, dim=axes).to(torch.bfloat16))(X, (S, D), axes=(-2, -1))
        return y

    def _B_ref(x, w1, b1, w2, b2):
        B, S, D = x.shape
        h = _layernorm(x, axis=-1)
        u = torch.einsum("bsd,sf->bdf", h, w1) + b1
        u = _gelu(u)
        y = torch.einsum("bdf,fs->bds", u, w2) + b2
        return (lambda x, *d: x.permute(*d))(y, 0, 2, 1)

    x = A_x
    x = x + _A_ref(_layernorm(x, axis=-1), A_w_r, A_w_i)
    x = x + _B_ref(_layernorm(x, axis=-1), B_w1, B_b1, B_w2, B_b2)
    x = x + _A_ref(_layernorm(x, axis=-1), A_w_r, A_w_i)
    x = x + _B_ref(_layernorm(x, axis=-1), B_w1, B_b1, B_w2, B_b2)
    return x
```

```cpp
#include <hip/hip_runtime.h>
#include <hip/hip_bf16.h>

struct Params {
  const void* x; const void* wr; const void* wi; const void* w1; const void* b1; const void* w2; const void* b2;
  void* out; void* ws;
};

__global__ void __launch_bounds__(256, 1) interleave_layers__spectral_gfnet__mixer_token_mlp__prompt_kernel(Params p) {
  extern __shared__ char lds[];
  unsigned tid = threadIdx.x;
  asm volatile(
    ".Lcode_begin:\n"
    "s_load_dwordx16 s[76:91], %0, 0x0\n"
    "s_load_dwordx2 s[92:93], %0, 0x40\n"
    "v_mov_b32 v112, %2\n"
    "v_lshrrev_b32 v113, 6, %2\n"
    "s_and_b32 s54, %1, 7\n"
    "s_lshr_b32 s55, %1, 3\n"
    "v_readfirstlane_b32 s52, v113\n"
    "s_mov_b32 s10, %1\n"
    "s_lshl_b32 s62, s54, 8\n"
    "s_lshl_b32 s74, s52, 6\n"
    "s_add_u32 s62, s62, s74\n"
    "s_add_u32 s62, s62, s55\n"
    "s_mul_i32 s62, s62, 896\n"
    "s_add_u32 s63, s62, 28672\n"
    "v_and_b32 v244, 63, v112\n"
    "v_lshlrev_b32 v244, 4, v244\n"
    "s_mov_b32 s18, 1835008\n"
    "s_mov_b32 s19, 0x20000\n"
    "s_waitcnt lgkmcnt(0)\n"
    "s_mov_b32 s16, s76\n"
    "s_and_b32 s17, s77, 0xffff\n"
    "buffer_load_dwordx4 v[166:169], v244, s[16:19], s62 offen\n"
    "buffer_load_dwordx4 v[170:173], v244, s[16:19], s63 offen\n"
    "s_getpc_b64 s[48:49]\n"
    ".Lpc:\n"
    "s_sub_u32 s48, s48, .Lpc-.Lcode_begin\n"
    "s_subb_u32 s49, s49, 0\n"
    "s_and_b32 s49, s49, 0xffff\n"
    "s_mov_b32 s50, .Lcode_end-.Lcode_begin\n"
    "s_mov_b32 s51, 0x20000\n"
    "s_and_b32 s74, s55, 15\n"
    "s_lshl_b32 s74, s74, 12\n"
    "v_lshlrev_b32 v114, 4, v112\n"
    "buffer_load_dwordx4 v[116:119], v114, s[48:51], s74 offen\n"
    "s_lshl_b32 s53, s54, 5\n"
    "s_add_u32 s53, s53, s55\n"
    "v_and_b32 v16, 3, v112\n"
    "v_mul_u32_u24 v16, 57600, v16\n"
    "v_lshrrev_b32 v17, 2, v112\n"
    "v_lshl_add_u32 v16, v17, 1, v16\n"
    "v_cmp_gt_u32 vcc, 33, v17\n"
    "v_mov_b32 v17, 1073741824\n"
    "s_bfe_u32 s70, s53, 0x20004\n"
    "s_lshl_b32 s70, s70, 9\n"
    "s_and_b32 s71, s53, 15\n"
    "s_add_u32 s70, s70, s71\n"
    "s_lshr_b32 s71, s53, 6\n"
    "s_lshl_b32 s71, s71, 4\n"
    "s_add_u32 s70, s70, s71\n"
    "s_mul_i32 s70, s70, 450\n"
    "s_add_u32 s71, s70, 28800\n"
    "v_cndmask_b32 v17, v17, v16, vcc\n"
    "s_mov_b32 s20, s78\n"
    "s_and_b32 s21, s79, 0xffff\n"
    "s_mov_b32 s24, s80\n"
    "s_and_b32 s25, s81, 0xffff\n"
    "s_mov_b32 s22, 921600\n"
    "s_mov_b32 s23, 0x20000\n"
    "s_mov_b32 s26, 921600\n"
    "s_mov_b32 s27, 0x20000\n"
    "buffer_load_ushort v0, v16, s[20:23], s70 offen offset:0\n"
    "buffer_load_ushort v8, v16, s[24:27], s70 offen offset:0\n"
    "buffer_load_ushort v1, v16, s[20:23], s71 offen offset:0\n"
    "buffer_load_ushort v9, v16, s[24:27], s71 offen offset:0\n"
    "buffer_load_ushort v2, v16, s[20:23], s70 offen offset:128\n"
    "buffer_load_ushort v10, v16, s[24:27], s70 offen offset:128\n"
    "buffer_load_ushort v3, v16, s[20:23], s71 offen offset:128\n"
    "buffer_load_ushort v11, v16, s[24:27], s71 offen offset:128\n"
    "buffer_load_ushort v4, v16, s[20:23], s70 offen offset:256\n"
    "buffer_load_ushort v12, v16, s[24:27], s70 offen offset:256\n"
    "buffer_load_ushort v5, v16, s[20:23], s71 offen offset:256\n"
    "buffer_load_ushort v13, v16, s[24:27], s71 offen offset:256\n"
    "buffer_load_ushort v6, v17, s[20:23], s70 offen offset:384\n"
    "buffer_load_ushort v14, v17, s[24:27], s70 offen offset:384\n"
    "buffer_load_ushort v7, v17, s[20:23], s71 offen offset:384\n"
    "buffer_load_ushort v15, v17, s[24:27], s71 offen offset:384\n"
    "s_mov_b32 s12, s92\n"
    "s_and_b32 s13, s93, 0xffff\n"
    "s_mov_b32 s14, 33554432\n"
    "s_mov_b32 s15, 0x20000\n"
    "buffer_load_dword v245, off, s[12:15], 0 sc1\n"
    "s_mov_b32 s28, s82\n"
    "s_and_b32 s29, s83, 0xffff\n"
    "s_mov_b32 s36, s84\n"
    "s_and_b32 s37, s85, 0xffff\n"
    "s_mov_b32 s32, s86\n"
    "s_and_b32 s33, s87, 0xffff\n"
    "s_mov_b32 s40, s88\n"
    "s_and_b32 s41, s89, 0xffff\n"
    "s_mov_b32 s44, s90\n"
    "s_and_b32 s45, s91, 0xffff\n"
    "s_mov_b32 s30, 7340032\n"
    "s_mov_b32 s31, 0x20000\n"
    "s_mov_b32 s38, 3584\n"
    "s_mov_b32 s39, 0x20000\n"
    "s_mov_b32 s34, 7340032\n"
    "s_mov_b32 s35, 0x20000\n"
    "s_mov_b32 s42, 4096\n"
    "s_mov_b32 s43, 0x20000\n"
    "s_mov_b32 s46, 1835008\n"
    "s_mov_b32 s47, 0x20000\n"
    "s_and_b32 s56, s54, 1\n"
    "s_lshl_b32 s56, s56, 2\n"
    "s_and_b32 s74, s55, 3\n"
    "s_add_u32 s56, s56, s74\n"
    "s_lshr_b32 s75, s54, 1\n"
    "s_lshl_b32 s6, s75, 3\n"
    "s_lshr_b32 s74, s55, 2\n"
    "s_add_u32 s6, s6, s74\n"
    "s_mul_i32 s57, s75, 7\n"
    "s_cmp_lt_u32 s74, 7\n"
    "s_cselect_b32 s7, 0, 1073741824\n"
    "s_min_u32 s74, s74, 6\n"
    "s_add_u32 s57, s57, s74\n"
    "s_mov_b32 s60, 200000\n"
    "s_lshl_b32 s11, s52, 15\n"
    "s_mul_i32 s48, s52, 1835008\n"
    "s_lshl_b32 s74, s57, 7\n"
    "s_add_u32 s48, s48, s74\n"
    "s_mul_i32 s49, s52, 1835008\n"
    "s_and_b32 s50, s56, 0\n"
    "s_and_b32 s51, s50, 1\n"
    "s_mov_b32 s4, 0x00ff00ff\n"
    "s_mov_b32 s5, 0x00ff00ff\n"
    "s_lshl_b32 s74, s6, 7\n"
    "s_add_u32 s49, s49, s74\n"
    "s_mov_b32 s68, 0x3fff3fff\n"
    "s_mov_b32 s69, 0x3fff3fff\n"
    "s_mov_b32 s73, 0x80008000\n"
    "s_mov_b32 s72, 0x36124925\n"
    "s_cmp_eq_u32 s53, 0\n"
    "s_cselect_b32 s72, 0x35924925, s72\n"
    "s_cmp_eq_u32 s53, 224\n"
    "s_cselect_b32 s72, 0x35924925, s72\n"
    "s_mov_b32 s9, 0\n"
    "s_min_u32 s8, s53, 224\n"
    ".Lmark_start_1:\n"
    "v_and_b32 v85, 7, v112\n"
    "v_lshlrev_b32 v85, 4, v85\n"
    "v_bfe_u32 v90, v112, 3, 3\n"
    "v_mul_u32_u24 v90, 114688, v90\n"
    "v_add_u32 v85, v85, v90\n"
    "v_and_b32 v86, 7, v112\n"
    "v_lshlrev_b32 v86, 4, v86\n"
    "v_bfe_u32 v90, v112, 3, 3\n"
    "v_mul_u32_u24 v90, 4096, v90\n"
    "v_add_u32 v86, v86, v90\n"
    "v_mov_b32 v80, -1\n"
    "v_mov_b32 v81, -1\n"
    "v_mov_b32 v82, -1\n"
    "v_mov_b32 v83, -1\n"
    "s_lshl_b32 s74, s53, 13\n"
    "v_lshl_add_u32 v84, v112, 4, s74\n"
    "s_mov_b32 s25, 8388608\n"
    "buffer_store_dwordx4 v[80:83], v84, s[12:15], s25 offen sc1\n"
    "s_mov_b32 s25, 8392704\n"
    "buffer_store_dwordx4 v[80:83], v84, s[12:15], s25 offen sc1\n"
    "v_bfe_u32 v90, v112, 4, 2\n"
    "v_lshlrev_b32 v68, 10, v90\n"
    "v_and_b32 v90, 1, v90\n"
    "v_lshlrev_b32 v90, 1, v90\n"
    "v_bfe_u32 v69, v112, 3, 1\n"
    "v_or_b32 v90, v90, v69\n"
    "v_xor_b32 v90, s52, v90\n"
    "v_lshl_add_u32 v68, v90, 5, v68\n"
    "v_bfe_u32 v90, v112, 2, 2\n"
    "v_lshl_add_u32 v68, v90, 7, v68\n"
    "v_and_b32 v90, 3, v112\n"
    "v_lshl_add_u32 v68, v90, 3, v68\n"
    "v_add_u32 v68, s11, v68\n"
    "v_xor_b32 v69, 32, v68\n"
    "v_xor_b32 v70, 64, v68\n"
    "v_xor_b32 v71, 96, v68\n"
    "v_bfe_u32 v91, v112, 4, 1\n"
    "v_lshlrev_b32 v91, 5, v91\n"
    "v_and_b32 v90, 63, v112\n"
    "v_lshlrev_b32 v90, 4, v90\n"
    "v_xor_b32 v90, v90, v91\n"
    "v_add_u32 v90, s11, v90\n"
    "v_xor_b32 v91, 64, v90\n"
    "v_and_b32 v238, 15, v112\n"
    "v_bfe_u32 v239, v112, 4, 2\n"
    "v_cvt_f32_u32 v240, v238\n"
    "v_lshlrev_b32 v241, 2, v239\n"
    "v_cvt_f32_u32 v241, v241\n"
    "v_mul_f32 v242, 0x3d000000, v240\n"
    "v_mul_f32 v190, v241, v242\n"
    "v_add_f32 v191, v190, v242\n"
    "v_add_f32 v192, v191, v242\n"
    "v_add_f32 v193, v192, v242\n"
    "v_sin_f32 v198, v190\n"
    "v_sin_f32 v199, v191\n"
    "v_sin_f32 v200, v192\n"
    "v_sin_f32 v201, v193\n"
    "v_cos_f32 v194, v190\n"
    "v_cos_f32 v195, v191\n"
    "v_cos_f32 v196, v192\n"
    "v_cos_f32 v197, v193\n"
    "v_and_b32 v243, 1, v238\n"
    "v_mul_u32_u24 v243, 0x8000, v243\n"
    "v_lshl_or_b32 v243, v243, 16, v243\n"
    "v_cvt_pk_bf16_f32 v114, v194, v195\n"
    "s_waitcnt vmcnt(19)\n"
    "v_cvt_pk_bf16_f32 v118, -v198, -v199\n"
    "v_cvt_pk_bf16_f32 v115, v196, v197\n"
    "v_cvt_pk_bf16_f32 v119, -v200, -v201\n"
    "v_xor_b32 v116, v114, v243\n"
    "v_xor_b32 v117, v115, v243\n"
    "v_xor_b32 v120, v118, v243\n"
    "v_xor_b32 v121, v119, v243\n"
    "v_xor_b32 v122, 0x80000000, v114\n"
    "v_xor_b32 v123, 0x80000000, v115\n"
    "v_xor_b32 v124, 0x80000000, v116\n"
    "v_xor_b32 v125, 0x80000000, v117\n"
    "v_xor_b32 v126, 0x80000000, v118\n"
    "v_xor_b32 v127, 0x80000000, v119\n"
    "v_xor_b32 v128, 0x80000000, v120\n"
    "v_xor_b32 v129, 0x80000000, v121\n"
    "v_mul_f32 v242, 0x3b124925, v240\n"
    "v_mul_f32 v190, v241, v242\n"
    "v_add_f32 v191, v190, v242\n"
    "v_add_f32 v192, v191, v242\n"
    "v_add_f32 v193, v192, v242\n"
    "v_sin_f32 v138, v190\n"
    "v_sin_f32 v139, v191\n"
    "v_sin_f32 v140, v192\n"
    "v_sin_f32 v141, v193\n"
    "v_cos_f32 v130, v190\n"
    "v_cos_f32 v131, v191\n"
    "v_cos_f32 v132, v192\n"
    "v_cos_f32 v133, v193\n"
    "v_add_f32 v242, 0x41800000, v240\n"
    "v_mul_f32 v242, 0x3b124925, v242\n"
    "v_mul_f32 v190, v241, v242\n"
    "v_add_f32 v191, v190, v242\n"
    "v_add_f32 v192, v191, v242\n"
    "v_add_f32 v193, v192, v242\n"
    "v_sin_f32 v142, v190\n"
    "v_sin_f32 v143, v191\n"
    "v_sin_f32 v144, v192\n"
    "v_sin_f32 v145, v193\n"
    "v_cos_f32 v134, v190\n"
    "v_cos_f32 v135, v191\n"
    "v_cos_f32 v136, v192\n"
    "v_cos_f32 v137, v193\n"
    "v_lshrrev_b32 v242, 1, v238\n"
    "v_cvt_f32_u32 v242, v242\n"
    "v_mul_f32 v242, 0x3d924925, v242\n"
    "v_and_b32 v190, 1, v238\n"
    "v_cvt_f32_u32 v190, v190\n"
    "v_mul_f32 v190, 0x3e800000, v190\n"
    "v_fmac_f32 v190, v241, v242\n"
    "v_add_f32 v191, v190, v242\n"
    "v_add_f32 v192, v191, v242\n"
    "v_add_f32 v193, v192, v242\n"
    "v_sin_f32 v198, v190\n"
    "v_sin_f32 v199, v191\n"
    "v_sin_f32 v200, v192\n"
    "v_sin_f32 v201, v193\n"
    "v_cos_f32 v194, v190\n"
    "v_cos_f32 v195, v191\n"
    "v_cos_f32 v196, v192\n"
    "v_cos_f32 v197, v193\n"
    "v_cmp_ne_u32 vcc, 3, v239\n"
    "v_cvt_pk_bf16_f32 v146, v194, v198\n"
    "v_cvt_pk_bf16_f32 v147, v195, v199\n"
    "v_cvt_pk_bf16_f32 v148, v196, v200\n"
    "v_cvt_pk_bf16_f32 v149, v197, v201\n"
    "v_cndmask_b32 v148, 0, v148, vcc\n"
    "v_cndmask_b32 v149, 0, v149, vcc\n"
    "v_and_b32 v246, 15, v112\n"
    "v_bfe_u32 v249, v112, 4, 2\n"
    "s_mul_i32 s74, s52, 2112\n"
    "s_add_u32 s74, s74, 131072\n"
    "v_lshl_add_u32 v246, v249, 6, v246\n"
    "v_lshl_add_u32 v246, v246, 3, s74\n"
    "v_and_b32 v247, 3, v112\n"
    "v_mul_u32_u24 v247, 2112, v247\n"
    "v_lshrrev_b32 v248, 2, v112\n"
    "v_lshl_add_u32 v247, v248, 3, v247\n"
    "v_add_u32 v247, 131072, v247\n"
    "s_lshl_b32 s74, s53, 14\n"
    "s_add_u32 s74, s74, 2097152\n"
    "v_lshl_add_u32 v248, v112, 4, s74\n"
    "s_mov_b32 s79, 4096\n"
    "s_mov_b32 s80, 8192\n"
    "s_mov_b32 s81, 12288\n"
    "v_cmp_gt_u32 vcc, 132, v112\n"
    "v_mov_b32 v249, 1073741824\n"
    "s_nop 0\n"
    "v_cndmask_b32 v249, v249, v248, vcc\n"
    "v_and_b32 v40, 3, v112\n"
    "v_cmp_eq_u32 s[16:17], 1, v40\n"
    "v_cmp_eq_u32 s[18:19], 2, v40\n"
    "v_cmp_eq_u32 s[26:27], 3, v40\n"
    "v_lshlrev_b32 v40, 8, v40\n"
    "v_and_b32 v41, 0xfc, v112\n"
    "s_lshl_b32 s74, s53, 10\n"
    "s_add_u32 s74, s74, 29360128\n"
    "v_add3_u32 v40, v40, v41, s74\n"
    "s_lshl_b32 s74, s52, 11\n"
    "s_add_u32 s74, s74, 147968\n"
    "v_add_u32 v244, s74, v244\n"
    "v_mul_u32_u24 v242, 112, v239\n"
    "v_lshl_add_u32 v242, v238, 1, v242\n"
    "v_add_u32 v242, s74, v242\n"
    "v_mov_b32 v150, 0\n"
    "v_mov_b32 v151, 0\n"
    "v_mov_b32 v152, 0\n"
    "v_mov_b32 v153, 0\n"
    "v_mov_b32 v154, 0\n"
    "v_mov_b32 v155, 0\n"
    "v_mov_b32 v156, 0\n"
    "v_mov_b32 v157, 0\n"
    "v_mov_b32 v158, 0\n"
    "v_mov_b32 v159, 0\n"
    "v_mov_b32 v160, 0\n"
    "v_mov_b32 v161, 0\n"
    "v_mov_b32 v162, 0\n"
    "v_mov_b32 v163, 0\n"
    "v_mov_b32 v164, 0\n"
    "v_mov_b32 v165, 0\n"
    "ds_write_b128 v244, v[166:169]\n"
    "ds_write_b128 v244, v[170:173] offset:1024\n"
    "s_waitcnt lgkmcnt(0)\n"
    "s_mov_b64 exec, s[68:69]\n"
    "ds_read_u16_d16_hi v150, v242 offset:0\n"
    "ds_read_u16_d16_hi v151, v242 offset:28\n"
    "ds_read_u16_d16_hi v152, v242 offset:56\n"
    "ds_read_u16_d16_hi v153, v242 offset:84\n"
    "ds_read_u16_d16_hi v154, v242 offset:448\n"
    "ds_read_u16_d16_hi v155, v242 offset:476\n"
    "ds_read_u16_d16_hi v156, v242 offset:504\n"
    "ds_read_u16_d16_hi v157, v242 offset:532\n"
    "ds_read_u16_d16_hi v158, v242 offset:1024\n"
    "ds_read_u16_d16_hi v159, v242 offset:1052\n"
    "ds_read_u16_d16_hi v160, v242 offset:1080\n"
    "ds_read_u16_d16_hi v161, v242 offset:1108\n"
    "ds_read_u16_d16_hi v162, v242 offset:1472\n"
    "ds_read_u16_d16_hi v163, v242 offset:1500\n"
    "ds_read_u16_d16_hi v164, v242 offset:1528\n"
    "ds_read_u16_d16_hi v165, v242 offset:1556\n"
    "s_mov_b64 exec, -1\n"
    "s_lshl_b32 s61, s53, 2\n"
    "s_add_u32 s61, s61, s52\n"
    "s_mul_i32 s61, s61, 7168\n"
    "s_add_u32 s61, s61, 0\n"
    "v_and_b32 v75, 63, v112\n"
    "v_min_u32 v75, 55, v75\n"
    "v_lshlrev_b32 v75, 7, v75\n"
    "buffer_load_dword v74, v75, s[28:31], s61 offen\n"
    ".Lmark_p1in_2:\n"
    "s_waitcnt lgkmcnt(12)\n"
    "v_pk_add_f32 v[174:175], v[150:151], v[152:153]\n"
    "v_pk_mul_f32 v[176:177], v[150:151], v[150:151]\n"
    "s_waitcnt lgkmcnt(4)\n"
    "v_pk_add_f32 v[178:179], v[158:159], v[160:161]\n"
    "v_pk_mul_f32 v[180:181], v[158:159], v[158:159]\n"
    "v_pk_add_f32 v[174:175], v[174:175], v[154:155]\n"
    "s_waitcnt lgkmcnt(2)\n"
    "v_pk_add_f32 v[178:179], v[178:179], v[162:163]\n"
    "v_pk_add_f32 v[174:175], v[174:175], v[156:157]\n"
    "s_waitcnt lgkmcnt(0)\n"
    "v_pk_add_f32 v[178:179], v[178:179], v[164:165]\n"
    "v_pk_fma_f32 v[176:177], v[152:153], v[152:153], v[176:177]\n"
    "v_pk_fma_f32 v[180:181], v[160:161], v[160:161], v[180:181]\n"
    "v_pk_fma_f32 v[176:177], v[154:155], v[154:155], v[176:177]\n"
    "v_pk_fma_f32 v[180:181], v[162:163], v[162:163], v[180:181]\n"
    "v_pk_fma_f32 v[176:177], v[156:157], v[156:157], v[176:177]\n"
    "v_pk_fma_f32 v[180:181], v[164:165], v[164:165], v[180:181]\n"
    "v_add_f32 v174, v174, v175\n"
    "v_add_f32 v178, v178, v179\n"
    "v_add_f32 v176, v176, v177\n"
    "v_add_f32 v180, v180, v181\n"
    "s_nop 0\n"
    "v_permlane32_swap_b32 v174, v176\n"
    "v_permlane32_swap_b32 v178, v180\n"
    "v_add_f32 v174, v174, v176\n"
    "v_add_f32 v178, v178, v180\n"
    "s_nop 1\n"
    "v_permlane16_swap_b32 v174, v178\n"
    "v_add_f32 v174, v174, v178\n"
    "s_nop 1\n"
    "v_add_f32_dpp v174, v174, v174 row_ror:8 row_mask:0xf bank_mask:0xf\n"
    "s_nop 1\n"
    "v_add_f32_dpp v174, v174, v174 row_ror:4 row_mask:0xf bank_mask:0xf\n"
    "s_nop 1\n"
    "v_add_f32_dpp v174, v174, v174 row_ror:2 row_mask:0xf bank_mask:0xf\n"
    "s_nop 1\n"
    "v_add_f32_dpp v174, v174, v174 row_ror:1 row_mask:0xf bank_mask:0xf\n"
    "v_mul_f32 v174, 0x3b124925, v174\n"
    "v_mov_b32 v175, v174\n"
    "s_nop 1\n"
    "v_permlane32_swap_b32 v174, v175\n"
    "v_fma_f32 v175, -v174, v174, v175\n"
    "v_max_f32 v175, 0, v175\n"
    "v_add_f32 v176, 0x3727c5ac, v175\n"
    "v_rsq_f32 v176, v176\n"
    "s_nop 0\n"
    "v_mul_f32 v174, -v174, v176\n"
    "v_readlane_b32 s64, v176, 0\n"
    "v_readlane_b32 s66, v176, 16\n"
    "v_readlane_b32 s65, v174, 0\n"
    "v_readlane_b32 s67, v174, 16\n"
    "s_nop 0\n"
    "v_pk_fma_f32 v[190:191], v[150:151], s[64:65], s[64:65] op_sel:[0,0,1] op_sel_hi:[1,0,1]\n"
    "v_pk_fma_f32 v[192:193], v[152:153], s[64:65], s[64:65] op_sel:[0,0,1] op_sel_hi:[1,0,1]\n"
    "v_pk_fma_f32 v[194:195], v[154:155], s[64:65], s[64:65] op_sel:[0,0,1] op_sel_hi:[1,0,1]\n"
    "v_pk_fma_f32 v[196:197], v[156:157], s[64:65], s[64:65] op_sel:[0,0,1] op_sel_hi:[1,0,1]\n"
    "v_pk_fma_f32 v[198:199], v[158:159], s[66:67], s[66:67] op_sel:[0,0,1] op_sel_hi:[1,0,1]\n"
    "v_pk_fma_f32 v[200:201], v[160:161], s[66:67], s[66:67] op_sel:[0,0,1] op_sel_hi:[1,0,1]\n"
    "v_pk_fma_f32 v[202:203], v[162:163], s[66:67], s[66:67] op_sel:[0,0,1] op_sel_hi:[1,0,1]\n"
    "v_pk_fma_f32 v[204:205], v[164:165], s[66:67], s[66:67] op_sel:[0,0,1] op_sel_hi:[1,0,1]\n"
    "v_cvt_pk_bf16_f32 v182, v190, v191\n"
    "v_cvt_pk_bf16_f32 v183, v192, v193\n"
    "v_cvt_pk_bf16_f32 v184, v194, v195\n"
    "v_cvt_pk_bf16_f32 v185, v196, v197\n"
    "v_cvt_pk_bf16_f32 v186, v198, v199\n"
    "v_cvt_pk_bf16_f32 v187, v200, v201\n"
    "v_cvt_pk_bf16_f32 v188, v202, v203\n"
    "v_cvt_pk_bf16_f32 v189, v204, v205\n"
    "v_mfma_f32_16x16x32_bf16 v[190:193], v[182:185], v[114:117], 0\n"
    "v_mfma_f32_16x16x32_bf16 v[194:197], v[182:185], v[118:121], 0\n"
    "v_mfma_f32_16x16x32_bf16 v[198:201], v[182:185], v[122:125], 0\n"
    "v_mfma_f32_16x16x32_bf16 v[202:205], v[182:185], v[126:129], 0\n"
    "v_mfma_f32_16x16x32_bf16 v[206:209], v[186:189], v[114:117], 0\n"
    "v_mfma_f32_16x16x32_bf16 v[210:213], v[186:189], v[118:121], 0\n"
    "v_mfma_f32_16x16x32_bf16 v[214:217], v[186:189], v[122:125], 0\n"
    "v_mfma_f32_16x16x32_bf16 v[218:221], v[186:189], v[126:129], 0\n"
    "s_waitcnt vmcnt(3)\n"
    "v_readfirstlane_b32 s58, v245\n"
    "s_nop 0\n"
    "s_add_u32 s58, s58, 1\n"
    "v_lshl_or_b32 v32, v8, 16, v0\n"
    "v_lshl_or_b32 v33, v9, 16, v1\n"
    "v_lshl_or_b32 v34, v10, 16, v2\n"
    "v_lshl_or_b32 v35, v11, 16, v3\n"
    "v_lshl_or_b32 v36, v12, 16, v4\n"
    "v_lshl_or_b32 v37, v13, 16, v5\n"
    "v_lshl_or_b32 v38, v14, 16, v6\n"
    "v_lshl_or_b32 v39, v15, 16, v7\n"
    "v_pk_mul_f32 v[166:167], v[190:191], v[130:131]\n"
    "v_pk_mul_f32 v[170:171], v[194:195], v[130:131]\n"
    "v_pk_mul_f32 v[168:169], v[192:193], v[132:133]\n"
    "v_pk_mul_f32 v[172:173], v[196:197], v[132:133]\n"
    "v_pk_fma_f32 v[166:167], v[194:195], v[138:139], v[166:167]\n"
    "v_pk_fma_f32 v[170:171], v[190:191], v[138:139], v[170:171] neg_lo:[1,0,0] neg_hi:[1,0,0]\n"
    "v_pk_fma_f32 v[168:169], v[196:197], v[140:141], v[168:169]\n"
    "v_pk_fma_f32 v[172:173], v[192:193], v[140:141], v[172:173] neg_lo:[1,0,0] neg_hi:[1,0,0]\n"
    "v_cvt_pk_bf16_f32 v222, v166, v170\n"
    "v_cvt_pk_bf16_f32 v223, v167, v171\n"
    "v_cvt_pk_bf16_f32 v224, v168, v172\n"
    "v_cvt_pk_bf16_f32 v225, v169, v173\n"
    "v_pk_mul_f32 v[174:175], v[198:199], v[134:135]\n"
    "v_pk_mul_f32 v[178:179], v[202:203], v[134:135]\n"
    "v_pk_mul_f32 v[176:177], v[200:201], v[136:137]\n"
    "v_pk_mul_f32 v[180:181], v[204:205], v[136:137]\n"
    "v_pk_fma_f32 v[174:175], v[202:203], v[142:143], v[174:175]\n"
    "v_pk_fma_f32 v[178:179], v[198:199], v[142:143], v[178:179] neg_lo:[1,0,0] neg_hi:[1,0,0]\n"
    "v_pk_fma_f32 v[176:177], v[204:205], v[144:145], v[176:177]\n"
    "v_pk_fma_f32 v[180:181], v[200:201], v[144:145], v[180:181] neg_lo:[1,0,0] neg_hi:[1,0,0]\n"
    "v_cvt_pk_bf16_f32 v226, v174, v178\n"
    "v_cvt_pk_bf16_f32 v227, v175, v179\n"
    "v_cvt_pk_bf16_f32 v228, v176, v180\n"
    "v_cvt_pk_bf16_f32 v229, v177, v181\n"
    "v_pk_mul_f32 v[174:175], v[206:207], v[130:131]\n"
    "v_pk_mul_f32 v[178:179], v[210:211], v[130:131]\n"
    "v_pk_mul_f32 v[176:177], v[208:209], v[132:133]\n"
    "v_pk_mul_f32 v[180:181], v[212:213], v[132:133]\n"
    "v_pk_fma_f32 v[174:175], v[210:211], v[138:139], v[174:175]\n"
    "v_pk_fma_f32 v[178:179], v[206:207], v[138:139], v[178:179] neg_lo:[1,0,0] neg_hi:[1,0,0]\n"
    "v_pk_fma_f32 v[176:177], v[212:213], v[140:141], v[176:177]\n"
    "v_pk_fma_f32 v[180:181], v[208:209], v[140:141], v[180:181] neg_lo:[1,0,0] neg_hi:[1,0,0]\n"
    "v_cvt_pk_bf16_f32 v230, v174, v178\n"
    "v_cvt_pk_bf16_f32 v231, v175, v179\n"
    "v_cvt_pk_bf16_f32 v232, v176, v180\n"
    "v_cvt_pk_bf16_f32 v233, v177, v181\n"
    "v_pk_mul_f32 v[166:167], v[214:215], v[134:135]\n"
    "v_pk_mul_f32 v[170:171], v[218:219], v[134:135]\n"
    "v_pk_mul_f32 v[168:169], v[216:217], v[136:137]\n"
    "v_pk_mul_f32 v[172:173], v[220:221], v[136:137]\n"
    "v_pk_fma_f32 v[166:167], v[218:219], v[142:143], v[166:167]\n"
    "v_pk_fma_f32 v[170:171], v[214:215], v[142:143], v[170:171] neg_lo:[1,0,0] neg_hi:[1,0,0]\n"
    "v_pk_fma_f32 v[168:169], v[220:221], v[144:145], v[168:169]\n"
    "v_pk_fma_f32 v[172:173], v[216:217], v[144:145], v[172:173] neg_lo:[1,0,0] neg_hi:[1,0,0]\n"
    "v_cvt_pk_bf16_f32 v234, v166, v170\n"
    "v_cvt_pk_bf16_f32 v235, v167, v171\n"
    "v_cvt_pk_bf16_f32 v236, v168, v172\n"
    "v_cvt_pk_bf16_f32 v237, v169, v173\n"
    "v_mfma_f32_16x16x32_bf16 v[190:193], v[146:149], v[222:225], 0\n"
    "v_mfma_f32_16x16x32_bf16 v[198:201], v[146:149], v[226:229], 0\n"
    "v_mfma_f32_16x16x32_bf16 v[194:197], v[146:149], v[230:233], 0\n"
    "v_mfma_f32_16x16x32_bf16 v[202:205], v[146:149], v[234:237], 0\n"
    "v_cvt_pk_bf16_f32 v222, v190, v191\n"
    "s_nop 2\n"
    "v_cvt_pk_bf16_f32 v223, v194, v195\n"
    "v_cvt_pk_bf16_f32 v224, v192, v193\n"
    "v_cvt_pk_bf16_f32 v225, v196, v197\n"
    "v_cvt_pk_bf16_f32 v226, v198, v199\n"
    "v_cvt_pk_bf16_f32 v227, v202, v203\n"
    "v_cvt_pk_bf16_f32 v228, v200, v201\n"
    "v_cvt_pk_bf16_f32 v229, v204, v205\n"
    "ds_write_b64 v246, v[222:223] offset:0\n"
    "ds_write_b64 v246, v[224:225] offset:256\n"
    "ds_write_b64 v246, v[226:227] offset:128\n"
    "ds_write_b64 v246, v[228:229] offset:384\n"
    "v_mov_b32 v193, v33\n"
    "v_add_u32 v44, v32, v33\n"
    "v_mov_b32 v197, v35\n"
    "v_add_u32 v45, v34, v35\n"
    "v_mov_b32 v201, v37\n"
    "v_add_u32 v46, v36, v37\n"
    "v_mov_b32 v205, v39\n"
    "v_add_u32 v47, v38, v39\n"
    "s_waitcnt vmcnt(1)\n"
    "s_waitcnt lgkmcnt(0)\n"
    "s_barrier\n"
    "ds_read_b64 v[190:191], v247 offset:0\n"
    "ds_read_b64 v[194:195], v247 offset:512\n"
    "ds_read_b64 v[198:199], v247 offset:1024\n"
    "ds_read_b64 v[202:203], v247 offset:1536\n"
    "s_waitcnt lgkmcnt(3)\n"
    "v_add3_u32 v44, v44, v190, v191\n"
    "v_mov_b32 v192, v191\n"
    "v_mov_b32 v191, v32\n"
    "buffer_store_dwordx4 v[190:193], v248, s[12:15], 0 offen sc1\n"
    "s_waitcnt lgkmcnt(2)\n"
    "v_add3_u32 v45, v45, v194, v195\n"
    "v_mov_b32 v196, v195\n"
    "v_mov_b32 v195, v34\n"
    "buffer_store_dwordx4 v[194:197], v248, s[12:15], s79 offen sc1\n"
    "s_waitcnt lgkmcnt(1)\n"
    "v_add3_u32 v46, v46, v198, v199\n"
    "v_mov_b32 v200, v199\n"
    "v_mov_b32 v199, v36\n"
    "buffer_store_dwordx4 v[198:201], v248, s[12:15], s80 offen sc1\n"
    "s_waitcnt lgkmcnt(0)\n"
    "v_add3_u32 v47, v47, v202, v203\n"
    "v_mov_b32 v204, v203\n"
    "v_mov_b32 v203, v38\n"
    "buffer_store_dwordx4 v[202:205], v249, s[12:15], s81 offen sc1\n"
    "v_add_u32_dpp v44, v44, v44 quad_perm:[1,0,3,2] row_mask:0xf bank_mask:0xf\n"
    "v_add_u32_dpp v45, v45, v45 quad_perm:[1,0,3,2] row_mask:0xf bank_mask:0xf\n"
    "v_add_u32_dpp v46, v46, v46 quad_perm:[1,0,3,2] row_mask:0xf bank_mask:0xf\n"
    "v_add_u32_dpp v47, v47, v47 quad_perm:[1,0,3,2] row_mask:0xf bank_mask:0xf\n"
    "v_add_u32_dpp v44, v44, v44 quad_perm:[2,3,0,1] row_mask:0xf bank_mask:0xf\n"
    "v_add_u32_dpp v45, v45, v45 quad_perm:[2,3,0,1] row_mask:0xf bank_mask:0xf\n"
    "v_add_u32_dpp v46, v46, v46 quad_perm:[2,3,0,1] row_mask:0xf bank_mask:0xf\n"
    "v_add_u32_dpp v47, v47, v47 quad_perm:[2,3,0,1] row_mask:0xf bank_mask:0xf\n"
    "v_cndmask_b32 v44, v44, v45, s[16:17]\n"
    "v_cndmask_b32 v44, v44, v46, s[18:19]\n"
    "v_cndmask_b32 v44, v44, v47, s[26:27]\n"
    "v_sub_u32 v44, s58, v44\n"
    "buffer_store_dword v44, v40, s[12:15], 0 offen sc1\n"
    ".Lmark_p1out_3:\n"
    "v_cvt_pk_bf16_f32 v96, v150, v151\n"
    "v_cvt_pk_bf16_f32 v97, v152, v153\n"
    "v_cvt_pk_bf16_f32 v98, v154, v155\n"
    "v_cvt_pk_bf16_f32 v99, v156, v157\n"
    "v_cvt_pk_bf16_f32 v100, v158, v159\n"
    "v_cvt_pk_bf16_f32 v101, v160, v161\n"
    "v_cvt_pk_bf16_f32 v102, v162, v163\n"
    "v_cvt_pk_bf16_f32 v103, v164, v165\n"
    "s_add_u32 s59, s48, 0\n"
    "buffer_load_dwordx4 a[0:3], v85, s[28:31], s59 offen offset:0\n"
    "buffer_load_dwordx4 a[4:7], v85, s[28:31], s59 offen offset:3584\n"
    "s_add_u32 s59, s48, 7168\n"
    "buffer_load_dwordx4 a[8:11], v85, s[28:31], s59 offen offset:0\n"
    "buffer_load_dwordx4 a[12:15], v85, s[28:31], s59 offen offset:3584\n"
    "v_bfe_u32 v114, v112, 2, 4\n"
    "v_lshlrev_b32 v115, 4, v114\n"
    "v_lshlrev_b32 v114, 14, v114\n"
    "v_and_b32 v252, 3, v112\n"
    "v_lshl_add_u32 v114, v252, 4, v114\n"
    "s_lshl_b32 s74, s52, 20\n"
    "s_lshl_b32 s75, s8, 6\n"
    "s_add_u32 s74, s74, s75\n"
    "s_add_u32 s78, s74, 2097152\n"
    "s_add_u32 s79, s74, 2359296\n"
    "s_add_u32 s80, s74, 2621440\n"
    "s_add_u32 s81, s74, 2883584\n"
    "v_lshl_add_u32 v254, v252, 8, v115\n"
    "v_lshlrev_b32 v254, 6, v254\n"
    "s_lshr_b32 s74, s74, 4\n"
    "s_add_u32 s82, s74, 29360128\n"
    "v_and_b32 v253, 1, v252\n"
    "v_lshl_add_u32 v115, v253, 3, v115\n"
    "v_lshrrev_b32 v252, 1, v252\n"
    "v_lshl_add_u32 v115, v252, 8, v115\n"
    "s_lshl_b32 s74, s52, 11\n"
    "s_add_u32 s74, s74, 139776\n"
    "v_add_u32 v115, s74, v115\n"
    "s_add_u32 s59, s48, 14336\n"
    "buffer_load_dwordx4 a[16:19], v85, s[28:31], s59 offen offset:0\n"
    "v_add_u32 v77, 1024, v115\n"
    "v_add_u32 v78, 512, v115\n"
    "v_add_u32 v79, 1536, v115\n"
    "v_and_b32 v113, 63, v112\n"
    "v_lshlrev_b32 v113, 4, v113\n"
    "v_add_u32 v113, 139776, v113\n"
    "s_lshl_b32 s74, s52, 11\n"
    "s_add_u32 s74, s74, 8192\n"
    "v_add_u32 v255, s74, v113\n"
    "v_and_b32 v253, 15, v112\n"
    "v_lshlrev_b32 v253, 5, v253\n"
    "v_bfe_u32 v248, v112, 4, 1\n"
    "v_lshl_add_u32 v253, v248, 4, v253\n"
    "v_bfe_u32 v248, v112, 5, 3\n"
    "v_lshl_add_u32 v253, v248, 10, v253\n"
    "s_lshl_b32 s70, s53, 13\n"
    "s_add_u32 s70, s70, 8388608\n"
    "s_cmp_eq_u32 s53, s8\n"
    "s_cselect_b32 s70, s70, 1073741824\n"
    "v_and_b32 v212, 15, v112\n"
    "v_bfe_u32 v213, v112, 4, 2\n"
    "v_cvt_f32_u32 v214, v212\n"
    "v_lshlrev_b32 v215, 2, v213\n"
    "buffer_load_dwordx4 a[20:23], v85, s[28:31], s59 offen offset:3584\n"
    "v_cvt_f32_u32 v215, v215\n"
    "v_and_b32 v217, 1, v212\n"
    "v_mul_u32_u24 v217, 0x8000, v217\n"
    "v_lshl_or_b32 v217, v217, 16, v217\n"
    "s_lshl_b32 s71, s52, 4\n"
    "v_add_u32 v218, s71, v212\n"
    "v_cvt_f32_u32 v218, v218\n"
    "v_mul_f32 v216, 0x3c800000, v218\n"
    "v_mul_f32 v236, v215, v216\n"
    "v_add_f32 v237, v236, v216\n"
    "v_add_f32 v238, v237, v216\n"
    "v_add_f32 v239, v238, v216\n"
    "v_sin_f32 v244, v236\n"
    "v_sin_f32 v245, v237\n"
    "v_sin_f32 v246, v238\n"
    "v_sin_f32 v247, v239\n"
    "v_cos_f32 v240, v236\n"
    "v_cos_f32 v241, v237\n"
    "s_add_u32 s59, s48, 21504\n"
    "buffer_load_dwordx4 a[24:27], v85, s[28:31], s59 offen offset:0\n"
    "v_cos_f32 v242, v238\n"
    "v_cos_f32 v243, v239\n"
    "v_cvt_pk_bf16_f32 v116, v240, v244\n"
    "v_cvt_pk_bf16_f32 v120, -v244, v240\n"
    "v_cvt_pk_bf16_f32 v117, v241, v245\n"
    "v_cvt_pk_bf16_f32 v121, -v245, v241\n"
    "v_cvt_pk_bf16_f32 v118, v242, v246\n"
    "v_cvt_pk_bf16_f32 v122, -v246, v242\n"
    "v_cvt_pk_bf16_f32 v119, v243, v247\n"
    "v_cvt_pk_bf16_f32 v123, -v247, v243\n"
    "v_mul_f32 v236, v215, v216\n"
    "v_fmamk_f32 v236, v216, 0x41800000, v236\n"
    "v_add_f32 v237, v236, v216\n"
    "v_add_f32 v238, v237, v216\n"
    "v_add_f32 v239, v238, v216\n"
    "v_sin_f32 v244, v236\n"
    "v_sin_f32 v245, v237\n"
    "v_sin_f32 v246, v238\n"
    "buffer_load_dwordx4 a[28:31], v85, s[28:31], s59 offen offset:3584\n"
    "v_sin_f32 v247, v239\n"
    "v_cos_f32 v240, v236\n"
    "v_cos_f32 v241, v237\n"
    "v_cos_f32 v242, v238\n"
    "v_cos_f32 v243, v239\n"
    "v_cvt_pk_bf16_f32 v124, v240, v244\n"
    "v_cvt_pk_bf16_f32 v128, -v244, v240\n"
    "v_cvt_pk_bf16_f32 v125, v241, v245\n"
    "v_cvt_pk_bf16_f32 v129, -v245, v241\n"
    "v_cvt_pk_bf16_f32 v126, v242, v246\n"
    "v_cvt_pk_bf16_f32 v130, -v246, v242\n"
    "v_cvt_pk_bf16_f32 v127, v243, v247\n"
    "v_cvt_pk_bf16_f32 v131, -v247, v243\n"
    "v_xor_b32 v132, v116, v217\n"
    "v_xor_b32 v133, v117, v217\n"
    "v_xor_b32 v134, v118, v217\n"
    "v_xor_b32 v135, v119, v217\n"
    "v_xor_b32 v136, v120, v217\n"
    "s_add_u32 s59, s48, 28672\n"
    "buffer_load_dwordx4 a[32:35], v85, s[28:31], s59 offen offset:0\n"
    "v_xor_b32 v137, v121, v217\n"
    "v_xor_b32 v138, v122, v217\n"
    "v_xor_b32 v139, v123, v217\n"
    "v_xor_b32 v140, v124, v217\n"
    "v_xor_b32 v141, v125, v217\n"
    "v_xor_b32 v142, v126, v217\n"
    "v_xor_b32 v143, v127, v217\n"
    "v_xor_b32 v144, v128, v217\n"
    "v_xor_b32 v145, v129, v217\n"
    "v_xor_b32 v146, v130, v217\n"
    "v_xor_b32 v147, v131, v217\n"
    "v_mul_f32 v216, 0x3d000000, v214\n"
    "v_mul_f32 v236, v215, v216\n"
    "v_add_f32 v237, v236, v216\n"
    "v_add_f32 v238, v237, v216\n"
    "v_add_f32 v239, v238, v216\n"
    "v_sin_f32 v244, v236\n"
    "v_sin_f32 v245, v237\n"
    "v_sin_f32 v246, v238\n"
    "v_sin_f32 v247, v239\n"
    "buffer_load_dwordx4 a[36:39], v85, s[28:31], s59 offen offset:3584\n"
    "v_cos_f32 v240, v236\n"
    "v_cos_f32 v241, v237\n"
    "v_cos_f32 v242, v238\n"
    "v_cos_f32 v243, v239\n"
    "v_cvt_pk_bf16_f32 v148, v240, v244\n"
    "v_cvt_pk_bf16_f32 v152, -v244, v240\n"
    "v_cvt_pk_bf16_f32 v149, v241, v245\n"
    "v_cvt_pk_bf16_f32 v153, -v245, v241\n"
    "v_cvt_pk_bf16_f32 v150, v242, v246\n"
    "v_cvt_pk_bf16_f32 v154, -v246, v242\n"
    "v_cvt_pk_bf16_f32 v151, v243, v247\n"
    "v_cvt_pk_bf16_f32 v155, -v247, v243\n"
    "v_xor_b32 v156, v148, v217\n"
    "v_xor_b32 v157, v149, v217\n"
    "v_xor_b32 v158, v150, v217\n"
    "v_xor_b32 v159, v151, v217\n"
    "v_xor_b32 v160, v152, v217\n"
    "v_xor_b32 v161, v153, v217\n"
    "v_xor_b32 v162, v154, v217\n"
    "s_add_u32 s59, s48, 35840\n"
    "buffer_load_dwordx4 a[40:43], v85, s[28:31], s59 offen offset:0\n"
    "v_xor_b32 v163, v155, v217\n"
    "v_mov_b32 v164, v148\n"
    "v_xor_b32 v165, s73, v149\n"
    "v_mov_b32 v166, v150\n"
    "v_xor_b32 v167, s73, v151\n"
    "v_mov_b32 v168, v152\n"
    "v_xor_b32 v169, s73, v153\n"
    "v_mov_b32 v170, v154\n"
    "v_xor_b32 v171, s73, v155\n"
    "v_mov_b32 v172, v156\n"
    "v_xor_b32 v173, s73, v157\n"
    "v_mov_b32 v174, v158\n"
    "v_xor_b32 v175, s73, v159\n"
    "v_mov_b32 v176, v160\n"
    "v_xor_b32 v177, s73, v161\n"
    "v_mov_b32 v178, v162\n"
    "v_xor_b32 v179, s73, v163\n"
    "v_mul_f32 v216, 0x3a000000, v218\n"
    "v_mul_f32 v236, v215, v216\n"
    "v_add_f32 v237, v236, v216\n"
    "v_add_f32 v238, v237, v216\n"
    "v_add_f32 v239, v238, v216\n"
    "v_sin_f32 v188, v236\n"
    "buffer_load_dwordx4 a[44:47], v85, s[28:31], s59 offen offset:3584\n"
    "v_sin_f32 v189, v237\n"
    "v_sin_f32 v190, v238\n"
    "v_sin_f32 v191, v239\n"
    "v_cos_f32 v180, v236\n"
    "v_cos_f32 v181, v237\n"
    "v_cos_f32 v182, v238\n"
    "v_cos_f32 v183, v239\n"
    "v_mul_f32 v236, v215, v216\n"
    "v_fmamk_f32 v236, v216, 0x41800000, v236\n"
    "v_add_f32 v237, v236, v216\n"
    "v_add_f32 v238, v237, v216\n"
    "v_add_f32 v239, v238, v216\n"
    "v_sin_f32 v192, v236\n"
    "v_sin_f32 v193, v237\n"
    "s_add_u32 s59, s48, 43008\n"
    "buffer_load_dwordx4 a[48:51], v85, s[28:31], s59 offen offset:0\n"
    "v_sin_f32 v194, v238\n"
    "v_sin_f32 v195, v239\n"
    "v_cos_f32 v184, v236\n"
    "v_cos_f32 v185, v237\n"
    "v_cos_f32 v186, v238\n"
    "v_cos_f32 v187, v239\n"
    "v_cvt_f32_u32 v218, s71\n"
    "v_add_f32 v218, v218, v215\n"
    "v_mul_f32 v216, 0x3a000000, v214\n"
    "v_mul_f32 v236, v218, v216\n"
    "v_add_f32 v237, v236, v216\n"
    "v_add_f32 v238, v237, v216\n"
    "v_add_f32 v239, v238, v216\n"
    "v_sin_f32 v204, v236\n"
    "v_sin_f32 v205, v237\n"
    "buffer_load_dwordx4 a[52:55], v85, s[28:31], s59 offen offset:3584\n"
    "v_sin_f32 v206, v238\n"
    "v_sin_f32 v207, v239\n"
    "v_cos_f32 v196, v236\n"
    "v_cos_f32 v197, v237\n"
    "v_cos_f32 v198, v238\n"
    "v_cos_f32 v199, v239\n"
    "v_add_f32 v214, 0x41800000, v214\n"
    "v_mul_f32 v216, 0x3a000000, v214\n"
    "v_mul_f32 v236, v218, v216\n"
    "v_add_f32 v237, v236, v216\n"
    "v_add_f32 v238, v237, v216\n"
    "v_add_f32 v239, v238, v216\n"
    "v_sin_f32 v208, v236\n"
    "v_sin_f32 v209, v237\n"
    "v_sin_f32 v210, v238\n"
    "s_add_u32 s59, s48, 50176\n"
    "buffer_load_dwordx4 a[56:59], v85, s[28:31], s59 offen offset:0\n"
    "v_sin_f32 v211, v239\n"
    "v_cos_f32 v200, v236\n"
    "v_cos_f32 v201, v237\n"
    "v_cos_f32 v202, v238\n"
    "v_cos_f32 v203, v239\n"
    "v_mov_b32 v80, -1\n"
    "v_mov_b32 v81, -1\n"
    "v_mov_b32 v82, -1\n"
    "v_mov_b32 v83, -1\n"
    "s_lshl_b32 s74, s53, 13\n"
    "v_lshl_add_u32 v84, v112, 4, s74\n"
    "s_waitcnt vmcnt(0)\n"
    "buffer_load_dwordx4 v[220:223], v114, s[12:15], s78 offen sc1\n"
    "buffer_load_dwordx4 v[224:227], v114, s[12:15], s79 offen sc1\n"
    "buffer_load_dwordx4 v[228:231], v114, s[12:15], s80 offen sc1\n"
    "buffer_load_dwordx4 v[232:235], v114, s[12:15], s81 offen sc1\n"
    "buffer_load_dword v32, v254, s[12:15], s82 offen sc1\n"
    "v_pk_mul_f32 v[196:197], v[196:197], s[72:73] op_sel_hi:[1,0]\n"
    "v_pk_mul_f32 v[198:199], v[198:199], s[72:73] op_sel_hi:[1,0]\n"
    "v_pk_mul_f32 v[200:201], v[200:201], s[72:73] op_sel_hi:[1,0]\n"
    "v_pk_mul_f32 v[202:203], v[202:203], s[72:73] op_sel_hi:[1,0]\n"
    "v_pk_mul_f32 v[204:205], v[204:205], s[72:73] op_sel_hi:[1,0]\n"
    "v_pk_mul_f32 v[206:207], v[206:207], s[72:73] op_sel_hi:[1,0]\n"
    "v_pk_mul_f32 v[208:209], v[208:209], s[72:73] op_sel_hi:[1,0]\n"
    "v_pk_mul_f32 v[210:211], v[210:211], s[72:73] op_sel_hi:[1,0]\n"
    "ds_write_b128 v90, a[0:3] offset:28672\n"
    "ds_write_b128 v91, a[4:7] offset:29696\n"
    "ds_write_b128 v90, a[8:11] offset:30720\n"
    "ds_write_b128 v91, a[12:15] offset:31744\n"
    "ds_read_b64_tr_b16 a[0:1], v68 offset:28672\n"
    "ds_read_b64_tr_b16 a[2:3], v68 offset:29184\n"
    "ds_read_b64_tr_b16 a[4:5], v69 offset:28672\n"
    "buffer_load_dwordx4 a[60:63], v85, s[28:31], s59 offen offset:3584\n"
    "ds_read_b64_tr_b16 a[6:7], v69 offset:29184\n"
    "ds_read_b64_tr_b16 a[8:9], v70 offset:28672\n"
    "ds_read_b64_tr_b16 a[10:11], v70 offset:29184\n"
    "ds_read_b64_tr_b16 a[12:13], v71 offset:28672\n"
    "ds_read_b64_tr_b16 a[14:15], v71 offset:29184\n"
    "ds_write_b128 v90, a[16:19] offset:28672\n"
    "ds_write_b128 v91, a[20:23] offset:29696\n"
    "s_add_u32 s59, s48, 57344\n"
    "buffer_load_dwordx4 a[64:67], v85, s[28:31], s59 offen offset:0\n"
    "ds_write_b128 v90, a[24:27] offset:30720\n"
    "ds_write_b128 v91, a[28:31] offset:31744\n"
    "ds_read_b64_tr_b16 a[16:17], v68 offset:28672\n"
    "ds_read_b64_tr_b16 a[18:19], v68 offset:29184\n"
    "ds_read_b64_tr_b16 a[20:21], v69 offset:28672\n"
    "ds_read_b64_tr_b16 a[22:23], v69 offset:29184\n"
    "ds_read_b64_tr_b16 a[24:25], v70 offset:28672\n"
    "buffer_load_dwordx4 a[68:71], v85, s[28:31], s59 offen offset:3584\n"
    "ds_read_b64_tr_b16 a[26:27], v70 offset:29184\n"
    "ds_read_b64_tr_b16 a[28:29], v71 offset:28672\n"
    "ds_read_b64_tr_b16 a[30:31], v71 offset:29184\n"
    "ds_write_b128 v90, a[32:35] offset:28672\n"
    "ds_write_b128 v91, a[36:39] offset:29696\n"
    "ds_write_b128 v90, a[40:43] offset:30720\n"
    "ds_write_b128 v91, a[44:47] offset:31744\n"
    "s_add_u32 s59, s48, 64512\n"
    "buffer_load_dwordx4 a[72:75], v85, s[28:31], s59 offen offset:0\n"
    "ds_read_b64_tr_b16 a[32:33], v68 offset:28672\n"
    "ds_read_b64_tr_b16 a[34:35], v68 offset:29184\n"
    "ds_read_b64_tr_b16 a[36:37], v69 offset:28672\n"
    "ds_read_b64_tr_b16 a[38:39], v69 offset:29184\n"
    "ds_read_b64_tr_b16 a[40:41], v70 offset:28672\n"
    "ds_read_b64_tr_b16 a[42:43], v70 offset:29184\n"
    "ds_read_b64_tr_b16 a[44:45], v71 offset:28672\n"
    "ds_read_b64_tr_b16 a[46:47], v71 offset:29184\n"
    "s_waitcnt vmcnt(8)\n"
    "v_add3_u32 v236, v220, v221, v222\n"
    "s_waitcnt vmcnt(7)\n"
    "v_add3_u32 v237, v224, v225, v226\n"
    "s_waitcnt vmcnt(6)\n"
    "v_add3_u32 v238, v228, v229, v230\n"
    "s_waitcnt vmcnt(5)\n"
    "v_add3_u32 v239, v232, v233, v234\n"
    "v_add_u32 v236, v236, v223\n"
    "v_add_u32 v237, v237, v227\n"
    "v_add_u32 v238, v238, v231\n"
    "v_add_u32 v239, v239, v235\n"
    "v_add_u32_dpp v236, v236, v236 quad_perm:[1,0,3,2] row_mask:0xf bank_mask:0xf\n"
    "v_add_u32_dpp v237, v237, v237 quad_perm:[1,0,3,2] row_mask:0xf bank_mask:0xf\n"
    "v_add_u32_dpp v238, v238, v238 quad_perm:[1,0,3,2] row_mask:0xf bank_mask:0xf\n"
    "v_add_u32_dpp v239, v239, v239 quad_perm:[1,0,3,2] row_mask:0xf bank_mask:0xf\n"
    "v_add_u32_dpp v236, v236, v236 quad_perm:[2,3,0,1] row_mask:0xf bank_mask:0xf\n"
    "v_add_u32_dpp v237, v237, v237 quad_perm:[2,3,0,1] row_mask:0xf bank_mask:0xf\n"
    "v_add_u32_dpp v238, v238, v238 quad_perm:[2,3,0,1] row_mask:0xf bank_mask:0xf\n"
    "v_add_u32_dpp v239, v239, v239 quad_perm:[2,3,0,1] row_mask:0xf bank_mask:0xf\n"
    "v_cndmask_b32 v236, v236, v237, s[16:17]\n"
    "v_cndmask_b32 v236, v236, v238, s[18:19]\n"
    "v_cndmask_b32 v236, v236, v239, s[26:27]\n"
    "s_waitcnt vmcnt(4)\n"
    "v_add_u32 v236, v236, v32\n"
    "v_cmp_ne_u32 vcc, s58, v236\n"
    "s_cbranch_vccnz .Lretry_4\n"
    ".Lx0_5:\n"
    "ds_write2_b32 v115, v220, v222 offset0:0 offset1:1\n"
    "ds_write2_b32 v77, v224, v226 offset0:0 offset1:1\n"
    "ds_write2_b32 v78, v228, v230 offset0:0 offset1:1\n"
    "ds_write2_b32 v79, v232, v234 offset0:0 offset1:1\n"
    "v_add_u32 v32, 8192, v115\n"
    "v_add_u32 v33, 8704, v115\n"
    "v_add_u32 v34, 9216, v115\n"
    "v_add_u32 v35, 9728, v115\n"
    "ds_write2_b32 v32, v221, v223 offset0:0 offset1:1\n"
    "ds_write2_b32 v33, v225, v227 offset0:0 offset1:1\n"
    "ds_write2_b32 v34, v229, v231 offset0:0 offset1:1\n"
    "ds_write2_b32 v35, v233, v235 offset0:0 offset1:1\n"
    "ds_read_b128 v[104:107], v255 offset:0\n"
    "ds_read_b128 v[108:111], v255 offset:1024\n"
    "s_mov_b32 s25, 10485760\n"
    "buffer_store_dwordx4 v[80:83], v84, s[12:15], s25 offen sc1\n"
    "s_mov_b32 s25, 10489856\n"
    "buffer_store_dwordx4 v[80:83], v84, s[12:15], s25 offen sc1\n"
    ".Lmark_p2in_6:\n"
    "s_waitcnt lgkmcnt(0)\n"
    "s_barrier\n"
    "ds_read_b128 v[212:215], v113 offset:0\n"
    "ds_read_b128 v[216:219], v113 offset:2048\n"
    "ds_read_b128 v[220:223], v113 offset:4096\n"
    "ds_read_b128 v[224:227], v113 offset:6144\n"
    "ds_read_b128 v[228:231], v113 offset:1024\n"
    "ds_read_b128 v[232:235], v113 offset:3072\n"
    "s_waitcnt lgkmcnt(5)\n"
    "v_mfma_f32_16x16x32_bf16 v[236:239], v[212:215], v[116:119], 0\n"
    "v_mfma_f32_16x16x32_bf16 v[240:243], v[212:215], v[120:123], 0\n"
    "ds_read_b128 v[212:215], v113 offset:5120\n"
    "s_waitcnt lgkmcnt(5)\n"
    "v_mfma_f32_16x16x32_bf16 v[236:239], v[216:219], v[124:127], v[236:239]\n"
    "v_mfma_f32_16x16x32_bf16 v[240:243], v[216:219], v[128:131], v[240:243]\n"
    "buffer_load_dwordx4 a[76:79], v85, s[28:31], s59 offen offset:3584\n"
    "ds_read_b128 v[216:219], v113 offset:7168\n"
    "s_waitcnt lgkmcnt(5)\n"
    "v_mfma_f32_16x16x32_bf16 v[236:239], v[220:223], v[132:135], v[236:239]\n"
    "v_mfma_f32_16x16x32_bf16 v[240:243], v[220:223], v[136:139], v[240:243]\n"
    "s_waitcnt lgkmcnt(4)\n"
    "v_mfma_f32_16x16x32_bf16 v[236:239], v[224:227], v[140:143], v[236:239]\n"
    "v_mfma_f32_16x16x32_bf16 v[240:243], v[224:227], v[144:147], v[240:243]\n"
    "s_waitcnt lgkmcnt(3)\n"
    "v_mfma_f32_16x16x32_bf16 v[244:247], v[228:231], v[116:119], 0\n"
    "v_mfma_f32_16x16x32_bf16 v[248:251], v[228:231], v[120:123], 0\n"
    "s_add_u32 s59, s48, 71680\n"
    "buffer_load_dwordx4 a[80:83], v85, s[28:31], s59 offen offset:0\n"
    "s_waitcnt lgkmcnt(2)\n"
    "v_mfma_f32_16x16x32_bf16 v[244:247], v[232:235], v[124:127], v[244:247]\n"
    "v_mfma_f32_16x16x32_bf16 v[248:251], v[232:235], v[128:131], v[248:251]\n"
    "s_waitcnt lgkmcnt(1)\n"
    "v_mfma_f32_16x16x32_bf16 v[244:247], v[212:215], v[132:135], v[244:247]\n"
    "v_mfma_f32_16x16x32_bf16 v[248:251], v[212:215], v[136:139], v[248:251]\n"
    "s_waitcnt lgkmcnt(0)\n"
    "v_mfma_f32_16x16x32_bf16 v[244:247], v[216:219], v[140:143], v[244:247]\n"
    "v_mfma_f32_16x16x32_bf16 v[248:251], v[216:219], v[144:147], v[248:251]\n"
    "buffer_load_dwordx4 a[84:87], v85, s[28:31], s59 offen offset:3584\n"
    "v_pk_mul_f32 v[220:221], v[236:237], v[180:181]\n"
    "v_pk_mul_f32 v[224:225], v[240:241], v[180:181]\n"
    "v_pk_mul_f32 v[222:223], v[238:239], v[182:183]\n"
    "v_pk_mul_f32 v[226:227], v[242:243], v[182:183]\n"
    "v_pk_fma_f32 v[220:221], v[240:241], v[188:189], v[220:221]\n"
    "v_pk_fma_f32 v[224:225], v[236:237], v[188:189], v[224:225] neg_lo:[1,0,0] neg_hi:[1,0,0]\n"
    "v_pk_fma_f32 v[222:223], v[242:243], v[190:191], v[222:223]\n"
    "v_pk_fma_f32 v[226:227], v[238:239], v[190:191], v[226:227] neg_lo:[1,0,0] neg_hi:[1,0,0]\n"
    "v_cvt_pk_bf16_f32 v220, v220, v224\n"
    "v_cvt_pk_bf16_f32 v221, v221, v225\n"
    "v_cvt_pk_bf16_f32 v222, v222, v226\n"
    "v_cvt_pk_bf16_f32 v223, v223, v227\n"
    "v_pk_mul_f32 v[228:229], v[244:245], v[184:185]\n"
    "v_pk_mul_f32 v[232:233], v[248:249], v[184:185]\n"
    "v_pk_mul_f32 v[230:231], v[246:247], v[186:187]\n"
    "v_pk_mul_f32 v[234:235], v[250:251], v[186:187]\n"
    "v_pk_fma_f32 v[228:229], v[248:249], v[192:193], v[228:229]\n"
    "v_pk_fma_f32 v[232:233], v[244:245], v[192:193], v[232:233] neg_lo:[1,0,0] neg_hi:[1,0,0]\n"
    "v_pk_fma_f32 v[230:231], v[250:251], v[194:195], v[230:231]\n"
    "v_pk_fma_f32 v[234:235], v[246:247], v[194:195], v[234:235] neg_lo:[1,0,0] neg_hi:[1,0,0]\n"
    "v_cvt_pk_bf16_f32 v228, v228, v232\n"
    "v_cvt_pk_bf16_f32 v229, v229, v233\n"
    "v_cvt_pk_bf16_f32 v230, v230, v234\n"
    "s_add_u32 s59, s48, 78848\n"
    "buffer_load_dwordx4 a[88:91], v85, s[28:31], s59 offen offset:0\n"
    "v_cvt_pk_bf16_f32 v231, v231, v235\n"
    "v_mfma_f32_16x16x32_bf16 v[236:239], v[148:151], v[220:223], 0\n"
    "v_mfma_f32_16x16x32_bf16 v[240:243], v[152:155], v[220:223], 0\n"
    "v_mfma_f32_16x16x32_bf16 v[236:239], v[156:159], v[228:231], v[236:239]\n"
    "v_mfma_f32_16x16x32_bf16 v[240:243], v[160:163], v[228:231], v[240:243]\n"
    "v_mfma_f32_16x16x32_bf16 v[244:247], v[164:167], v[220:223], 0\n"
    "v_mfma_f32_16x16x32_bf16 v[248:251], v[168:171], v[220:223], 0\n"
    "buffer_load_dwordx4 a[92:95], v85, s[28:31], s59 offen offset:3584\n"
    "v_mfma_f32_16x16x32_bf16 v[244:247], v[172:175], v[228:231], v[244:247]\n"
    "v_mfma_f32_16x16x32_bf16 v[248:251], v[176:179], v[228:231], v[248:251]\n"
    "v_lshlrev_b32 v180, 16, v104\n"
    "v_and_b32 v188, 0xffff0000, v104\n"
    "v_lshlrev_b32 v181, 16, v105\n"
    "v_and_b32 v189, 0xffff0000, v105\n"
    "v_lshlrev_b32 v182, 16, v106\n"
    "v_and_b32 v190, 0xffff0000, v106\n"
    "v_lshlrev_b32 v183, 16, v107\n"
    "v_and_b32 v191, 0xffff0000, v107\n"
    "v_lshlrev_b32 v184, 16, v108\n"
    "v_and_b32 v192, 0xffff0000, v108\n"
    "v_lshlrev_b32 v185, 16, v109\n"
    "v_and_b32 v193, 0xffff0000, v109\n"
    "v_lshlrev_b32 v186, 16, v110\n"
    "v_and_b32 v194, 0xffff0000, v110\n"
    "v_lshlrev_b32 v187, 16, v111\n"
    "s_add_u32 s59, s48, 86016\n"
    "buffer_load_dwordx4 a[96:99], v85, s[28:31], s59 offen offset:0\n"
    "v_and_b32 v195, 0xffff0000, v111\n"
    "v_pk_mul_f32 v[212:213], v[236:237], v[180:181]\n"
    "v_pk_mul_f32 v[216:217], v[240:241], v[180:181]\n"
    "v_pk_mul_f32 v[214:215], v[238:239], v[182:183]\n"
    "v_pk_mul_f32 v[218:219], v[242:243], v[182:183]\n"
    "v_pk_fma_f32 v[212:213], v[240:241], v[188:189], v[212:213] neg_lo:[1,0,0] neg_hi:[1,0,0]\n"
    "v_pk_fma_f32 v[216:217], v[236:237], v[188:189], v[216:217]\n"
    "v_pk_fma_f32 v[214:215], v[242:243], v[190:191], v[214:215] neg_lo:[1,0,0] neg_hi:[1,0,0]\n"
    "v_pk_fma_f32 v[218:219], v[238:239], v[190:191], v[218:219]\n"
    "v_cvt_pk_bf16_f32 v212, v212, -v216\n"
    "v_cvt_pk_bf16_f32 v213, v213, -v217\n"
    "v_cvt_pk_bf16_f32 v214, v214, -v218\n"
    "v_cvt_pk_bf16_f32 v215, v215, -v219\n"
    "v_pk_mul_f32 v[220:221], v[244:245], v[184:185]\n"
    "v_pk_mul_f32 v[224:225], v[248:249], v[184:185]\n"
    "v_pk_mul_f32 v[222:223], v[246:247], v[186:187]\n"
    "v_pk_mul_f32 v[226:227], v[250:251], v[186:187]\n"
    "v_pk_fma_f32 v[220:221], v[248:249], v[192:193], v[220:221] neg_lo:[1,0,0] neg_hi:[1,0,0]\n"
    "v_pk_fma_f32 v[224:225], v[244:245], v[192:193], v[224:225]\n"
    "v_pk_fma_f32 v[222:223], v[250:251], v[194:195], v[222:223] neg_lo:[1,0,0] neg_hi:[1,0,0]\n"
    "v_pk_fma_f32 v[226:227], v[246:247], v[194:195], v[226:227]\n"
    "v_cvt_pk_bf16_f32 v220, v220, -v224\n"
    "v_cvt_pk_bf16_f32 v221, v221, -v225\n"
    "buffer_load_dwordx4 a[100:103], v85, s[28:31], s59 offen offset:3584\n"
    "v_cvt_pk_bf16_f32 v222, v222, -v226\n"
    "v_cvt_pk_bf16_f32 v223, v223, -v227\n"
    "v_mfma_f32_16x16x32_bf16 v[236:239], v[212:215], v[148:151], 0\n"
    "v_mfma_f32_16x16x32_bf16 v[240:243], v[212:215], v[152:155], 0\n"
    "v_mfma_f32_16x16x32_bf16 v[236:239], v[220:223], v[156:159], v[236:239]\n"
    "v_mfma_f32_16x16x32_bf16 v[240:243], v[220:223], v[160:163], v[240:243]\n"
    "v_mfma_f32_16x16x32_bf16 v[244:247], v[212:215], v[164:167], 0\n"
    "v_mfma_f32_16x16x32_bf16 v[248:251], v[212:215], v[168:171], 0\n"
    "s_add_u32 s59, s48, 93184\n"
    "buffer_load_dwordx4 a[104:107], v85, s[28:31], s59 offen offset:0\n"
    "v_mfma_f32_16x16x32_bf16 v[244:247], v[220:223], v[172:175], v[244:247]\n"
    "v_mfma_f32_16x16x32_bf16 v[248:251], v[220:223], v[176:179], v[248:251]\n"
    "v_pk_mul_f32 v[212:213], v[236:237], v[196:197]\n"
    "v_pk_mul_f32 v[216:217], v[240:241], v[196:197]\n"
    "v_pk_mul_f32 v[214:215], v[238:239], v[198:199]\n"
    "v_pk_mul_f32 v[218:219], v[242:243], v[198:199]\n"
    "v_pk_fma_f32 v[212:213], v[240:241], v[204:205], v[212:213]\n"
    "v_pk_fma_f32 v[216:217], v[236:237], v[204:205], v[216:217] neg_lo:[1,0,0] neg_hi:[1,0,0]\n"
    "v_pk_fma_f32 v[214:215], v[242:243], v[206:207], v[214:215]\n"
    "v_pk_fma_f32 v[218:219], v[238:239], v[206:207], v[218:219] neg_lo:[1,0,0] neg_hi:[1,0,0]\n"
    "v_cvt_pk_bf16_f32 v212, v212, v216\n"
    "v_cvt_pk_bf16_f32 v213, v213, v217\n"
    "v_cvt_pk_bf16_f32 v214, v214, v218\n"
    "v_cvt_pk_bf16_f32 v215, v215, v219\n"
    "ds_write_b128 v255, v[212:215] offset:0\n"
    "v_pk_mul_f32 v[220:221], v[244:245], v[200:201]\n"
    "v_pk_mul_f32 v[224:225], v[248:249], v[200:201]\n"
    "buffer_load_dwordx4 a[108:111], v85, s[28:31], s59 offen offset:3584\n"
    "v_pk_mul_f32 v[222:223], v[246:247], v[202:203]\n"
    "v_pk_mul_f32 v[226:227], v[250:251], v[202:203]\n"
    "v_pk_fma_f32 v[220:221], v[248:249], v[208:209], v[220:221]\n"
    "v_pk_fma_f32 v[224:225], v[244:245], v[208:209], v[224:225] neg_lo:[1,0,0] neg_hi:[1,0,0]\n"
    "v_pk_fma_f32 v[222:223], v[250:251], v[210:211], v[222:223]\n"
    "v_pk_fma_f32 v[226:227], v[246:247], v[210:211], v[226:227] neg_lo:[1,0,0] neg_hi:[1,0,0]\n"
    "v_cvt_pk_bf16_f32 v220, v220, v224\n"
    "v_cvt_pk_bf16_f32 v221, v221, v225\n"
    "v_cvt_pk_bf16_f32 v222, v222, v226\n"
    "v_cvt_pk_bf16_f32 v223, v223, v227\n"
    "ds_write_b128 v255, v[220:223] offset:1024\n"
    "s_waitcnt vmcnt(9)\n"
    "s_waitcnt lgkmcnt(0)\n"
    "s_barrier\n"
    "ds_read_b128 v[180:183], v113 offset:8192\n"
    "ds_read_b128 v[184:187], v113 offset:10240\n"
    "ds_read_b128 v[188:191], v113 offset:12288\n"
    "ds_read_b128 v[192:195], v113 offset:14336\n"
    "ds_read_b128 v[196:199], v113 offset:9216\n"
    "ds_read_b128 v[200:203], v113 offset:11264\n"
    "ds_read_b128 v[204:207], v113 offset:13312\n"
    "ds_read_b128 v[208:211], v113 offset:15360\n"
    "s_waitcnt lgkmcnt(7)\n"
    "v_mfma_f32_16x16x32_bf16 v[236:239], v[116:119], v[180:183], 0\n"
    "v_mfma_f32_16x16x32_bf16 v[240:243], v[120:123], v[180:183], 0\n"
    "s_waitcnt lgkmcnt(6)\n"
    "v_mfma_f32_16x16x32_bf16 v[236:239], v[124:127], v[184:187], v[236:239]\n"
    "v_mfma_f32_16x16x32_bf16 v[240:243], v[128:131], v[184:187], v[240:243]\n"
    "s_waitcnt lgkmcnt(5)\n"
    "v_mfma_f32_16x16x32_bf16 v[236:239], v[132:135], v[188:191], v[236:239]\n"
    "v_mfma_f32_16x16x32_bf16 v[240:243], v[136:139], v[188:191], v[240:243]\n"
    "s_waitcnt lgkmcnt(4)\n"
    "v_mfma_f32_16x16x32_bf16 v[236:239], v[140:143], v[192:195], v[236:239]\n"
    "v_mfma_f32_16x16x32_bf16 v[240:243], v[144:147], v[192:195], v[240:243]\n"
    "s_waitcnt lgkmcnt(3)\n"
    "v_mfma_f32_16x16x32_bf16 v[244:247], v[116:119], v[196:199], 0\n"
    "v_mfma_f32_16x16x32_bf16 v[248:251], v[120:123], v[196:199], 0\n"
    "s_waitcnt lgkmcnt(2)\n"
    "v_mfma_f32_16x16x32_bf16 v[244:247], v[124:127], v[200:203], v[244:247]\n"
    "v_mfma_f32_16x16x32_bf16 v[248:251], v[128:131], v[200:203], v[248:251]\n"
    "s_waitcnt lgkmcnt(1)\n"
    "v_mfma_f32_16x16x32_bf16 v[244:247], v[132:135], v[204:207], v[244:247]\n"
    "v_mfma_f32_16x16x32_bf16 v[248:251], v[136:139], v[204:207], v[248:251]\n"
    "s_waitcnt lgkmcnt(0)\n"
    "v_mfma_f32_16x16x32_bf16 v[244:247], v[140:143], v[208:211], v[244:247]\n"
    "v_mfma_f32_16x16x32_bf16 v[248:251], v[144:147], v[208:211], v[248:251]\n"
    "v_cvt_pk_bf16_f32 v212, v236, -v240\n"
    "v_cvt_pk_bf16_f32 v213, v237, -v241\n"
    "v_cvt_pk_bf16_f32 v214, v238, -v242\n"
    "v_cvt_pk_bf16_f32 v215, v239, -v243\n"
    "buffer_store_dwordx4 v[212:215], v253, s[12:15], s70 offen offset:0 sc1\n"
    "s_nop 2\n"
    "v_cvt_pk_bf16_f32 v216, v244, -v248\n"
    "v_cvt_pk_bf16_f32 v217, v245, -v249\n"
    "v_cvt_pk_bf16_f32 v218, v246, -v250\n"
    "v_cvt_pk_bf16_f32 v219, v247, -v251\n"
    "buffer_store_dwordx4 v[216:219], v253, s[12:15], s70 offen offset:512 sc1\n"
    ".Lmark_p2out_7:\n"
    "s_add_u32 s59, s48, 100352\n"
    "buffer_load_dwordx4 a[112:115], v85, s[28:31], s59 offen offset:0\n"
    "buffer_load_dwordx4 a[116:119], v85, s[28:31], s59 offen offset:3584\n"
    "s_add_u32 s59, s48, 107520\n"
    "buffer_load_dwordx4 a[120:123], v85, s[28:31], s59 offen offset:0\n"
    "buffer_load_dwordx4 a[124:127], v85, s[28:31], s59 offen offset:3584\n"
    "v_and_b32 v113, 15, v112\n"
    "v_bfe_u32 v114, v112, 4, 2\n"
    "v_bfe_u32 v115, v112, 1, 5\n"
    "v_lshlrev_b32 v116, 5, v115\n"
    "v_lshlrev_b32 v115, 13, v115\n"
    "v_and_b32 v248, 1, v112\n"
    "v_lshl_add_u32 v115, v248, 4, v115\n"
    "v_cmp_gt_u32 vcc, 194, v112\n"
    "v_mov_b32 v123, 1073741824\n"
    "v_bfe_u32 v249, v112, 4, 1\n"
    "v_xor_b32 v248, v248, v249\n"
    "v_cndmask_b32 v123, v123, v115, vcc\n"
    "v_lshl_add_u32 v116, v248, 4, v116\n"
    "s_lshl_b32 s74, s52, 11\n"
    "s_add_u32 s74, s74, 139776\n"
    "v_add_u32 v116, s74, v116\n"
    "s_lshl_b32 s74, s52, 19\n"
    "s_lshl_b32 s75, s53, 5\n"
    "s_add_u32 s74, s74, s75\n"
    "s_add_u32 s70, s74, 8388608\n"
    "s_add_u32 s71, s70, 262144\n"
    "v_cvt_f32_u32 v244, v113\n"
    "v_lshlrev_b32 v245, 2, v114\n"
    "s_add_u32 s59, s48, 917504\n"
    "buffer_load_dwordx4 a[128:131], v85, s[28:31], s59 offen offset:0\n"
    "v_cvt_f32_u32 v245, v245\n"
    "v_and_b32 v247, 1, v113\n"
    "v_mul_u32_u24 v247, 0x8000, v247\n"
    "v_lshl_or_b32 v247, v247, 16, v247\n"
    "v_mul_f32 v246, 0x3d924925, v244\n"
    "v_mul_f32 v196, v245, v246\n"
    "v_mul_f32 v196, 0.5, v196\n"
    "v_add_f32 v197, v196, v246\n"
    "v_sin_f32 v204, v196\n"
    "v_cos_f32 v200, v196\n"
    "v_sin_f32 v205, v197\n"
    "v_cos_f32 v201, v197\n"
    "v_cmp_gt_u32 vcc, 14, v113\n"
    "v_mov_b32 v124, 0\n"
    "v_mov_b32 v125, 0\n"
    "v_mov_b32 v126, 0\n"
    "v_mov_b32 v127, 0\n"
    "v_mov_b32 v128, 0\n"
    "v_mov_b32 v129, 0\n"
    "buffer_load_dwordx4 a[132:135], v85, s[28:31], s59 offen offset:3584\n"
    "v_mov_b32 v130, 0\n"
    "v_mov_b32 v131, 0\n"
    "v_mov_b32 v132, 0\n"
    "v_mov_b32 v133, 0\n"
    "v_mov_b32 v134, 0\n"
    "v_mov_b32 v135, 0\n"
    "v_mov_b32 v136, 0\n"
    "v_mov_b32 v137, 0\n"
    "v_mov_b32 v138, 0\n"
    "v_mov_b32 v139, 0\n"
    "v_cvt_pk_bf16_f32 v198, v200, -v204\n"
    "v_cvt_pk_bf16_f32 v199, v204, v200\n"
    "v_cndmask_b32 v124, 0, v198, vcc\n"
    "v_cndmask_b32 v128, 0, v199, vcc\n"
    "v_cndmask_b32 v133, 0, v198, vcc\n"
    "v_cndmask_b32 v137, 0, v199, vcc\n"
    "v_cvt_pk_bf16_f32 v198, v201, -v205\n"
    "v_cvt_pk_bf16_f32 v199, v205, v201\n"
    "v_cndmask_b32 v126, 0, v198, vcc\n"
    "v_cndmask_b32 v130, 0, v199, vcc\n"
    "v_cndmask_b32 v135, 0, v198, vcc\n"
    "v_cndmask_b32 v139, 0, v199, vcc\n"
    "v_mul_f32 v246, 0x3b124925, v244\n"
    "s_add_u32 s59, s48, 924672\n"
    "buffer_load_dwordx4 a[136:139], v85, s[28:31], s59 offen offset:0\n"
    "v_mul_f32 v196, v245, v246\n"
    "v_add_f32 v197, v196, v246\n"
    "v_add_f32 v198, v197, v246\n"
    "v_add_f32 v199, v198, v246\n"
    "v_sin_f32 v148, v196\n"
    "v_sin_f32 v149, v197\n"
    "v_sin_f32 v150, v198\n"
    "v_sin_f32 v151, v199\n"
    "v_cos_f32 v140, v196\n"
    "v_cos_f32 v141, v197\n"
    "v_cos_f32 v142, v198\n"
    "v_cos_f32 v143, v199\n"
    "v_mul_f32 v196, v245, v246\n"
    "v_fmamk_f32 v196, v246, 0x41800000, v196\n"
    "v_add_f32 v197, v196, v246\n"
    "buffer_load_dwordx4 a[140:143], v85, s[28:31], s59 offen offset:3584\n"
    "v_add_f32 v198, v197, v246\n"
    "v_add_f32 v199, v198, v246\n"
    "v_sin_f32 v152, v196\n"
    "v_sin_f32 v153, v197\n"
    "v_sin_f32 v154, v198\n"
    "v_sin_f32 v155, v199\n"
    "v_cos_f32 v144, v196\n"
    "v_cos_f32 v145, v197\n"
    "v_cos_f32 v146, v198\n"
    "v_cos_f32 v147, v199\n"
    "v_mul_f32 v246, 0x3d000000, v244\n"
    "v_mul_f32 v196, v245, v246\n"
    "v_add_f32 v197, v196, v246\n"
    "v_add_f32 v198, v197, v246\n"
    "v_add_f32 v199, v198, v246\n"
    "s_add_u32 s59, s48, 931840\n"
    "buffer_load_dwordx4 a[144:147], v85, s[28:31], s59 offen offset:0\n"
    "v_sin_f32 v204, v196\n"
    "v_sin_f32 v205, v197\n"
    "v_sin_f32 v206, v198\n"
    "v_sin_f32 v207, v199\n"
    "v_cos_f32 v200, v196\n"
    "v_cos_f32 v201, v197\n"
    "v_cos_f32 v202, v198\n"
    "v_cos_f32 v203, v199\n"
    "v_cvt_pk_bf16_f32 v156, v200, -v204\n"
    "v_cvt_pk_bf16_f32 v157, v201, -v205\n"
    "v_cvt_pk_bf16_f32 v158, v202, -v206\n"
    "v_cvt_pk_bf16_f32 v159, v203, -v207\n"
    "v_xor_b32 v160, v156, v247\n"
    "v_xor_b32 v161, v157, v247\n"
    "v_xor_b32 v162, v158, v247\n"
    "buffer_load_dwordx4 a[148:151], v85, s[28:31], s59 offen offset:3584\n"
    "v_xor_b32 v163, v159, v247\n"
    "v_mov_b32 v164, v156\n"
    "v_xor_b32 v165, s73, v157\n"
    "v_mov_b32 v166, v158\n"
    "v_xor_b32 v167, s73, v159\n"
    "v_mov_b32 v168, v160\n"
    "v_xor_b32 v169, s73, v161\n"
    "v_mov_b32 v170, v162\n"
    "v_xor_b32 v171, s73, v163\n"
    "v_lshlrev_b32 v172, 16, v96\n"
    "v_and_b32 v173, 0xffff0000, v96\n"
    "v_lshlrev_b32 v174, 16, v97\n"
    "v_and_b32 v175, 0xffff0000, v97\n"
    "v_lshlrev_b32 v176, 16, v98\n"
    "v_and_b32 v177, 0xffff0000, v98\n"
    "v_lshlrev_b32 v178, 16, v99\n"
    "v_and_b32 v179, 0xffff0000, v99\n"
    "v_lshlrev_b32 v180, 16, v100\n"
    "v_and_b32 v181, 0xffff0000, v100\n"
    "v_lshlrev_b32 v182, 16, v101\n"
    "v_and_b32 v183, 0xffff0000, v101\n"
    "v_lshlrev_b32 v184, 16, v102\n"
    "v_and_b32 v185, 0xffff0000, v102\n"
    "s_add_u32 s59, s48, 939008\n"
    "buffer_load_dwordx4 a[152:155], v85, s[28:31], s59 offen offset:0\n"
    "v_lshlrev_b32 v186, 16, v103\n"
    "v_and_b32 v187, 0xffff0000, v103\n"
    "v_lshl_add_u32 v117, v114, 6, v113\n"
    "v_lshlrev_b32 v117, 5, v117\n"
    "v_lshrrev_b32 v248, 3, v113\n"
    "v_lshlrev_b32 v248, 1, v248\n"
    "v_xor_b32 v248, s52, v248\n"
    "v_lshl_add_u32 v117, v248, 3, v117\n"
    "v_add_u32 v117, 139776, v117\n"
    "v_lshrrev_b32 v248, 4, v112\n"
    "v_mov_b32 v80, -1\n"
    "v_mov_b32 v81, -1\n"
    "v_mov_b32 v82, -1\n"
    "v_mov_b32 v83, -1\n"
    "s_lshl_b32 s74, s53, 13\n"
    "v_lshl_add_u32 v84, v112, 4, s74\n"
    "s_or_b32 s74, s53, s52\n"
    "s_cmp_eq_u32 s74, 0\n"
    "s_cselect_b32 s74, 0, 1073741824\n"
    "v_mov_b32 v94, s58\n"
    "v_mov_b32 v95, s74\n"
    "s_waitcnt vmcnt(0)\n"
    "buffer_load_dwordx4 v[188:191], v115, s[12:15], s70 offen sc1\n"
    "buffer_load_dwordx4 v[192:195], v123, s[12:15], s71 offen sc1\n"
    "v_and_b32 v249, 3, v112\n"
    "v_lshl_add_u32 v248, v248, 2, v249\n"
    "v_mul_u32_u24 v249, 147, v248\n"
    "v_lshrrev_b32 v249, 11, v249\n"
    "v_mul_u32_u24 v250, 14, v249\n"
    "v_sub_u32 v250, v248, v250\n"
    "v_mul_u32_u24 v119, 112, v249\n"
    "v_and_b32 v248, 3, v250\n"
    "v_lshlrev_b32 v248, 4, v248\n"
    "v_lshrrev_b32 v249, 2, v250\n"
    "v_lshl_add_u32 v248, v249, 3, v248\n"
    "v_lshlrev_b32 v249, 3, v250\n"
    "v_cmp_gt_u32 vcc, 8, v250\n"
    "s_nop 1\n"
    "v_cndmask_b32 v248, v249, v248, vcc\n"
    "v_add_u32 v119, v119, v248\n"
    "v_bfe_u32 v248, v112, 2, 2\n"
    "v_mul_u32_u24 v248, 1088, v248\n"
    "v_add_u32 v119, v119, v248\n"
    "v_add_u32 v119, 147968, v119\n"
    "v_lshrrev_b32 v248, 4, v112\n"
    "v_and_b32 v249, 3, v112\n"
    "v_lshl_add_u32 v248, v248, 2, v249\n"
    "v_add_u32 v248, 64, v248\n"
    "v_min_u32 v248, 111, v248\n"
    "v_mul_u32_u24 v249, 147, v248\n"
    "v_lshrrev_b32 v249, 11, v249\n"
    "v_mul_u32_u24 v250, 14, v249\n"
    "v_sub_u32 v250, v248, v250\n"
    "v_mul_u32_u24 v120, 112, v249\n"
    "v_and_b32 v248, 3, v250\n"
    "v_lshlrev_b32 v248, 4, v248\n"
    "v_lshrrev_b32 v249, 2, v250\n"
    "v_lshl_add_u32 v248, v249, 3, v248\n"
    "v_lshlrev_b32 v249, 3, v250\n"
    "v_cmp_gt_u32 vcc, 8, v250\n"
    "s_nop 1\n"
    "v_cndmask_b32 v248, v249, v248, vcc\n"
    "v_add_u32 v120, v120, v248\n"
    "v_bfe_u32 v248, v112, 2, 2\n"
    "v_mul_u32_u24 v248, 1088, v248\n"
    "v_add_u32 v120, v120, v248\n"
    "v_add_u32 v120, 147968, v120\n"
    "s_mul_i32 s78, s53, 7168\n"
    "s_add_u32 s78, s78, 10485760\n"
    "v_lshl_add_u32 v121, v112, 4, s78\n"
    "v_add_u32 v122, 4096, v121\n"
    "v_mov_b32 v248, 1073741824\n"
    "v_cmp_gt_u32 vcc, 192, v112\n"
    "s_mul_i32 s79, s52, 2176\n"
    "s_add_u32 s79, s79, 147968\n"
    "v_cndmask_b32 v122, v248, v122, vcc\n"
    "v_mad_u32_u24 v118, v114, 14, v113\n"
    "v_lshl_add_u32 v118, v118, 4, s79\n"
    "v_mov_b32 v248, 156672\n"
    "v_cmp_gt_u32 vcc, 14, v113\n"
    "s_nop 1\n"
    "v_cndmask_b32 v118, v248, v118, vcc\n"
    "s_mov_b32 s25, 12582912\n"
    "buffer_store_dwordx4 v[80:83], v84, s[12:15], s25 offen sc1\n"
    "s_mov_b32 s25, 12587008\n"
    "buffer_store_dwordx4 v[80:83], v84, s[12:15], s25 offen sc1\n"
    "s_mov_b32 s25, 14680064\n"
    "buffer_store_dwordx4 v[80:83], v84, s[12:15], s25 offen sc1\n"
    "s_mov_b32 s25, 14684160\n"
    "buffer_store_dwordx4 v[80:83], v84, s[12:15], s25 offen sc1\n"
    "s_lshl_b32 s61, s53, 2\n"
    "s_add_u32 s61, s61, s52\n"
    "s_mul_i32 s61, s61, 7168\n"
    "s_add_u32 s61, s61, 0\n"
    "v_and_b32 v75, 63, v112\n"
    "v_min_u32 v75, 55, v75\n"
    "v_lshlrev_b32 v75, 7, v75\n"
    "buffer_load_dword v74, v75, s[32:35], s61 offen\n"
    "ds_write_b128 v90, a[48:51] offset:28672\n"
    "ds_write_b128 v91, a[52:55] offset:29696\n"
    "ds_write_b128 v90, a[56:59] offset:30720\n"
    "ds_write_b128 v91, a[60:63] offset:31744\n"
    "ds_read_b64_tr_b16 a[48:49], v68 offset:28672\n"
    "ds_read_b64_tr_b16 a[50:51], v68 offset:29184\n"
    "ds_read_b64_tr_b16 a[52:53], v69 offset:28672\n"
    "ds_read_b64_tr_b16 a[54:55], v69 offset:29184\n"
    "ds_read_b64_tr_b16 a[56:57], v70 offset:28672\n"
    "buffer_load_dwordx4 a[156:159], v85, s[28:31], s59 offen offset:3584\n"
    "ds_read_b64_tr_b16 a[58:59], v70 offset:29184\n"
    "ds_read_b64_tr_b16 a[60:61], v71 offset:28672\n"
    "ds_read_b64_tr_b16 a[62:63], v71 offset:29184\n"
    "ds_write_b128 v90, a[64:67] offset:28672\n"
    "ds_write_b128 v91, a[68:71] offset:29696\n"
    "ds_write_b128 v90, a[72:75] offset:30720\n"
    "ds_write_b128 v91, a[76:79] offset:31744\n"
    "ds_read_b64_tr_b16 a[64:65], v68 offset:28672\n"
    "ds_read_b64_tr_b16 a[66:67], v68 offset:29184\n"
    "s_add_u32 s59, s48, 946176\n"
    "buffer_load_dwordx4 a[160:163], v85, s[28:31], s59 offen offset:0\n"
    "ds_read_b64_tr_b16 a[68:69], v69 offset:28672\n"
    "ds_read_b64_tr_b16 a[70:71], v69 offset:29184\n"
    "ds_read_b64_tr_b16 a[72:73], v70 offset:28672\n"
    "ds_read_b64_tr_b16 a[74:75], v70 offset:29184\n"
    "ds_read_b64_tr_b16 a[76:77], v71 offset:28672\n"
    "ds_read_b64_tr_b16 a[78:79], v71 offset:29184\n"
    "ds_write_b128 v90, a[80:83] offset:28672\n"
    "ds_write_b128 v91, a[84:87] offset:29696\n"
    "ds_write_b128 v90, a[88:91] offset:30720\n"
    "buffer_load_dwordx4 a[164:167], v85, s[28:31], s59 offen offset:3584\n"
    "ds_write_b128 v91, a[92:95] offset:31744\n"
    "ds_read_b64_tr_b16 a[80:81], v68 offset:28672\n"
    "ds_read_b64_tr_b16 a[82:83], v68 offset:29184\n"
    "ds_read_b64_tr_b16 a[84:85], v69 offset:28672\n"
    "ds_read_b64_tr_b16 a[86:87], v69 offset:29184\n"
    "ds_read_b64_tr_b16 a[88:89], v70 offset:28672\n"
    "ds_read_b64_tr_b16 a[90:91], v70 offset:29184\n"
    "ds_read_b64_tr_b16 a[92:93], v71 offset:28672\n"
    "ds_read_b64_tr_b16 a[94:95], v71 offset:29184\n"
    "s_add_u32 s59, s48, 953344\n"
    "buffer_load_dwordx4 a[168:171], v85, s[28:31], s59 offen offset:0\n"
    "ds_write_b128 v90, a[96:99] offset:28672\n"
    "ds_write_b128 v91, a[100:103] offset:29696\n"
    "ds_write_b128 v90, a[104:107] offset:30720\n"
    "ds_write_b128 v91, a[108:111] offset:31744\n"
    "ds_read_b64_tr_b16 a[96:97], v68 offset:28672\n"
    "ds_read_b64_tr_b16 a[98:99], v68 offset:29184\n"
    "ds_read_b64_tr_b16 a[100:101], v69 offset:28672\n"
    "ds_read_b64_tr_b16 a[102:103], v69 offset:29184\n"
    "ds_read_b64_tr_b16 a[104:105], v70 offset:28672\n"
    "ds_read_b64_tr_b16 a[106:107], v70 offset:29184\n"
    "ds_read_b64_tr_b16 a[108:109], v71 offset:28672\n"
    "ds_read_b64_tr_b16 a[110:111], v71 offset:29184\n"
    "s_waitcnt vmcnt(10)\n"
    "v_max3_u32 v244, v188, v189, v190\n"
    "s_waitcnt vmcnt(9)\n"
    "v_max3_u32 v244, v244, v191, v192\n"
    "v_max3_u32 v244, v244, v193, v194\n"
    "v_max_u32 v244, v244, v195\n"
    "v_cmp_eq_u32 vcc, -1, v244\n"
    "s_cbranch_vccnz .Lretry_8\n"
    ".Lz_9:\n"
    "ds_write_b128 v116, v[188:191] offset:0\n"
    "ds_write_b128 v116, v[192:195] offset:1024\n"
    "buffer_store_dword v94, v95, s[12:15], 0 offen sc1\n"
    ".Lmark_p3in_10:\n"
    "s_waitcnt lgkmcnt(0)\n"
    "s_barrier\n"
    "ds_read_b64 v[188:189], v117 offset:0\n"
    "ds_read_b64 v[190:191], v117 offset:1024\n"
    "ds_read_b64 v[192:193], v117 offset:512\n"
    "ds_read_b64 v[194:195], v117 offset:1536\n"
    "s_waitcnt lgkmcnt(2)\n"
    "v_mfma_f32_16x16x32_bf16 v[196:199], v[188:191], v[124:127], 0\n"
    "v_mfma_f32_16x16x32_bf16 v[200:203], v[188:191], v[128:131], 0\n"
    "s_waitcnt lgkmcnt(0)\n"
    "v_mfma_f32_16x16x32_bf16 v[204:207], v[192:195], v[124:127], 0\n"
    "v_mfma_f32_16x16x32_bf16 v[208:211], v[192:195], v[128:131], 0\n"
    "v_mfma_f32_16x16x32_bf16 v[212:215], v[188:191], v[132:135], 0\n"
    "buffer_load_dwordx4 a[172:175], v85, s[28:31], s59 offen offset:3584\n"
    "v_mfma_f32_16x16x32_bf16 v[216:219], v[188:191], v[136:139], 0\n"
    "v_mfma_f32_16x16x32_bf16 v[220:223], v[192:195], v[132:135], 0\n"
    "v_mfma_f32_16x16x32_bf16 v[224:227], v[192:195], v[136:139], 0\n"
    "v_pk_mul_f32 v[188:189], v[196:197], v[140:141]\n"
    "v_pk_mul_f32 v[192:193], v[200:201], v[140:141]\n"
    "v_pk_mul_f32 v[190:191], v[198:199], v[142:143]\n"
    "v_pk_mul_f32 v[194:195], v[202:203], v[142:143]\n"
    "v_pk_fma_f32 v[188:189], v[200:201], v[148:149], v[188:189] neg_lo:[1,0,0] neg_hi:[1,0,0]\n"
    "v_pk_fma_f32 v[192:193], v[196:197], v[148:149], v[192:193]\n"
    "v_pk_fma_f32 v[190:191], v[202:203], v[150:151], v[190:191] neg_lo:[1,0,0] neg_hi:[1,0,0]\n"
    "v_pk_fma_f32 v[194:195], v[198:199], v[150:151], v[194:195]\n"
    "v_cvt_pk_bf16_f32 v228, v188, v192\n"
    "v_cvt_pk_bf16_f32 v229, v189, v193\n"
    "v_cvt_pk_bf16_f32 v230, v190, v194\n"
    "s_add_u32 s59, s48, 960512\n"
    "buffer_load_dwordx4 a[176:179], v85, s[28:31], s59 offen offset:0\n"
    "v_cvt_pk_bf16_f32 v231, v191, v195\n"
    "v_pk_mul_f32 v[244:245], v[204:205], v[144:145]\n"
    "v_pk_mul_f32 v[248:249], v[208:209], v[144:145]\n"
    "v_pk_mul_f32 v[246:247], v[206:207], v[146:147]\n"
    "v_pk_mul_f32 v[250:251], v[210:211], v[146:147]\n"
    "v_pk_fma_f32 v[244:245], v[208:209], v[152:153], v[244:245] neg_lo:[1,0,0] neg_hi:[1,0,0]\n"
    "v_pk_fma_f32 v[248:249], v[204:205], v[152:153], v[248:249]\n"
    "v_pk_fma_f32 v[246:247], v[210:211], v[154:155], v[246:247] neg_lo:[1,0,0] neg_hi:[1,0,0]\n"
    "v_pk_fma_f32 v[250:251], v[206:207], v[154:155], v[250:251]\n"
    "v_cvt_pk_bf16_f32 v232, v244, v248\n"
    "v_cvt_pk_bf16_f32 v233, v245, v249\n"
    "v_cvt_pk_bf16_f32 v234, v246, v250\n"
    "v_cvt_pk_bf16_f32 v235, v247, v251\n"
    "v_pk_mul_f32 v[244:245], v[212:213], v[140:141]\n"
    "v_pk_mul_f32 v[248:249], v[216:217], v[140:141]\n"
    "v_pk_mul_f32 v[246:247], v[214:215], v[142:143]\n"
    "v_pk_mul_f32 v[250:251], v[218:219], v[142:143]\n"
    "v_pk_fma_f32 v[244:245], v[216:217], v[148:149], v[244:245] neg_lo:[1,0,0] neg_hi:[1,0,0]\n"
    "v_pk_fma_f32 v[248:249], v[212:213], v[148:149], v[248:249]\n"
    "v_pk_fma_f32 v[246:247], v[218:219], v[150:151], v[246:247] neg_lo:[1,0,0] neg_hi:[1,0,0]\n"
    "v_pk_fma_f32 v[250:251], v[214:215], v[150:151], v[250:251]\n"
    "v_cvt_pk_bf16_f32 v236, v244, v248\n"
    "v_cvt_pk_bf16_f32 v237, v245, v249\n"
    "buffer_load_dwordx4 a[180:183], v85, s[28:31], s59 offen offset:3584\n"
    "v_cvt_pk_bf16_f32 v238, v246, v250\n"
    "v_cvt_pk_bf16_f32 v239, v247, v251\n"
    "v_pk_mul_f32 v[188:189], v[220:221], v[144:145]\n"
    "v_pk_mul_f32 v[192:193], v[224:225], v[144:145]\n"
    "v_pk_mul_f32 v[190:191], v[222:223], v[146:147]\n"
    "v_pk_mul_f32 v[194:195], v[226:227], v[146:147]\n"
    "v_pk_fma_f32 v[188:189], v[224:225], v[152:153], v[188:189] neg_lo:[1,0,0] neg_hi:[1,0,0]\n"
    "v_pk_fma_f32 v[192:193], v[220:221], v[152:153], v[192:193]\n"
    "v_pk_fma_f32 v[190:191], v[226:227], v[154:155], v[190:191] neg_lo:[1,0,0] neg_hi:[1,0,0]\n"
    "v_pk_fma_f32 v[194:195], v[222:223], v[154:155], v[194:195]\n"
    "v_cvt_pk_bf16_f32 v240, v188, v192\n"
    "v_cvt_pk_bf16_f32 v241, v189, v193\n"
    "v_cvt_pk_bf16_f32 v242, v190, v194\n"
    "v_cvt_pk_bf16_f32 v243, v191, v195\n"
    "v_mfma_f32_16x16x32_bf16 v[172:175], v[156:159], v[228:231], v[172:175]\n"
    "v_mfma_f32_16x16x32_bf16 v[176:179], v[164:167], v[228:231], v[176:179]\n"
    "v_mfma_f32_16x16x32_bf16 v[172:175], v[160:163], v[232:235], v[172:175]\n"
    "s_add_u32 s59, s48, 967680\n"
    "buffer_load_dwordx4 a[184:187], v85, s[28:31], s59 offen offset:0\n"
    "v_mfma_f32_16x16x32_bf16 v[176:179], v[168:171], v[232:235], v[176:179]\n"
    "v_mfma_f32_16x16x32_bf16 v[180:183], v[156:159], v[236:239], v[180:183]\n"
    "v_mfma_f32_16x16x32_bf16 v[184:187], v[164:167], v[236:239], v[184:187]\n"
    "v_mfma_f32_16x16x32_bf16 v[180:183], v[160:163], v[240:243], v[180:183]\n"
    "v_mfma_f32_16x16x32_bf16 v[184:187], v[168:171], v[240:243], v[184:187]\n"
    "v_pk_add_f32 v[244:245], v[172:173], v[174:175]\n"
    "v_pk_mul_f32 v[246:247], v[172:173], v[172:173]\n"
    "s_nop 1\n"
    "v_pk_add_f32 v[248:249], v[180:181], v[182:183]\n"
    "buffer_load_dwordx4 a[188:191], v85, s[28:31], s59 offen offset:3584\n"
    "v_pk_mul_f32 v[250:251], v[180:181], v[180:181]\n"
    "v_pk_add_f32 v[244:245], v[244:245], v[176:177]\n"
    "v_pk_add_f32 v[248:249], v[248:249], v[184:185]\n"
    "v_pk_add_f32 v[244:245], v[244:245], v[178:179]\n"
    "v_pk_add_f32 v[248:249], v[248:249], v[186:187]\n"
    "v_pk_fma_f32 v[246:247], v[174:175], v[174:175], v[246:247]\n"
    "v_pk_fma_f32 v[250:251], v[182:183], v[182:183], v[250:251]\n"
    "v_pk_fma_f32 v[246:247], v[176:177], v[176:177], v[246:247]\n"
    "v_pk_fma_f32 v[250:251], v[184:185], v[184:185], v[250:251]\n"
    "v_pk_fma_f32 v[246:247], v[178:179], v[178:179], v[246:247]\n"
    "v_pk_fma_f32 v[250:251], v[186:187], v[186:187], v[250:251]\n"
    "v_add_f32 v244, v244, v245\n"
    "v_add_f32 v248, v248, v249\n"
    "v_add_f32 v246, v246, v247\n"
    "v_add_f32 v250, v250, v251\n"
    "s_nop 0\n"
    "v_permlane32_swap_b32 v244, v246\n"
    "v_permlane32_swap_b32 v248, v250\n"
    "v_add_f32 v244, v244, v246\n"
    "v_add_f32 v248, v248, v250\n"
    "s_nop 1\n"
    "v_permlane16_swap_b32 v244, v248\n"
    "v_add_f32 v244, v244, v248\n"
    "s_nop 1\n"
    "v_add_f32_dpp v244, v244, v244 row_ror:8 row_mask:0xf bank_mask:0xf\n"
    "s_nop 1\n"
    "v_add_f32_dpp v244, v244, v244 row_ror:4 row_mask:0xf bank_mask:0xf\n"
    "s_add_u32 s59, s48, 974848\n"
    "buffer_load_dwordx4 a[192:195], v85, s[28:31], s59 offen offset:0\n"
    "v_add_f32_dpp v244, v244, v244 row_ror:2 row_mask:0xf bank_mask:0xf\n"
    "s_nop 1\n"
    "v_add_f32_dpp v244, v244, v244 row_ror:1 row_mask:0xf bank_mask:0xf\n"
    "v_mul_f32 v244, 0x3b124925, v244\n"
    "v_mov_b32 v245, v244\n"
    "s_nop 1\n"
    "v_permlane32_swap_b32 v244, v245\n"
    "v_fma_f32 v245, -v244, v244, v245\n"
    "v_max_f32 v245, 0, v245\n"
    "v_add_f32 v246, 0x3727c5ac, v245\n"
    "v_rsq_f32 v246, v246\n"
    "s_nop 0\n"
    "v_mul_f32 v247, v246, v246\n"
    "v_fmaak_f32 v247, v247, v245, 0x3727c5ac\n"
    "v_rsq_f32 v247, v247\n"
    "s_nop 0\n"
    "v_mul_f32 v246, v246, v247\n"
    "v_mul_f32 v244, -v244, v246\n"
    "v_readlane_b32 s64, v246, 0\n"
    "v_readlane_b32 s66, v246, 16\n"
    "v_readlane_b32 s65, v244, 0\n"
    "v_readlane_b32 s67, v244, 16\n"
    "s_nop 0\n"
    "v_pk_fma_f32 v[196:197], v[172:173], s[64:65], s[64:65] op_sel:[0,0,1] op_sel_hi:[1,0,1]\n"
    "v_pk_fma_f32 v[198:199], v[174:175], s[64:65], s[64:65] op_sel:[0,0,1] op_sel_hi:[1,0,1]\n"
    "v_pk_fma_f32 v[200:201], v[176:177], s[64:65], s[64:65] op_sel:[0,0,1] op_sel_hi:[1,0,1]\n"
    "buffer_load_dwordx4 a[196:199], v85, s[28:31], s59 offen offset:3584\n"
    "v_pk_fma_f32 v[202:203], v[178:179], s[64:65], s[64:65] op_sel:[0,0,1] op_sel_hi:[1,0,1]\n"
    "v_pk_fma_f32 v[204:205], v[180:181], s[66:67], s[66:67] op_sel:[0,0,1] op_sel_hi:[1,0,1]\n"
    "v_pk_fma_f32 v[206:207], v[182:183], s[66:67], s[66:67] op_sel:[0,0,1] op_sel_hi:[1,0,1]\n"
    "v_pk_fma_f32 v[208:209], v[184:185], s[66:67], s[66:67] op_sel:[0,0,1] op_sel_hi:[1,0,1]\n"
    "v_pk_fma_f32 v[210:211], v[186:187], s[66:67], s[66:67] op_sel:[0,0,1] op_sel_hi:[1,0,1]\n"
    "v_cvt_pk_bf16_f32 v228, v196, v197\n"
    "v_cvt_pk_bf16_f32 v229, v198, v199\n"
    "v_cvt_pk_bf16_f32 v230, v200, v201\n"
    "v_cvt_pk_bf16_f32 v231, v202, v203\n"
    "v_cvt_pk_bf16_f32 v232, v204, v205\n"
    "v_cvt_pk_bf16_f32 v233, v206, v207\n"
    "v_cvt_pk_bf16_f32 v234, v208, v209\n"
    "v_cvt_pk_bf16_f32 v235, v210, v211\n"
    "ds_write_b128 v118, v[228:231] offset:0\n"
    "ds_write_b128 v118, v[232:235] offset:1088\n"
    "s_waitcnt vmcnt(13)\n"
    "s_waitcnt lgkmcnt(0)\n"
    "s_barrier\n"
    "ds_read_b64_tr_b16 v[196:197], v119 offset:0\n"
    "ds_read_b64_tr_b16 v[198:199], v119 offset:4352\n"
    "ds_read_b64_tr_b16 v[200:201], v120 offset:0\n"
    "ds_read_b64_tr_b16 v[202:203], v120 offset:4352\n"
    "s_waitcnt lgkmcnt(2)\n"
    "buffer_store_dwordx4 v[196:199], v121, s[12:15], 0 offen sc1\n"
    "s_waitcnt lgkmcnt(0)\n"
    "buffer_store_dwordx4 v[200:203], v122, s[12:15], 0 offen sc1\n"
    ".Lmark_p3out_11:\n"
    "v_cvt_pk_bf16_f32 v96, v172, v173\n"
    "v_cvt_pk_bf16_f32 v97, v174, v175\n"
    "v_cvt_pk_bf16_f32 v98, v176, v177\n"
    "v_cvt_pk_bf16_f32 v99, v178, v179\n"
    "v_cvt_pk_bf16_f32 v100, v180, v181\n"
    "v_cvt_pk_bf16_f32 v101, v182, v183\n"
    "v_cvt_pk_bf16_f32 v102, v184, v185\n"
    "v_cvt_pk_bf16_f32 v103, v186, v187\n"
    "ds_write_b128 v90, a[112:115] offset:28672\n"
    "ds_write_b128 v91, a[116:119] offset:29696\n"
    "ds_write_b128 v90, a[120:123] offset:30720\n"
    "ds_write_b128 v91, a[124:127] offset:31744\n"
    "ds_read_b64_tr_b16 a[112:113], v68 offset:28672\n"
    "ds_read_b64_tr_b16 a[114:115], v68 offset:29184\n"
    "ds_read_b64_tr_b16 a[116:117], v69 offset:28672\n"
    "s_add_u32 s59, s48, 982016\n"
    "buffer_load_dwordx4 a[200:203], v85, s[28:31], s59 offen offset:0\n"
    "ds_read_b64_tr_b16 a[118:119], v69 offset:29184\n"
    "ds_read_b64_tr_b16 a[120:121], v70 offset:28672\n"
    "ds_read_b64_tr_b16 a[122:123], v70 offset:29184\n"
    "ds_read_b64_tr_b16 a[124:125], v71 offset:28672\n"
    "ds_read_b64_tr_b16 a[126:127], v71 offset:29184\n"
    "ds_write_b128 v90, a[128:131] offset:28672\n"
    "ds_write_b128 v91, a[132:135] offset:29696\n"
    "buffer_load_dwordx4 a[204:207], v85, s[28:31], s59 offen offset:3584\n"
    "ds_write_b128 v90, a[136:139] offset:30720\n"
    "ds_write_b128 v91, a[140:143] offset:31744\n"
    "ds_read_b64_tr_b16 a[128:129], v68 offset:28672\n"
    "ds_read_b64_tr_b16 a[130:131], v68 offset:29184\n"
    "ds_read_b64_tr_b16 a[132:133], v69 offset:28672\n"
    "ds_read_b64_tr_b16 a[134:135], v69 offset:29184\n"
    "ds_read_b64_tr_b16 a[136:137], v70 offset:28672\n"
    "s_add_u32 s59, s48, 989184\n"
    "buffer_load_dwordx4 a[208:211], v85, s[28:31], s59 offen offset:0\n"
    "ds_read_b64_tr_b16 a[138:139], v70 offset:29184\n"
    "ds_read_b64_tr_b16 a[140:141], v71 offset:28672\n"
    "ds_read_b64_tr_b16 a[142:143], v71 offset:29184\n"
    "ds_write_b128 v90, a[144:147] offset:28672\n"
    "ds_write_b128 v91, a[148:151] offset:29696\n"
    "ds_write_b128 v90, a[152:155] offset:30720\n"
    "s_waitcnt vmcnt(16)\n"
    "ds_write_b128 v91, a[156:159] offset:31744\n"
    "buffer_load_dwordx4 a[212:215], v85, s[28:31], s59 offen offset:3584\n"
    "ds_read_b64_tr_b16 a[144:145], v68 offset:28672\n"
    "ds_read_b64_tr_b16 a[146:147], v68 offset:29184\n"
    "ds_read_b64_tr_b16 a[148:149], v69 offset:28672\n"
    "ds_read_b64_tr_b16 a[150:151], v69 offset:29184\n"
    "ds_read_b64_tr_b16 a[152:153], v70 offset:28672\n"
    "ds_read_b64_tr_b16 a[154:155], v70 offset:29184\n"
    "ds_read_b64_tr_b16 a[156:157], v71 offset:28672\n"
    "ds_read_b64_tr_b16 a[158:159], v71 offset:29184\n"
    "s_waitcnt lgkmcnt(0)\n"
    "s_barrier\n"
    "v_and_b32 v188, 15, v112\n"
    "v_bfe_u32 v189, v112, 4, 2\n"
    "v_mul_u32_u24 v113, 7168, v189\n"
    "s_mul_i32 s75, s56, 896\n"
    "s_add_u32 s75, s75, 28672\n"
    "v_add_u32 v113, s75, v113\n"
    "v_lshrrev_b32 v114, 3, v188\n"
    "s_mul_i32 s76, s51, -57344\n"
    "s_add_u32 s76, s76, 28672\n"
    "v_mul_lo_u32 v114, s76, v114\n"
    "v_add_u32 v114, v114, v113\n"
    "v_lshl_add_u32 v113, v188, 4, v113\n"
    "v_and_b32 v188, 7, v188\n"
    "v_lshl_add_u32 v114, v188, 4, v114\n"
    "v_and_b32 v188, 15, v112\n"
    "v_bfe_u32 v190, v112, 4, 2\n"
    "v_lshlrev_b32 v115, 10, v190\n"
    "v_and_b32 v190, 1, v190\n"
    "v_lshlrev_b32 v190, 1, v190\n"
    "v_bfe_u32 v116, v112, 3, 1\n"
    "v_or_b32 v190, v190, v116\n"
    "v_xor_b32 v190, s52, v190\n"
    "v_lshl_add_u32 v115, v190, 5, v115\n"
    "v_bfe_u32 v190, v112, 2, 2\n"
    "v_lshl_add_u32 v115, v190, 7, v115\n"
    "v_and_b32 v190, 3, v112\n"
    "v_lshl_add_u32 v115, v190, 3, v115\n"
    "v_add_u32 v115, s11, v115\n"
    "v_xor_b32 v116, 32, v115\n"
    "v_xor_b32 v117, 64, v115\n"
    "v_xor_b32 v118, 96, v115\n"
    "v_and_b32 v119, 63, v112\n"
    "v_mul_u32_u24 v250, 7168, v119\n"
    "v_cmp_gt_u32 vcc, 56, v119\n"
    "v_lshlrev_b32 v119, 4, v119\n"
    "s_lshl_b32 s74, s52, 13\n"
    "s_add_u32 s74, s74, 131072\n"
    "v_add_u32 v119, s74, v119\n"
    "v_xor_b32 v120, 8192, v119\n"
    "v_xor_b32 v121, 16384, v119\n"
    "v_xor_b32 v122, 24576, v119\n"
    "s_add_u32 s75, s75, 892\n"
    "v_add_u32 v125, s75, v250\n"
    "v_mov_b32 v190, 1073741824\n"
    "v_cndmask_b32 v250, v190, v125, vcc\n"
    "s_mul_i32 s71, s52, 458752\n"
    "s_add_u32 s71, s71, 10457088\n"
    "s_mul_i32 s82, s52, 401408\n"
    "s_add_u32 s82, s82, 12554240\n"
    "s_lshl_b32 s74, s57, 2\n"
    "s_add_u32 s74, s74, s52\n"
    "s_lshl_b32 s75, s74, 5\n"
    "v_lshrrev_b32 v191, 1, v189\n"
    "v_lshl_add_u32 v252, v191, 4, s75\n"
    "s_mul_i32 s74, s74, 14336\n"
    "s_mul_i32 s75, s56, 896\n"
    "s_add_u32 s74, s74, s75\n"
    "s_add_u32 s74, s74, 12582912\n"
    "s_add_u32 s74, s74, s7\n"
    "v_mul_u32_u24 v248, 7168, v191\n"
    "v_and_b32 v191, 31, v112\n"
    "v_lshl_add_u32 v248, v191, 4, v248\n"
    "v_add_u32 v248, s74, v248\n"
    "v_cmp_gt_u32 vcc, 24, v191\n"
    "s_lshl_b32 s74, s6, 7\n"
    "s_lshl_b32 s75, s52, 5\n"
    "s_add_u32 s74, s74, s75\n"
    "v_cndmask_b32 v249, v190, v248, vcc\n"
    "v_lshl_add_u32 v123, v188, 1, s74\n"
    "s_mov_b32 s64, 0xbdd2d3e8\n"
    "s_mov_b32 s65, 0xc0135761\n"
    "v_mov_b32 v232, v85\n"
    "v_mov_b32 v233, v86\n"
    "v_mov_b32 v234, v90\n"
    "v_mov_b32 v235, v91\n"
    "v_bfe_u32 v237, v112, 4, 1\n"
    "v_lshlrev_b32 v237, 5, v237\n"
    "v_and_b32 v236, 63, v112\n"
    "v_lshlrev_b32 v236, 4, v236\n"
    "v_xor_b32 v236, v236, v237\n"
    "v_add_u32 v236, s9, v236\n"
    "v_xor_b32 v237, 64, v236\n"
    "v_and_b32 v236, 0x70, v236\n"
    "v_bfe_u32 v237, v112, 3, 3\n"
    "v_lshl_add_u32 v236, v237, 12, v236\n"
    "v_xor_b32 v237, 64, v236\n"
    "s_waitcnt vmcnt(0)\n"
    ".Lg4top_12:\n"
    "buffer_load_dword v124, v125, s[12:15], s71 offen sc1\n"
    "s_waitcnt vmcnt(0)\n"
    "v_cmp_eq_u32 vcc, -1, v124\n"
    "s_cbranch_vccnz .Lg4retry_13\n"
    ".Lg4ok_14:\n"
    "s_add_u32 s70, s71, 0\n"
    "buffer_load_dwordx4 v[188:191], v113, s[12:15], s70 offen offset:512 sc1\n"
    "buffer_load_dwordx4 v[192:195], v114, s[12:15], s70 offen offset:768 sc1\n"
    "s_add_u32 s70, s70, 28672\n"
    "buffer_load_dwordx4 v[196:199], v113, s[12:15], s70 offen offset:512 sc1\n"
    "s_add_u32 s70, s70, 28672\n"
    "buffer_load_dwordx4 v[200:203], v113, s[12:15], s70 offen offset:512 sc1\n"
    "buffer_load_dwordx4 v[204:207], v114, s[12:15], s70 offen offset:768 sc1\n"
    "s_add_u32 s70, s70, 28672\n"
    "buffer_load_dwordx4 v[208:211], v113, s[12:15], s70 offen offset:512 sc1\n"
    "s_add_u32 s70, s70, 28672\n"
    "buffer_load_dwordx4 v[212:215], v113, s[12:15], s70 offen offset:512 sc1\n"
    "buffer_load_dwordx4 v[216:219], v114, s[12:15], s70 offen offset:768 sc1\n"
    ".Lmark_p4in_15:\n"
    "s_waitcnt vmcnt(7)\n"
    "v_mfma_f32_16x16x32_bf16 v[124:127], a[0:3], v[188:191], 0\n"
    "s_add_u32 s70, s70, 28672\n"
    "buffer_load_dwordx4 v[220:223], v113, s[12:15], s70 offen offset:512 sc1\n"
    "v_mfma_f32_16x16x32_bf16 v[132:135], a[4:7], v[188:191], 0\n"
    "s_waitcnt vmcnt(7)\n"
    "v_cndmask_b32 v228, 0, v192, s[4:5]\n"
    "v_cndmask_b32 v229, 0, v193, s[4:5]\n"
    "v_mfma_f32_16x16x32_bf16 v[140:143], a[8:11], v[188:191], 0\n"
    "s_add_u32 s59, s48, 996352\n"
    "buffer_load_dwordx4 a[216:219], v232, s[28:31], s59 offen offset:0\n"
    "v_cndmask_b32 v230, 0, v194, s[4:5]\n"
    "v_cndmask_b32 v231, 0, v195, s[4:5]\n"
    "v_mfma_f32_16x16x32_bf16 v[148:151], a[12:15], v[188:191], 0\n"
    "s_add_u32 s70, s70, 28672\n"
    "buffer_load_dwordx4 v[224:227], v113, s[12:15], s70 offen offset:512 sc1\n"
    "v_mfma_f32_16x16x32_bf16 v[128:131], a[0:3], v[228:231], 0\n"
    "buffer_load_dwordx4 v[188:191], v114, s[12:15], s70 offen offset:768 sc1\n"
    "v_cndmask_b32 v192, v192, 0, s[4:5]\n"
    "buffer_load_dwordx4 a[220:223], v232, s[28:31], s59 offen offset:3584\n"
    "v_mfma_f32_16x16x32_bf16 v[136:139], a[4:7], v[228:231], 0\n"
    "v_cndmask_b32 v193, v193, 0, s[4:5]\n"
    "v_cndmask_b32 v194, v194, 0, s[4:5]\n"
    "v_mfma_f32_16x16x32_bf16 v[144:147], a[8:11], v[228:231], 0\n"
    "v_cndmask_b32 v195, v195, 0, s[4:5]\n"
    "buffer_load_dwordx4 v[252:255], v252, s[36:39], 0 offen\n"
    "v_mfma_f32_16x16x32_bf16 v[152:155], a[12:15], v[228:231], 0\n"
    "s_add_u32 s59, s48, 1003520\n"
    "buffer_load_dwordx4 a[224:227], v232, s[28:31], s59 offen offset:0\n"
    "buffer_load_ushort v123, v123, s[40:43], 0 offen\n"
    "s_waitcnt vmcnt(13)\n"
    "v_mfma_f32_16x16x32_bf16 v[124:127], a[16:19], v[196:199], v[124:127]\n"
    "v_mfma_f32_16x16x32_bf16 v[132:135], a[20:23], v[196:199], v[132:135]\n"
    "v_mfma_f32_16x16x32_bf16 v[140:143], a[24:27], v[196:199], v[140:143]\n"
    "v_mfma_f32_16x16x32_bf16 v[148:151], a[28:31], v[196:199], v[148:151]\n"
    "buffer_load_dwordx4 a[228:231], v232, s[28:31], s59 offen offset:3584\n"
    "s_add_u32 s70, s70, 28672\n"
    "buffer_load_dwordx4 v[196:199], v113, s[12:15], s70 offen offset:512 sc1\n"
    "v_mfma_f32_16x16x32_bf16 v[128:131], a[16:19], v[192:195], v[128:131]\n"
    "v_mfma_f32_16x16x32_bf16 v[136:139], a[20:23], v[192:195], v[136:139]\n"
    "v_mfma_f32_16x16x32_bf16 v[144:147], a[24:27], v[192:195], v[144:147]\n"
    "s_add_u32 s59, s48, 1010688\n"
    "buffer_load_dwordx4 a[232:235], v232, s[28:31], s59 offen offset:0\n"
    "s_waitcnt vmcnt(14)\n"
    "v_cndmask_b32 v228, 0, v204, s[4:5]\n"
    "v_cndmask_b32 v229, 0, v205, s[4:5]\n"
    "v_mfma_f32_16x16x32_bf16 v[152:155], a[28:31], v[192:195], v[152:155]\n"
    "v_cndmask_b32 v230, 0, v206, s[4:5]\n"
    "v_mfma_f32_16x16x32_bf16 v[124:127], a[32:35], v[200:203], v[124:127]\n"
    "s_add_u32 s70, s70, 28672\n"
    "buffer_load_dwordx4 v[192:195], v113, s[12:15], s70 offen offset:512 sc1\n"
    "v_mfma_f32_16x16x32_bf16 v[132:135], a[36:39], v[200:203], v[132:135]\n"
    "buffer_load_dwordx4 a[236:239], v232, s[28:31], s59 offen offset:3584\n"
    "v_cndmask_b32 v231, 0, v207, s[4:5]\n"
    "v_mfma_f32_16x16x32_bf16 v[140:143], a[40:43], v[200:203], v[140:143]\n"
    "v_mfma_f32_16x16x32_bf16 v[148:151], a[44:47], v[200:203], v[148:151]\n"
    "buffer_load_dwordx4 v[200:203], v114, s[12:15], s70 offen offset:768 sc1\n"
    "v_mfma_f32_16x16x32_bf16 v[128:131], a[32:35], v[228:231], v[128:131]\n"
    "s_add_u32 s59, s48, 1017856\n"
    "buffer_load_dwordx4 a[240:243], v232, s[28:31], s59 offen offset:0\n"
    "v_cndmask_b32 v204, v204, 0, s[4:5]\n"
    "v_cndmask_b32 v205, v205, 0, s[4:5]\n"
    "v_mfma_f32_16x16x32_bf16 v[136:139], a[36:39], v[228:231], v[136:139]\n"
    "v_cndmask_b32 v206, v206, 0, s[4:5]\n"
    "v_cndmask_b32 v207, v207, 0, s[4:5]\n"
    "v_mfma_f32_16x16x32_bf16 v[144:147], a[40:43], v[228:231], v[144:147]\n"
    "v_mfma_f32_16x16x32_bf16 v[152:155], a[44:47], v[228:231], v[152:155]\n"
    "buffer_load_dwordx4 a[244:247], v232, s[28:31], s59 offen offset:3584\n"
    "s_waitcnt vmcnt(18)\n"
    "v_mfma_f32_16x16x32_bf16 v[124:127], a[48:51], v[208:211], v[124:127]\n"
    "v_mfma_f32_16x16x32_bf16 v[132:135], a[52:55], v[208:211], v[132:135]\n"
    "v_mfma_f32_16x16x32_bf16 v[140:143], a[56:59], v[208:211], v[140:143]\n"
    "v_mfma_f32_16x16x32_bf16 v[148:151], a[60:63], v[208:211], v[148:151]\n"
    "s_add_u32 s59, s48, 1025024\n"
    "buffer_load_dwordx4 a[248:251], v232, s[28:31], s59 offen offset:0\n"
    "s_add_u32 s70, s70, 28672\n"
    "buffer_load_dwordx4 v[208:211], v113, s[12:15], s70 offen offset:512 sc1\n"
    "v_mfma_f32_16x16x32_bf16 v[128:131], a[48:51], v[204:207], v[128:131]\n"
    "v_mfma_f32_16x16x32_bf16 v[136:139], a[52:55], v[204:207], v[136:139]\n"
    "v_mfma_f32_16x16x32_bf16 v[144:147], a[56:59], v[204:207], v[144:147]\n"
    "buffer_load_dwordx4 a[252:255], v232, s[28:31], s59 offen offset:3584\n"
    "s_waitcnt vmcnt(19)\n"
    "v_cndmask_b32 v228, 0, v216, s[4:5]\n"
    "v_cndmask_b32 v229, 0, v217, s[4:5]\n"
    "v_mfma_f32_16x16x32_bf16 v[152:155], a[60:63], v[204:207], v[152:155]\n"
    "v_cndmask_b32 v230, 0, v218, s[4:5]\n"
    "v_mfma_f32_16x16x32_bf16 v[124:127], a[64:67], v[212:215], v[124:127]\n"
    "s_add_u32 s70, s70, 28672\n"
    "buffer_load_dwordx4 v[204:207], v113, s[12:15], s70 offen offset:512 sc1\n"
    "v_mfma_f32_16x16x32_bf16 v[132:135], a[68:71], v[212:215], v[132:135]\n"
    "s_add_u32 s59, s49, 0\n"
    "buffer_load_dwordx4 v[0:3], v233, s[32:35], s59 offen\n"
    "v_cndmask_b32 v231, 0, v219, s[4:5]\n"
    "v_mfma_f32_16x16x32_bf16 v[140:143], a[72:75], v[212:215], v[140:143]\n"
    "v_mfma_f32_16x16x32_bf16 v[148:151], a[76:79], v[212:215], v[148:151]\n"
    "buffer_load_dwordx4 v[212:215], v114, s[12:15], s70 offen offset:768 sc1\n"
    "v_mfma_f32_16x16x32_bf16 v[128:131], a[64:67], v[228:231], v[128:131]\n"
    "s_add_u32 s59, s49, 32768\n"
    "buffer_load_dwordx4 v[4:7], v233, s[32:35], s59 offen\n"
    "v_cndmask_b32 v216, v216, 0, s[4:5]\n"
    "v_cndmask_b32 v217, v217, 0, s[4:5]\n"
    "v_mfma_f32_16x16x32_bf16 v[136:139], a[68:71], v[228:231], v[136:139]\n"
    "v_cndmask_b32 v218, v218, 0, s[4:5]\n"
    "v_cndmask_b32 v219, v219, 0, s[4:5]\n"
    "v_mfma_f32_16x16x32_bf16 v[144:147], a[72:75], v[228:231], v[144:147]\n"
    "v_mfma_f32_16x16x32_bf16 v[152:155], a[76:79], v[228:231], v[152:155]\n"
    "s_add_u32 s59, s49, 65536\n"
    "buffer_load_dwordx4 v[8:11], v233, s[32:35], s59 offen\n"
    "s_waitcnt vmcnt(23)\n"
    "v_mfma_f32_16x16x32_bf16 v[124:127], a[80:83], v[220:223], v[124:127]\n"
    "v_mfma_f32_16x16x32_bf16 v[132:135], a[84:87], v[220:223], v[132:135]\n"
    "v_mfma_f32_16x16x32_bf16 v[140:143], a[88:91], v[220:223], v[140:143]\n"
    "v_mfma_f32_16x16x32_bf16 v[148:151], a[92:95], v[220:223], v[148:151]\n"
    "s_add_u32 s59, s49, 98304\n"
    "buffer_load_dwordx4 v[12:15], v233, s[32:35], s59 offen\n"
    "s_add_u32 s70, s70, 28672\n"
    "buffer_load_dwordx4 v[220:223], v113, s[12:15], s70 offen offset:512 sc1\n"
    "v_mfma_f32_16x16x32_bf16 v[128:131], a[80:83], v[216:219], v[128:131]\n"
    "v_mfma_f32_16x16x32_bf16 v[136:139], a[84:87], v[216:219], v[136:139]\n"
    "v_mfma_f32_16x16x32_bf16 v[144:147], a[88:91], v[216:219], v[144:147]\n"
    "s_add_u32 s59, s49, 131072\n"
    "buffer_load_dwordx4 v[16:19], v233, s[32:35], s59 offen\n"
    "s_waitcnt vmcnt(23)\n"
    "v_cndmask_b32 v228, 0, v188, s[4:5]\n"
    "v_cndmask_b32 v229, 0, v189, s[4:5]\n"
    "v_mfma_f32_16x16x32_bf16 v[152:155], a[92:95], v[216:219], v[152:155]\n"
    "v_cndmask_b32 v230, 0, v190, s[4:5]\n"
    "v_mfma_f32_16x16x32_bf16 v[124:127], a[96:99], v[224:227], v[124:127]\n"
    "s_add_u32 s70, s70, 28672\n"
    "buffer_load_dwordx4 v[216:219], v113, s[12:15], s70 offen offset:512 sc1\n"
    "v_mfma_f32_16x16x32_bf16 v[132:135], a[100:103], v[224:227], v[132:135]\n"
    "s_add_u32 s59, s49, 163840\n"
    "buffer_load_dwordx4 v[20:23], v233, s[32:35], s59 offen\n"
    "v_cndmask_b32 v231, 0, v191, s[4:5]\n"
    "v_mfma_f32_16x16x32_bf16 v[140:143], a[104:107], v[224:227], v[140:143]\n"
    "v_mfma_f32_16x16x32_bf16 v[148:151], a[108:111], v[224:227], v[148:151]\n"
    "buffer_load_dwordx4 v[224:227], v114, s[12:15], s70 offen offset:768 sc1\n"
    "v_mfma_f32_16x16x32_bf16 v[128:131], a[96:99], v[228:231], v[128:131]\n"
    "s_add_u32 s59, s49, 196608\n"
    "buffer_load_dwordx4 v[24:27], v233, s[32:35], s59 offen\n"
    "v_cndmask_b32 v188, v188, 0, s[4:5]\n"
    "v_cndmask_b32 v189, v189, 0, s[4:5]\n"
    "v_mfma_f32_16x16x32_bf16 v[136:139], a[100:103], v[228:231], v[136:139]\n"
    "v_cndmask_b32 v190, v190, 0, s[4:5]\n"
    "v_cndmask_b32 v191, v191, 0, s[4:5]\n"
    "v_mfma_f32_16x16x32_bf16 v[144:147], a[104:107], v[228:231], v[144:147]\n"
    "v_mfma_f32_16x16x32_bf16 v[152:155], a[108:111], v[228:231], v[152:155]\n"
    "s_add_u32 s59, s49, 229376\n"
    "buffer_load_dwordx4 v[28:31], v233, s[32:35], s59 offen\n"
    "s_waitcnt vmcnt(22)\n"
    "v_mfma_f32_16x16x32_bf16 v[124:127], a[112:115], v[196:199], v[124:127]\n"
    "ds_write_b128 v234, a[160:163] offset:28672\n"
    "v_mfma_f32_16x16x32_bf16 v[132:135], a[116:119], v[196:199], v[132:135]\n"
    "ds_write_b128 v235, a[164:167] offset:29696\n"
    "v_mfma_f32_16x16x32_bf16 v[140:143], a[120:123], v[196:199], v[140:143]\n"
    "ds_write_b128 v234, a[168:171] offset:30720\n"
    "v_mfma_f32_16x16x32_bf16 v[148:151], a[124:127], v[196:199], v[148:151]\n"
    "ds_write_b128 v235, a[172:175] offset:31744\n"
    "s_add_u32 s70, s70, 28672\n"
    "buffer_load_dwordx4 v[196:199], v113, s[12:15], s70 offen offset:512 sc1\n"
    "v_mfma_f32_16x16x32_bf16 v[128:131], a[112:115], v[188:191], v[128:131]\n"
    "ds_read_b64_tr_b16 a[160:161], v115 offset:28672\n"
    "v_mfma_f32_16x16x32_bf16 v[136:139], a[116:119], v[188:191], v[136:139]\n"
    "ds_read_b64_tr_b16 a[162:163], v115 offset:29184\n"
    "v_mfma_f32_16x16x32_bf16 v[144:147], a[120:123], v[188:191], v[144:147]\n"
    "ds_read_b64_tr_b16 a[164:165], v116 offset:28672\n"
    "s_waitcnt vmcnt(19)\n"
    "v_cndmask_b32 v228, 0, v200, s[4:5]\n"
    "v_cndmask_b32 v229, 0, v201, s[4:5]\n"
    "v_mfma_f32_16x16x32_bf16 v[152:155], a[124:127], v[188:191], v[152:155]\n"
    "ds_read_b64_tr_b16 a[166:167], v116 offset:29184\n"
    "v_cndmask_b32 v230, 0, v202, s[4:5]\n"
    "v_mfma_f32_16x16x32_bf16 v[124:127], a[128:131], v[192:195], v[124:127]\n"
    "ds_read_b64_tr_b16 a[168:169], v117 offset:28672\n"
    "s_add_u32 s70, s70, 28672\n"
    "buffer_load_dwordx4 v[188:191], v113, s[12:15], s70 offen offset:512 sc1\n"
    "v_mfma_f32_16x16x32_bf16 v[132:135], a[132:135], v[192:195], v[132:135]\n"
    "ds_read_b64_tr_b16 a[170:171], v117 offset:29184\n"
    "v_cndmask_b32 v231, 0, v203, s[4:5]\n"
    "v_mfma_f32_16x16x32_bf16 v[140:143], a[136:139], v[192:195], v[140:143]\n"
    "ds_read_b64_tr_b16 a[172:173], v118 offset:28672\n"
    "v_mfma_f32_16x16x32_bf16 v[148:151], a[140:143], v[192:195], v[148:151]\n"
    "ds_read_b64_tr_b16 a[174:175], v118 offset:29184\n"
    "buffer_load_dwordx4 v[192:195], v114, s[12:15], s70 offen offset:768 sc1\n"
    "v_mfma_f32_16x16x32_bf16 v[128:131], a[128:131], v[228:231], v[128:131]\n"
    "ds_write_b128 v234, a[176:179] offset:28672\n"
    "v_cndmask_b32 v200, v200, 0, s[4:5]\n"
    "v_cndmask_b32 v201, v201, 0, s[4:5]\n"
    "v_mfma_f32_16x16x32_bf16 v[136:139], a[132:135], v[228:231], v[136:139]\n"
    "ds_write_b128 v235, a[180:183] offset:29696\n"
    "v_cndmask_b32 v202, v202, 0, s[4:5]\n"
    "v_cndmask_b32 v203, v203, 0, s[4:5]\n"
    "v_mfma_f32_16x16x32_bf16 v[144:147], a[136:139], v[228:231], v[144:147]\n"
    "ds_write_b128 v234, a[184:187] offset:30720\n"
    "v_mfma_f32_16x16x32_bf16 v[152:155], a[140:143], v[228:231], v[152:155]\n"
    "ds_write_b128 v235, a[188:191] offset:31744\n"
    "s_waitcnt vmcnt(17)\n"
    "v_mfma_f32_16x16x32_bf16 v[124:127], a[144:147], v[208:211], v[124:127]\n"
    "ds_read_b64_tr_b16 a[176:177], v115 offset:28672\n"
    "v_mfma_f32_16x16x32_bf16 v[132:135], a[148:151], v[208:211], v[132:135]\n"
    "ds_read_b64_tr_b16 a[178:179], v115 offset:29184\n"
    "v_mfma_f32_16x16x32_bf16 v[140:143], a[152:155], v[208:211], v[140:143]\n"
    "ds_read_b64_tr_b16 a[180:181], v116 offset:28672\n"
    "v_mfma_f32_16x16x32_bf16 v[148:151], a[156:159], v[208:211], v[148:151]\n"
    "ds_read_b64_tr_b16 a[182:183], v116 offset:29184\n"
    "s_add_u32 s70, s70, 28672\n"
    "buffer_load_dwordx4 v[208:211], v113, s[12:15], s70 offen offset:512 sc1\n"
    "v_mfma_f32_16x16x32_bf16 v[128:131], a[144:147], v[200:203], v[128:131]\n"
    "ds_read_b64_tr_b16 a[184:185], v117 offset:28672\n"
    "v_mfma_f32_16x16x32_bf16 v[136:139], a[148:151], v[200:203], v[136:139]\n"
    "ds_read_b64_tr_b16 a[186:187], v117 offset:29184\n"
    "v_mfma_f32_16x16x32_bf16 v[144:147], a[152:155], v[200:203], v[144:147]\n"
    "ds_read_b64_tr_b16 a[188:189], v118 offset:28672\n"
    "s_waitcnt vmcnt(14)\n"
    "v_cndmask_b32 v228, 0, v212, s[4:5]\n"
    "v_cndmask_b32 v229, 0, v213, s[4:5]\n"
    "v_mfma_f32_16x16x32_bf16 v[152:155], a[156:159], v[200:203], v[152:155]\n"
    "ds_read_b64_tr_b16 a[190:191], v118 offset:29184\n"
    "v_cndmask_b32 v230, 0, v214, s[4:5]\n"
    "s_waitcnt lgkmcnt(15)\n"
    "v_mfma_f32_16x16x32_bf16 v[124:127], a[160:163], v[204:207], v[124:127]\n"
    "ds_write_b128 v234, a[192:195] offset:28672\n"
    "s_add_u32 s70, s71, 0\n"
    "buffer_load_dwordx4 v[200:203], v113, s[12:15], s70 offen offset:0 sc1\n"
    "v_mfma_f32_16x16x32_bf16 v[132:135], a[164:167], v[204:207], v[132:135]\n"
    "ds_write_b128 v235, a[196:199] offset:29696\n"
    "v_cndmask_b32 v231, 0, v215, s[4:5]\n"
    "s_waitcnt lgkmcnt(15)\n"
    "v_mfma_f32_16x16x32_bf16 v[140:143], a[168:171], v[204:207], v[140:143]\n"
    "ds_write_b128 v234, a[200:203] offset:30720\n"
    "s_waitcnt lgkmcnt(15)\n"
    "v_mfma_f32_16x16x32_bf16 v[148:151], a[172:175], v[204:207], v[148:151]\n"
    "ds_write_b128 v235, a[204:207] offset:31744\n"
    "buffer_load_dwordx4 v[204:207], v113, s[12:15], s70 offen offset:256 sc1\n"
    "v_mfma_f32_16x16x32_bf16 v[128:131], a[160:163], v[228:231], v[128:131]\n"
    "ds_read_b64_tr_b16 a[192:193], v115 offset:28672\n"
    "v_cndmask_b32 v212, v212, 0, s[4:5]\n"
    "v_cndmask_b32 v213, v213, 0, s[4:5]\n"
    "v_mfma_f32_16x16x32_bf16 v[136:139], a[164:167], v[228:231], v[136:139]\n"
    "ds_read_b64_tr_b16 a[194:195], v115 offset:29184\n"
    "v_cndmask_b32 v214, v214, 0, s[4:5]\n"
    "v_cndmask_b32 v215, v215, 0, s[4:5]\n"
    "v_mfma_f32_16x16x32_bf16 v[144:147], a[168:171], v[228:231], v[144:147]\n"
    "ds_read_b64_tr_b16 a[196:197], v116 offset:28672\n"
    "v_mfma_f32_16x16x32_bf16 v[152:155], a[172:175], v[228:231], v[152:155]\n"
    "ds_read_b64_tr_b16 a[198:199], v116 offset:29184\n"
    "s_waitcnt vmcnt(12) lgkmcnt(14)\n"
    "v_mfma_f32_16x16x32_bf16 v[124:127], a[176:179], v[220:223], v[124:127]\n"
    "ds_read_b64_tr_b16 a[200:201], v117 offset:28672\n"
    "s_waitcnt lgkmcnt(13)\n"
    "v_mfma_f32_16x16x32_bf16 v[132:135], a[180:183], v[220:223], v[132:135]\n"
    "ds_read_b64_tr_b16 a[202:203], v117 offset:29184\n"
    "s_waitcnt lgkmcnt(12)\n"
    "v_mfma_f32_16x16x32_bf16 v[140:143], a[184:187], v[220:223], v[140:143]\n"
    "ds_read_b64_tr_b16 a[204:205], v118 offset:28672\n"
    "v_lshlrev_b32 v123, 16, v123\n"
    "s_waitcnt lgkmcnt(11)\n"
    "v_mfma_f32_16x16x32_bf16 v[148:151], a[188:191], v[220:223], v[148:151]\n"
    "ds_read_b64_tr_b16 a[206:207], v118 offset:29184\n"
    "s_add_u32 s70, s70, 28672\n"
    "buffer_load_dwordx4 v[220:223], v113, s[12:15], s70 offen offset:0 sc1\n"
    "v_mfma_f32_16x16x32_bf16 v[128:131], a[176:179], v[212:215], v[128:131]\n"
    "ds_write_b128 v234, a[208:211] offset:28672\n"
    "v_mfma_f32_16x16x32_bf16 v[136:139], a[180:183], v[212:215], v[136:139]\n"
    "ds_write_b128 v235, a[212:215] offset:29696\n"
    "v_mfma_f32_16x16x32_bf16 v[144:147], a[184:187], v[212:215], v[144:147]\n"
    "ds_write_b128 v234, a[216:219] offset:30720\n"
    "s_waitcnt vmcnt(9)\n"
    "v_cndmask_b32 v228, 0, v224, s[4:5]\n"
    "v_cndmask_b32 v229, 0, v225, s[4:5]\n"
    "v_mfma_f32_16x16x32_bf16 v[152:155], a[188:191], v[212:215], v[152:155]\n"
    "ds_write_b128 v235, a[220:223] offset:31744\n"
    "buffer_load_dwordx4 v[212:215], v113, s[12:15], s70 offen offset:256 sc1\n"
    "s_waitcnt lgkmcnt(10)\n"
    "v_mfma_f32_16x16x32_bf16 v[124:127], a[192:195], v[216:219], v[124:127]\n"
    "ds_read_b64_tr_b16 a[208:209], v115 offset:28672\n"
    "v_cndmask_b32 v230, 0, v226, s[4:5]\n"
    "v_cndmask_b32 v231, 0, v227, s[4:5]\n"
    "s_waitcnt lgkmcnt(9)\n"
    "v_mfma_f32_16x16x32_bf16 v[132:135], a[196:199], v[216:219], v[132:135]\n"
    "ds_read_b64_tr_b16 a[210:211], v115 offset:29184\n"
    "s_waitcnt lgkmcnt(8)\n"
    "v_mfma_f32_16x16x32_bf16 v[140:143], a[200:203], v[216:219], v[140:143]\n"
    "ds_read_b64_tr_b16 a[212:213], v116 offset:28672\n"
    "s_waitcnt lgkmcnt(7)\n"
    "v_mfma_f32_16x16x32_bf16 v[148:151], a[204:207], v[216:219], v[148:151]\n"
    "ds_read_b64_tr_b16 a[214:215], v116 offset:29184\n"
    "v_mfma_f32_16x16x32_bf16 v[128:131], a[192:195], v[228:231], v[128:131]\n"
    "ds_read_b64_tr_b16 a[216:217], v117 offset:28672\n"
    "s_add_u32 s70, s70, 28672\n"
    "buffer_load_dwordx4 v[216:219], v113, s[12:15], s70 offen offset:0 sc1\n"
    "v_mfma_f32_16x16x32_bf16 v[136:139], a[196:199], v[228:231], v[136:139]\n"
    "ds_read_b64_tr_b16 a[218:219], v117 offset:29184\n"
    "v_cndmask_b32 v224, v224, 0, s[4:5]\n"
    "v_cndmask_b32 v225, v225, 0, s[4:5]\n"
    "v_mfma_f32_16x16x32_bf16 v[144:147], a[200:203], v[228:231], v[144:147]\n"
    "ds_read_b64_tr_b16 a[220:221], v118 offset:28672\n"
    "v_cndmask_b32 v226, v226, 0, s[4:5]\n"
    "v_cndmask_b32 v227, v227, 0, s[4:5]\n"
    "v_mfma_f32_16x16x32_bf16 v[152:155], a[204:207], v[228:231], v[152:155]\n"
    "ds_read_b64_tr_b16 a[222:223], v118 offset:29184\n"
    "s_waitcnt vmcnt(8) lgkmcnt(6)\n"
    "v_mfma_f32_16x16x32_bf16 v[124:127], a[208:211], v[196:199], v[124:127]\n"
    "ds_write_b128 v234, a[224:227] offset:28672\n"
    "s_waitcnt lgkmcnt(5)\n"
    "v_mfma_f32_16x16x32_bf16 v[132:135], a[212:215], v[196:199], v[132:135]\n"
    "ds_write_b128 v235, a[228:231] offset:29696\n"
    "s_waitcnt lgkmcnt(4)\n"
    "v_mfma_f32_16x16x32_bf16 v[140:143], a[216:219], v[196:199], v[140:143]\n"
    "ds_write_b128 v234, a[232:235] offset:30720\n"
    "s_waitcnt lgkmcnt(3)\n"
    "v_mfma_f32_16x16x32_bf16 v[148:151], a[220:223], v[196:199], v[148:151]\n"
    "ds_write_b128 v235, a[236:239] offset:31744\n"
    "buffer_load_dwordx4 v[196:199], v113, s[12:15], s70 offen offset:256 sc1\n"
    "v_mfma_f32_16x16x32_bf16 v[128:131], a[208:211], v[224:227], v[128:131]\n"
    "ds_read_b64_tr_b16 a[224:225], v115 offset:28672\n"
    "v_mfma_f32_16x16x32_bf16 v[136:139], a[212:215], v[224:227], v[136:139]\n"
    "ds_read_b64_tr_b16 a[226:227], v115 offset:29184\n"
    "v_mfma_f32_16x16x32_bf16 v[144:147], a[216:219], v[224:227], v[144:147]\n"
    "ds_read_b64_tr_b16 a[228:229], v116 offset:28672\n"
    "s_waitcnt vmcnt(7)\n"
    "v_cndmask_b32 v228, 0, v192, s[4:5]\n"
    "v_cndmask_b32 v229, 0, v193, s[4:5]\n"
    "v_mfma_f32_16x16x32_bf16 v[152:155], a[220:223], v[224:227], v[152:155]\n"
    "ds_read_b64_tr_b16 a[230:231], v116 offset:29184\n"
    "v_cndmask_b32 v230, 0, v194, s[4:5]\n"
    "ds_read_b64_tr_b16 a[232:233], v117 offset:28672\n"
    "ds_read_b64_tr_b16 a[234:235], v117 offset:29184\n"
    "ds_read_b64_tr_b16 a[236:237], v118 offset:28672\n"
    "ds_read_b64_tr_b16 a[238:239], v118 offset:29184\n"
    "s_waitcnt lgkmcnt(6)\n"
    "v_mfma_f32_16x16x32_bf16 v[124:127], a[224:227], v[188:191], v[124:127]\n"
    "ds_write_b128 v234, a[240:243] offset:28672\n"
    "s_add_u32 s70, s70, 28672\n"
    "buffer_load_dwordx4 v[224:227], v113, s[12:15], s70 offen offset:0 sc1\n"
    "s_waitcnt lgkmcnt(5)\n"
    "v_mfma_f32_16x16x32_bf16 v[132:135], a[228:231], v[188:191], v[132:135]\n"
    "ds_write_b128 v235, a[244:247] offset:29696\n"
    "v_cndmask_b32 v231, 0, v195, s[4:5]\n"
    "s_waitcnt lgkmcnt(4)\n"
    "v_mfma_f32_16x16x32_bf16 v[140:143], a[232:235], v[188:191], v[140:143]\n"
    "ds_write_b128 v234, a[248:251] offset:30720\n"
    "s_waitcnt lgkmcnt(3)\n"
    "v_mfma_f32_16x16x32_bf16 v[148:151], a[236:239], v[188:191], v[148:151]\n"
    "ds_write_b128 v235, a[252:255] offset:31744\n"
    "buffer_load_dwordx4 v[188:191], v113, s[12:15], s70 offen offset:256 sc1\n"
    "v_mfma_f32_16x16x32_bf16 v[128:131], a[224:227], v[228:231], v[128:131]\n"
    "ds_read_b64_tr_b16 a[240:241], v115 offset:28672\n"
    "v_cndmask_b32 v192, v192, 0, s[4:5]\n"
    "v_cndmask_b32 v193, v193, 0, s[4:5]\n"
    "v_mfma_f32_16x16x32_bf16 v[136:139], a[228:231], v[228:231], v[136:139]\n"
    "ds_read_b64_tr_b16 a[242:243], v115 offset:29184\n"
    "v_cndmask_b32 v194, v194, 0, s[4:5]\n"
    "v_cndmask_b32 v195, v195, 0, s[4:5]\n"
    "v_mfma_f32_16x16x32_bf16 v[144:147], a[232:235], v[228:231], v[144:147]\n"
    "ds_read_b64_tr_b16 a[244:245], v116 offset:28672\n"
    "v_mfma_f32_16x16x32_bf16 v[152:155], a[236:239], v[228:231], v[152:155]\n"
    "ds_read_b64_tr_b16 a[246:247], v116 offset:29184\n"
    "ds_read_b64_tr_b16 a[248:249], v117 offset:28672\n"
    "ds_read_b64_tr_b16 a[250:251], v117 offset:29184\n"
    "ds_read_b64_tr_b16 a[252:253], v118 offset:28672\n"
    "ds_read_b64_tr_b16 a[254:255], v118 offset:29184\n"
    "s_waitcnt vmcnt(8) lgkmcnt(6)\n"
    "v_mfma_f32_16x16x32_bf16 v[124:127], a[240:243], v[208:211], v[124:127]\n"
    "s_waitcnt lgkmcnt(4)\n"
    "v_mfma_f32_16x16x32_bf16 v[132:135], a[244:247], v[208:211], v[132:135]\n"
    "s_waitcnt lgkmcnt(2)\n"
    "v_mfma_f32_16x16x32_bf16 v[140:143], a[248:251], v[208:211], v[140:143]\n"
    "s_waitcnt lgkmcnt(0)\n"
    "v_mfma_f32_16x16x32_bf16 v[148:151], a[252:255], v[208:211], v[148:151]\n"
    "s_add_u32 s59, s49, 262144\n"
    "buffer_load_dwordx4 v[32:35], v233, s[32:35], s59 offen\n"
    "s_add_u32 s70, s70, 28672\n"
    "buffer_load_dwordx4 v[208:211], v113, s[12:15], s70 offen offset:0 sc1\n"
    "v_mfma_f32_16x16x32_bf16 v[128:131], a[240:243], v[192:195], v[128:131]\n"
    "v_mfma_f32_16x16x32_bf16 v[136:139], a[244:247], v[192:195], v[136:139]\n"
    "v_mfma_f32_16x16x32_bf16 v[144:147], a[248:251], v[192:195], v[144:147]\n"
    "s_add_u32 s59, s49, 294912\n"
    "buffer_load_dwordx4 v[36:39], v233, s[32:35], s59 offen\n"
    "v_mfma_f32_16x16x32_bf16 v[152:155], a[252:255], v[192:195], v[152:155]\n"
    "buffer_load_dwordx4 v[192:195], v113, s[12:15], s70 offen offset:256 sc1\n"
    ".Lmark_p4k_16:\n"
    "s_waitcnt vmcnt(11)\n"
    "v_mfma_f32_16x16x32_bf16 v[156:159], a[0:3], v[200:203], 0\n"
    "v_mfma_f32_16x16x32_bf16 v[164:167], a[4:7], v[200:203], 0\n"
    "v_cmp_u_f32 vcc, v132, v136\n"
    "s_cbranch_vccnz .Lredo_21\n"
    ".Lredook_22:\n"
    ".Lback_18:\n"
    "v_mov_b32 v240, -1\n"
    "v_mov_b32 v241, -1\n"
    "s_add_u32 s59, s49, 327680\n"
    "buffer_load_dwordx4 v[40:43], v233, s[32:35], s59 offen\n"
    "v_mov_b32 v242, -1\n"
    "v_mov_b32 v243, -1\n"
    "s_mul_i32 s74, s54, 28\n"
    "s_add_u32 s74, s74, s55\n"
    "s_cmp_lt_u32 s74, 205\n"
    "s_cselect_b32 s75, s7, 1073741824\n"
    "s_mul_i32 s74, s74, 20480\n"
    "s_add_u32 s74, s74, s75\n"
    "v_lshl_add_u32 v238, v112, 4, s74\n"
    "s_mov_b32 s25, 16777216\n"
    "buffer_store_dwordx4 v[240:243], v238, s[12:15], s25 offen sc1\n"
    "s_mov_b32 s25, 16781312\n"
    "buffer_store_dwordx4 v[240:243], v238, s[12:15], s25 offen sc1\n"
    "s_mov_b32 s25, 16785408\n"
    "buffer_store_dwordx4 v[240:243], v238, s[12:15], s25 offen sc1\n"
    "s_mov_b32 s25, 16789504\n"
    "buffer_store_dwordx4 v[240:243], v238, s[12:15], s25 offen sc1\n"
    "s_mov_b32 s25, 16793600\n"
    "buffer_store_dwordx4 v[240:243], v238, s[12:15], s25 offen sc1\n"
    "ds_write_b128 v119, v[132:135] offset:0\n"
    "ds_write_b128 v119, v[136:139] offset:1024\n"
    "ds_write_b128 v119, v[140:143] offset:2048\n"
    "ds_write_b128 v119, v[144:147] offset:3072\n"
    "ds_write_b128 v119, v[148:151] offset:4096\n"
    "ds_write_b128 v119, v[152:155] offset:5120\n"
    "v_mfma_f32_16x16x32_bf16 v[172:175], a[8:11], v[200:203], 0\n"
    "s_add_u32 s59, s49, 360448\n"
    "buffer_load_dwordx4 v[44:47], v233, s[32:35], s59 offen\n"
    "s_waitcnt lgkmcnt(0)\n"
    "s_barrier\n"
    "ds_read_b128 v[132:135], v120 offset:0\n"
    "ds_read_b128 v[136:139], v120 offset:1024\n"
    "ds_read_b128 v[140:143], v121 offset:2048\n"
    "ds_read_b128 v[144:147], v121 offset:3072\n"
    "ds_read_b128 v[148:151], v122 offset:4096\n"
    "ds_read_b128 v[152:155], v122 offset:5120\n"
    "v_mfma_f32_16x16x32_bf16 v[180:183], a[12:15], v[200:203], 0\n"
    "s_waitcnt vmcnt(17)\n"
    "v_mfma_f32_16x16x32_bf16 v[160:163], a[0:3], v[204:207], 0\n"
    "s_add_u32 s59, s49, 393216\n"
    "buffer_load_dwordx4 v[48:51], v233, s[32:35], s59 offen\n"
    "s_add_u32 s70, s70, 28672\n"
    "buffer_load_dwordx4 v[200:203], v113, s[12:15], s70 offen offset:0 sc1\n"
    "v_mfma_f32_16x16x32_bf16 v[168:171], a[4:7], v[204:207], 0\n"
    "v_mfma_f32_16x16x32_bf16 v[176:179], a[8:11], v[204:207], 0\n"
    "v_mfma_f32_16x16x32_bf16 v[184:187], a[12:15], v[204:207], 0\n"
    "buffer_load_dwordx4 v[204:207], v113, s[12:15], s70 offen offset:256 sc1\n"
    "s_waitcnt vmcnt(19)\n"
    "v_mfma_f32_16x16x32_bf16 v[156:159], a[16:19], v[220:223], v[156:159]\n"
    "s_add_u32 s59, s49, 425984\n"
    "buffer_load_dwordx4 v[52:55], v233, s[32:35], s59 offen\n"
    "s_waitcnt lgkmcnt(3)\n"
    "v_pk_add_f32 v[132:133], v[132:133], v[140:141]\n"
    "v_pk_add_f32 v[134:135], v[134:135], v[142:143]\n"
    "s_waitcnt lgkmcnt(2)\n"
    "v_pk_add_f32 v[136:137], v[136:137], v[144:145]\n"
    "v_pk_add_f32 v[138:139], v[138:139], v[146:147]\n"
    "s_waitcnt lgkmcnt(1)\n"
    "v_pk_add_f32 v[124:125], v[124:125], v[148:149]\n"
    "v_pk_add_f32 v[126:127], v[126:127], v[150:151]\n"
    "s_waitcnt lgkmcnt(0)\n"
    "v_pk_add_f32 v[128:129], v[128:129], v[152:153]\n"
    "v_pk_add_f32 v[130:131], v[130:131], v[154:155]\n"
    "v_pk_add_f32 v[124:125], v[124:125], v[132:133]\n"
    "v_pk_add_f32 v[126:127], v[126:127], v[134:135]\n"
    "v_pk_add_f32 v[128:129], v[128:129], v[136:137]\n"
    "v_pk_add_f32 v[130:131], v[130:131], v[138:139]\n"
    "v_lshlrev_b32 v140, 16, v252\n"
    "v_and_b32 v141, 0xffff0000, v252\n"
    "v_lshlrev_b32 v142, 16, v253\n"
    "v_and_b32 v143, 0xffff0000, v253\n"
    "s_add_u32 s59, s49, 458752\n"
    "buffer_load_dwordx4 v[56:59], v233, s[32:35], s59 offen\n"
    "v_lshlrev_b32 v144, 16, v254\n"
    "v_and_b32 v145, 0xffff0000, v254\n"
    "v_lshlrev_b32 v146, 16, v255\n"
    "v_and_b32 v147, 0xffff0000, v255\n"
    "v_mov_b32 v152, v249\n"
    "v_add_f32_dpp v128, v128, v128 row_ror:8 row_mask:0xf bank_mask:0xf\n"
    "v_add_f32_dpp v129, v129, v129 row_ror:8 row_mask:0xf bank_mask:0xf\n"
    "v_add_f32_dpp v130, v130, v130 row_ror:8 row_mask:0xf bank_mask:0xf\n"
    "v_add_f32_dpp v131, v131, v131 row_ror:8 row_mask:0xf bank_mask:0xf\n"
    "v_permlane16_swap_b32 v124, v128\n"
    "v_permlane16_swap_b32 v125, v129\n"
    "v_permlane16_swap_b32 v126, v130\n"
    "v_permlane16_swap_b32 v127, v131\n"
    "v_pk_add_f32 v[124:125], v[124:125], v[140:141]\n"
    "v_pk_add_f32 v[126:127], v[126:127], v[142:143]\n"
    "v_pk_add_f32 v[128:129], v[128:129], v[144:145]\n"
    "s_add_u32 s59, s49, 491520\n"
    "buffer_load_dwordx4 v[60:63], v233, s[32:35], s59 offen\n"
    "v_pk_add_f32 v[130:131], v[130:131], v[146:147]\n"
    "v_pk_mul_f32 v[132:133], v[124:125], v[124:125]\n"
    "v_pk_mul_f32 v[134:135], v[126:127], v[126:127]\n"
    "v_pk_mul_f32 v[136:137], v[128:129], v[128:129]\n"
    "v_pk_mul_f32 v[138:139], v[130:131], v[130:131]\n"
    "v_pk_fma_f32 v[132:133], v[132:133], s[64:65], s[64:65] op_sel:[0,0,1] op_sel_hi:[1,0,1]\n"
    "v_pk_fma_f32 v[134:135], v[134:135], s[64:65], s[64:65] op_sel:[0,0,1] op_sel_hi:[1,0,1]\n"
    "v_pk_fma_f32 v[136:137], v[136:137], s[64:65], s[64:65] op_sel:[0,0,1] op_sel_hi:[1,0,1]\n"
    "v_pk_fma_f32 v[138:139], v[138:139], s[64:65], s[64:65] op_sel:[0,0,1] op_sel_hi:[1,0,1]\n"
    "v_pk_mul_f32 v[132:133], v[132:133], v[124:125]\n"
    "v_pk_mul_f32 v[134:135], v[134:135], v[126:127]\n"
    "v_pk_mul_f32 v[136:137], v[136:137], v[128:129]\n"
    "v_pk_mul_f32 v[138:139], v[138:139], v[130:131]\n"
    "v_exp_f32 v132, v132\n"
    "v_exp_f32 v133, v133\n"
    "s_add_u32 s59, s49, 524288\n"
    "buffer_load_dwordx4 v[64:67], v233, s[32:35], s59 offen\n"
    "v_exp_f32 v134, v134\n"
    "v_exp_f32 v135, v135\n"
    "v_exp_f32 v136, v136\n"
    "v_exp_f32 v137, v137\n"
    "v_exp_f32 v138, v138\n"
    "v_exp_f32 v139, v139\n"
    "v_pk_add_f32 v[132:133], v[132:133], 1.0 op_sel_hi:[1,0]\n"
    "v_pk_add_f32 v[134:135], v[134:135], 1.0 op_sel_hi:[1,0]\n"
    "v_pk_add_f32 v[136:137], v[136:137], 1.0 op_sel_hi:[1,0]\n"
    "v_pk_add_f32 v[138:139], v[138:139], 1.0 op_sel_hi:[1,0]\n"
    "s_add_u32 s59, s49, 557056\n"
    "buffer_load_dwordx4 v[68:71], v233, s[32:35], s59 offen\n"
    "v_rcp_f32 v132, v132\n"
    "v_rcp_f32 v133, v133\n"
    "v_rcp_f32 v134, v134\n"
    "v_rcp_f32 v135, v135\n"
    "v_rcp_f32 v136, v136\n"
    "v_rcp_f32 v137, v137\n"
    "v_rcp_f32 v138, v138\n"
    "v_rcp_f32 v139, v139\n"
    "s_add_u32 s59, s49, 589824\n"
    "buffer_load_dwordx4 v[72:75], v233, s[32:35], s59 offen\n"
    "v_pk_mul_f32 v[132:133], v[132:133], v[124:125]\n"
    "v_pk_mul_f32 v[134:135], v[134:135], v[126:127]\n"
    "v_pk_mul_f32 v[136:137], v[136:137], v[128:129]\n"
    "v_pk_mul_f32 v[138:139], v[138:139], v[130:131]\n"
    "v_cvt_pk_bf16_f32 v148, v132, v133\n"
    "v_cvt_pk_bf16_f32 v149, v134, v135\n"
    "v_cvt_pk_bf16_f32 v150, v136, v137\n"
    "v_cvt_pk_bf16_f32 v151, v138, v139\n"
    "buffer_store_dwordx4 v[148:151], v152, s[12:15], 0 offen offset:512 sc1\n"
    "v_mov_b32 v124, v123\n"
    "v_mov_b32 v125, v123\n"
    "v_mov_b32 v126, v123\n"
    "v_mov_b32 v127, v123\n"
    "v_mul_f32 v128, 0.5, v123\n"
    "v_mul_f32 v129, 0.5, v123\n"
    "v_mfma_f32_16x16x32_bf16 v[164:167], a[20:23], v[220:223], v[164:167]\n"
    "s_add_u32 s59, s49, 622592\n"
    "buffer_load_dwordx4 v[76:79], v233, s[32:35], s59 offen\n"
    "v_mul_f32 v130, 0.5, v123\n"
    "v_mul_f32 v131, 0.5, v123\n"
    "s_waitcnt lgkmcnt(0)\n"
    "s_barrier\n"
    "v_mfma_f32_16x16x32_bf16 v[172:175], a[24:27], v[220:223], v[172:175]\n"
    "v_mfma_f32_16x16x32_bf16 v[180:183], a[28:31], v[220:223], v[180:183]\n"
    "s_waitcnt vmcnt(26)\n"
    "v_mfma_f32_16x16x32_bf16 v[160:163], a[16:19], v[212:215], v[160:163]\n"
    "s_add_u32 s59, s49, 655360\n"
    "buffer_load_dwordx4 v[80:83], v233, s[32:35], s59 offen\n"
    "s_add_u32 s70, s70, 28672\n"
    "buffer_load_dwordx4 v[220:223], v113, s[12:15], s70 offen offset:0 sc1\n"
    "v_mfma_f32_16x16x32_bf16 v[168:171], a[20:23], v[212:215], v[168:171]\n"
    "v_mfma_f32_16x16x32_bf16 v[176:179], a[24:27], v[212:215], v[176:179]\n"
    "v_mfma_f32_16x16x32_bf16 v[184:187], a[28:31], v[212:215], v[184:187]\n"
    "buffer_load_dwordx4 v[212:215], v113, s[12:15], s70 offen offset:256 sc1\n"
    "s_waitcnt vmcnt(28)\n"
    "v_mfma_f32_16x16x32_bf16 v[156:159], a[32:35], v[216:219], v[156:159]\n"
    "s_add_u32 s59, s49, 688128\n"
    "buffer_load_dwordx4 v[84:87], v233, s[32:35], s59 offen\n"
    "v_mfma_f32_16x16x32_bf16 v[164:167], a[36:39], v[216:219], v[164:167]\n"
    "v_mfma_f32_16x16x32_bf16 v[172:175], a[40:43], v[216:219], v[172:175]\n"
    "v_mfma_f32_16x16x32_bf16 v[180:183], a[44:47], v[216:219], v[180:183]\n"
    "s_waitcnt vmcnt(28)\n"
    "v_mfma_f32_16x16x32_bf16 v[160:163], a[32:35], v[196:199], v[160:163]\n"
    "s_add_u32 s59, s49, 720896\n"
    "buffer_load_dwordx4 v[88:91], v233, s[32:35], s59 offen\n"
    "s_add_u32 s70, s70, 28672\n"
    "buffer_load_dwordx4 v[216:219], v113, s[12:15], s70 offen offset:0 sc1\n"
    "v_mfma_f32_16x16x32_bf16 v[168:171], a[36:39], v[196:199], v[168:171]\n"
    "v_mfma_f32_16x16x32_bf16 v[176:179], a[40:43], v[196:199], v[176:179]\n"
    "v_mfma_f32_16x16x32_bf16 v[184:187], a[44:47], v[196:199], v[184:187]\n"
    "buffer_load_dwordx4 v[196:199], v113, s[12:15], s70 offen offset:256 sc1\n"
    "s_waitcnt vmcnt(30)\n"
    "v_mfma_f32_16x16x32_bf16 v[156:159], a[48:51], v[224:227], v[156:159]\n"
    "s_add_u32 s59, s49, 753664\n"
    "buffer_load_dwordx4 v[92:95], v233, s[32:35], s59 offen\n"
    "v_mfma_f32_16x16x32_bf16 v[164:167], a[52:55], v[224:227], v[164:167]\n"
    "v_mfma_f32_16x16x32_bf16 v[172:175], a[56:59], v[224:227], v[172:175]\n"
    "v_mfma_f32_16x16x32_bf16 v[180:183], a[60:63], v[224:227], v[180:183]\n"
    "s_waitcnt vmcnt(30)\n"
    "v_mfma_f32_16x16x32_bf16 v[160:163], a[48:51], v[188:191], v[160:163]\n"
    "s_add_u32 m0, s11, 0\n"
    "s_add_u32 s59, s49, 786432\n"
    "buffer_load_dwordx4 v236, s[32:35], s59 offen lds\n"
    "s_add_u32 s70, s70, 28672\n"
    "buffer_load_dwordx4 v[224:227], v113, s[12:15], s70 offen offset:0 sc1\n"
    "v_mfma_f32_16x16x32_bf16 v[168:171], a[52:55], v[188:191], v[168:171]\n"
    "v_mfma_f32_16x16x32_bf16 v[176:179], a[56:59], v[188:191], v[176:179]\n"
    "v_mfma_f32_16x16x32_bf16 v[184:187], a[60:63], v[188:191], v[184:187]\n"
    "buffer_load_dwordx4 v[188:191], v113, s[12:15], s70 offen offset:256 sc1\n"
    "s_waitcnt vmcnt(31)\n"
    "v_mfma_f32_16x16x32_bf16 v[156:159], a[64:67], v[208:211], v[156:159]\n"
    "ds_write_b128 v234, v[0:3] offset:28672\n"
    "v_mfma_f32_16x16x32_bf16 v[164:167], a[68:71], v[208:211], v[164:167]\n"
    "ds_write_b128 v235, v[4:7] offset:29696\n"
    "v_mfma_f32_16x16x32_bf16 v[172:175], a[72:75], v[208:211], v[172:175]\n"
    "ds_write_b128 v234, v[8:11] offset:30720\n"
    "v_mfma_f32_16x16x32_bf16 v[180:183], a[76:79], v[208:211], v[180:183]\n"
    "ds_write_b128 v235, v[12:15] offset:31744\n"
    "s_waitcnt vmcnt(29)\n"
    "v_mfma_f32_16x16x32_bf16 v[160:163], a[64:67], v[192:195], v[160:163]\n"
    "ds_read_b64_tr_b16 v[0:1], v115 offset:28672\n"
    "s_add_u32 s70, s70, 28672\n"
    "buffer_load_dwordx4 v[208:211], v113, s[12:15], s70 offen offset:0 sc1\n"
    "v_mfma_f32_16x16x32_bf16 v[168:171], a[68:71], v[192:195], v[168:171]\n"
    "ds_read_b64_tr_b16 v[2:3], v115 offset:29184\n"
    "v_mfma_f32_16x16x32_bf16 v[176:179], a[72:75], v[192:195], v[176:179]\n"
    "ds_read_b64_tr_b16 v[4:5], v116 offset:28672\n"
    "v_mfma_f32_16x16x32_bf16 v[184:187], a[76:79], v[192:195], v[184:187]\n"
    "ds_read_b64_tr_b16 v[6:7], v116 offset:29184\n"
    "buffer_load_dwordx4 v[192:195], v113, s[12:15], s70 offen offset:256 sc1\n"
    "s_waitcnt vmcnt(22)\n"
    "v_mfma_f32_16x16x32_bf16 v[156:159], a[80:83], v[200:203], v[156:159]\n"
    "ds_read_b64_tr_b16 v[8:9], v117 offset:28672\n"
    "v_mfma_f32_16x16x32_bf16 v[164:167], a[84:87], v[200:203], v[164:167]\n"
    "ds_read_b64_tr_b16 v[10:11], v117 offset:29184\n"
    "v_mfma_f32_16x16x32_bf16 v[172:175], a[88:91], v[200:203], v[172:175]\n"
    "ds_read_b64_tr_b16 v[12:13], v118 offset:28672\n"
    "v_mfma_f32_16x16x32_bf16 v[180:183], a[92:95], v[200:203], v[180:183]\n"
    "ds_read_b64_tr_b16 v[14:15], v118 offset:29184\n"
    "s_waitcnt vmcnt(21)\n"
    "v_mfma_f32_16x16x32_bf16 v[160:163], a[80:83], v[204:207], v[160:163]\n"
    "ds_write_b128 v234, v[16:19] offset:28672\n"
    "s_add_u32 s70, s70, 28672\n"
    "buffer_load_dwordx4 v[200:203], v113, s[12:15], s70 offen offset:0 sc1\n"
    "v_mfma_f32_16x16x32_bf16 v[168:171], a[84:87], v[204:207], v[168:171]\n"
    "ds_write_b128 v235, v[20:23] offset:29696\n"
    "v_mfma_f32_16x16x32_bf16 v[176:179], a[88:91], v[204:207], v[176:179]\n"
    "ds_write_b128 v234, v[24:27] offset:30720\n"
    "v_mfma_f32_16x16x32_bf16 v[184:187], a[92:95], v[204:207], v[184:187]\n"
    "ds_write_b128 v235, v[28:31] offset:31744\n"
    "buffer_load_dwordx4 v[204:207], v113, s[12:15], s70 offen offset:256 sc1\n"
    "s_waitcnt vmcnt(13)\n"
    "v_mfma_f32_16x16x32_bf16 v[156:159], a[96:99], v[220:223], v[156:159]\n"
    "ds_read_b64_tr_b16 v[16:17], v115 offset:28672\n"
    "v_mfma_f32_16x16x32_bf16 v[164:167], a[100:103], v[220:223], v[164:167]\n"
    "ds_read_b64_tr_b16 v[18:19], v115 offset:29184\n"
    "v_mfma_f32_16x16x32_bf16 v[172:175], a[104:107], v[220:223], v[172:175]\n"
    "ds_read_b64_tr_b16 v[20:21], v116 offset:28672\n"
    "v_mfma_f32_16x16x32_bf16 v[180:183], a[108:111], v[220:223], v[180:183]\n"
    "ds_read_b64_tr_b16 v[22:23], v116 offset:29184\n"
    "s_waitcnt vmcnt(12)\n"
    "v_mfma_f32_16x16x32_bf16 v[160:163], a[96:99], v[212:215], v[160:163]\n"
    "ds_read_b64_tr_b16 v[24:25], v117 offset:28672\n"
    "s_add_u32 s70, s70, 28672\n"
    "buffer_load_dwordx4 v[220:223], v113, s[12:15], s70 offen offset:0 sc1\n"
    "v_mfma_f32_16x16x32_bf16 v[168:171], a[100:103], v[212:215], v[168:171]\n"
    "ds_read_b64_tr_b16 v[26:27], v117 offset:29184\n"
    "v_mfma_f32_16x16x32_bf16 v[176:179], a[104:107], v[212:215], v[176:179]\n"
    "ds_read_b64_tr_b16 v[28:29], v118 offset:28672\n"
    "v_mfma_f32_16x16x32_bf16 v[184:187], a[108:111], v[212:215], v[184:187]\n"
    "ds_read_b64_tr_b16 v[30:31], v118 offset:29184\n"
    "buffer_load_dwordx4 v[212:215], v113, s[12:15], s70 offen offset:256 sc1\n"
    "s_waitcnt vmcnt(11)\n"
    "v_mfma_f32_16x16x32_bf16 v[156:159], a[112:115], v[216:219], v[156:159]\n"
    "ds_write_b128 v234, v[32:35] offset:28672\n"
    "v_mfma_f32_16x16x32_bf16 v[164:167], a[116:119], v[216:219], v[164:167]\n"
    "ds_write_b128 v235, v[36:39] offset:29696\n"
    "v_mfma_f32_16x16x32_bf16 v[172:175], a[120:123], v[216:219], v[172:175]\n"
    "ds_write_b128 v234, v[40:43] offset:30720\n"
    "v_mfma_f32_16x16x32_bf16 v[180:183], a[124:127], v[216:219], v[180:183]\n"
    "ds_write_b128 v235, v[44:47] offset:31744\n"
    "s_waitcnt vmcnt(10)\n"
    "v_mfma_f32_16x16x32_bf16 v[160:163], a[112:115], v[196:199], v[160:163]\n"
    "ds_read_b64_tr_b16 v[32:33], v115 offset:28672\n"
    "s_add_u32 s70, s70, 28672\n"
    "buffer_load_dwordx4 v[216:219], v113, s[12:15], s70 offen offset:0 sc1\n"
    "v_mfma_f32_16x16x32_bf16 v[168:171], a[116:119], v[196:199], v[168:171]\n"
    "ds_read_b64_tr_b16 v[34:35], v115 offset:29184\n"
    "v_mfma_f32_16x16x32_bf16 v[176:179], a[120:123], v[196:199], v[176:179]\n"
    "ds_read_b64_tr_b16 v[36:37], v116 offset:28672\n"
    "v_mfma_f32_16x16x32_bf16 v[184:187], a[124:127], v[196:199], v[184:187]\n"
    "ds_read_b64_tr_b16 v[38:39], v116 offset:29184\n"
    "buffer_load_dwordx4 v[196:199], v113, s[12:15], s70 offen offset:256 sc1\n"
    "s_waitcnt vmcnt(9)\n"
    "v_mfma_f32_16x16x32_bf16 v[156:159], a[128:131], v[224:227], v[156:159]\n"
    "ds_read_b64_tr_b16 v[40:41], v117 offset:28672\n"
    "v_mfma_f32_16x16x32_bf16 v[164:167], a[132:135], v[224:227], v[164:167]\n"
    "ds_read_b64_tr_b16 v[42:43], v117 offset:29184\n"
    "v_mfma_f32_16x16x32_bf16 v[172:175], a[136:139], v[224:227], v[172:175]\n"
    "ds_read_b64_tr_b16 v[44:45], v118 offset:28672\n"
    "v_mfma_f32_16x16x32_bf16 v[180:183], a[140:143], v[224:227], v[180:183]\n"
    "ds_read_b64_tr_b16 v[46:47], v118 offset:29184\n"
    "s_waitcnt vmcnt(8)\n"
    "v_mfma_f32_16x16x32_bf16 v[160:163], a[128:131], v[188:191], v[160:163]\n"
    "ds_write_b128 v234, v[48:51] offset:28672\n"
    "s_add_u32 s70, s70, 28672\n"
    "buffer_load_dwordx4 v[224:227], v113, s[12:15], s70 offen offset:0 sc1\n"
    "v_mfma_f32_16x16x32_bf16 v[168:171], a[132:135], v[188:191], v[168:171]\n"
    "ds_write_b128 v235, v[52:55] offset:29696\n"
    "v_mfma_f32_16x16x32_bf16 v[176:179], a[136:139], v[188:191], v[176:179]\n"
    "ds_write_b128 v234, v[56:59] offset:30720\n"
    "v_mfma_f32_16x16x32_bf16 v[184:187], a[140:143], v[188:191], v[184:187]\n"
    "ds_write_b128 v235, v[60:63] offset:31744\n"
    "buffer_load_dwordx4 v[188:191], v113, s[12:15], s70 offen offset:256 sc1\n"
    "s_waitcnt vmcnt(9)\n"
    "v_mfma_f32_16x16x32_bf16 v[156:159], a[144:147], v[208:211], v[156:159]\n"
    "ds_read_b64_tr_b16 v[48:49], v115 offset:28672\n"
    "v_mfma_f32_16x16x32_bf16 v[164:167], a[148:151], v[208:211], v[164:167]\n"
    "ds_read_b64_tr_b16 v[50:51], v115 offset:29184\n"
    "v_mfma_f32_16x16x32_bf16 v[172:175], a[152:155], v[208:211], v[172:175]\n"
    "ds_read_b64_tr_b16 v[52:53], v116 offset:28672\n"
    "v_mfma_f32_16x16x32_bf16 v[180:183], a[156:159], v[208:211], v[180:183]\n"
    "ds_read_b64_tr_b16 v[54:55], v116 offset:29184\n"
    "s_waitcnt vmcnt(8)\n"
    "v_mfma_f32_16x16x32_bf16 v[160:163], a[144:147], v[192:195], v[160:163]\n"
    "ds_read_b64_tr_b16 v[56:57], v117 offset:28672\n"
    "s_add_u32 s70, s70, 28672\n"
    "buffer_load_dwordx4 v[208:211], v113, s[12:15], s70 offen offset:0 sc1\n"
    "v_mfma_f32_16x16x32_bf16 v[168:171], a[148:151], v[192:195], v[168:171]\n"
    "ds_read_b64_tr_b16 v[58:59], v117 offset:29184\n"
    "v_mfma_f32_16x16x32_bf16 v[176:179], a[152:155], v[192:195], v[176:179]\n"
    "ds_read_b64_tr_b16 v[60:61], v118 offset:28672\n"
    "v_mfma_f32_16x16x32_bf16 v[184:187], a[156:159], v[192:195], v[184:187]\n"
    "ds_read_b64_tr_b16 v[62:63], v118 offset:29184\n"
    "buffer_load_dwordx4 v[192:195], v113, s[12:15], s70 offen offset:256 sc1\n"
    "s_waitcnt vmcnt(9)\n"
    "v_mfma_f32_16x16x32_bf16 v[156:159], a[160:163], v[200:203], v[156:159]\n"
    "ds_write_b128 v234, v[64:67] offset:28672\n"
    "v_mfma_f32_16x16x32_bf16 v[164:167], a[164:167], v[200:203], v[164:167]\n"
    "ds_write_b128 v235, v[68:71] offset:29696\n"
    "v_mfma_f32_16x16x32_bf16 v[172:175], a[168:171], v[200:203], v[172:175]\n"
    "ds_write_b128 v234, v[72:75] offset:30720\n"
    "v_mfma_f32_16x16x32_bf16 v[180:183], a[172:175], v[200:203], v[180:183]\n"
    "ds_write_b128 v235, v[76:79] offset:31744\n"
    "s_waitcnt vmcnt(8)\n"
    "v_mfma_f32_16x16x32_bf16 v[160:163], a[160:163], v[204:207], v[160:163]\n"
    "ds_read_b64_tr_b16 v[64:65], v115 offset:28672\n"
    "s_add_u32 s70, s70, 28672\n"
    "buffer_load_dwordx4 v[200:203], v113, s[12:15], s70 offen offset:0 sc1\n"
    "v_mfma_f32_16x16x32_bf16 v[168:171], a[164:167], v[204:207], v[168:171]\n"
    "ds_read_b64_tr_b16 v[66:67], v115 offset:29184\n"
    "v_mfma_f32_16x16x32_bf16 v[176:179], a[168:171], v[204:207], v[176:179]\n"
    "ds_read_b64_tr_b16 v[68:69], v116 offset:28672\n"
    "v_mfma_f32_16x16x32_bf16 v[184:187], a[172:175], v[204:207], v[184:187]\n"
    "ds_read_b64_tr_b16 v[70:71], v116 offset:29184\n"
    "buffer_load_dwordx4 v[204:207], v113, s[12:15], s70 offen offset:256 sc1\n"
    "s_waitcnt vmcnt(9)\n"
    "v_mfma_f32_16x16x32_bf16 v[156:159], a[176:179], v[220:223], v[156:159]\n"
    "ds_read_b64_tr_b16 v[72:73], v117 offset:28672\n"
    "v_mfma_f32_16x16x32_bf16 v[164:167], a[180:183], v[220:223], v[164:167]\n"
    "ds_read_b64_tr_b16 v[74:75], v117 offset:29184\n"
    "v_mfma_f32_16x16x32_bf16 v[172:175], a[184:187], v[220:223], v[172:175]\n"
    "ds_read_b64_tr_b16 v[76:77], v118 offset:28672\n"
    "v_mfma_f32_16x16x32_bf16 v[180:183], a[188:191], v[220:223], v[180:183]\n"
    "ds_read_b64_tr_b16 v[78:79], v118 offset:29184\n"
    "s_waitcnt vmcnt(8)\n"
    "v_mfma_f32_16x16x32_bf16 v[160:163], a[176:179], v[212:215], v[160:163]\n"
    "ds_write_b128 v234, v[80:83] offset:28672\n"
    "buffer_load_dword v251, v250, s[12:15], s82 offen sc1\n"
    "s_add_u32 s70, s82, 0\n"
    "buffer_load_dwordx4 v[220:223], v113, s[12:15], s70 offen offset:512 sc1\n"
    "v_mfma_f32_16x16x32_bf16 v[168:171], a[180:183], v[212:215], v[168:171]\n"
    "ds_write_b128 v235, v[84:87] offset:29696\n"
    "v_mfma_f32_16x16x32_bf16 v[176:179], a[184:187], v[212:215], v[176:179]\n"
    "ds_write_b128 v234, v[88:91] offset:30720\n"
    "v_mfma_f32_16x16x32_bf16 v[184:187], a[188:191], v[212:215], v[184:187]\n"
    "ds_write_b128 v235, v[92:95] offset:31744\n"
    "buffer_load_dwordx4 v[212:215], v114, s[12:15], s70 offen offset:768 sc1\n"
    "s_waitcnt vmcnt(10)\n"
    "v_mfma_f32_16x16x32_bf16 v[156:159], a[192:195], v[216:219], v[156:159]\n"
    "ds_read_b64_tr_b16 v[80:81], v115 offset:28672\n"
    "v_mfma_f32_16x16x32_bf16 v[164:167], a[196:199], v[216:219], v[164:167]\n"
    "ds_read_b64_tr_b16 v[82:83], v115 offset:29184\n"
    "v_mfma_f32_16x16x32_bf16 v[172:175], a[200:203], v[216:219], v[172:175]\n"
    "ds_read_b64_tr_b16 v[84:85], v116 offset:28672\n"
    "v_mfma_f32_16x16x32_bf16 v[180:183], a[204:207], v[216:219], v[180:183]\n"
    "ds_read_b64_tr_b16 v[86:87], v116 offset:29184\n"
    "s_waitcnt vmcnt(9)\n"
    "v_mfma_f32_16x16x32_bf16 v[160:163], a[192:195], v[196:199], v[160:163]\n"
    "ds_read_b64_tr_b16 v[88:89], v117 offset:28672\n"
    "s_add_u32 s70, s70, 28672\n"
    "buffer_load_dwordx4 v[216:219], v113, s[12:15], s70 offen offset:512 sc1\n"
    "v_mfma_f32_16x16x32_bf16 v[168:171], a[196:199], v[196:199], v[168:171]\n"
    "ds_read_b64_tr_b16 v[90:91], v117 offset:29184\n"
    "v_mfma_f32_16x16x32_bf16 v[176:179], a[200:203], v[196:199], v[176:179]\n"
    "ds_read_b64_tr_b16 v[92:93], v118 offset:28672\n"
    "v_mfma_f32_16x16x32_bf16 v[184:187], a[204:207], v[196:199], v[184:187]\n"
    "ds_read_b64_tr_b16 v[94:95], v118 offset:29184\n"
    "s_waitcnt vmcnt(9)\n"
    "v_mfma_f32_16x16x32_bf16 v[156:159], a[208:211], v[224:227], v[156:159]\n"
    "s_add_u32 s70, s70, 28672\n"
    "buffer_load_dwordx4 v[196:199], v113, s[12:15], s70 offen offset:512 sc1\n"
    "v_mfma_f32_16x16x32_bf16 v[164:167], a[212:215], v[224:227], v[164:167]\n"
    "v_mfma_f32_16x16x32_bf16 v[172:175], a[216:219], v[224:227], v[172:175]\n"
    "v_mfma_f32_16x16x32_bf16 v[180:183], a[220:223], v[224:227], v[180:183]\n"
    "s_add_u32 m0, s11, 1024\n"
    "s_add_u32 s59, s49, 819200\n"
    "buffer_load_dwordx4 v237, s[32:35], s59 offen lds\n"
    "buffer_load_dwordx4 v[224:227], v114, s[12:15], s70 offen offset:768 sc1\n"
    "s_waitcnt vmcnt(11)\n"
    "v_mfma_f32_16x16x32_bf16 v[160:163], a[208:211], v[188:191], v[160:163]\n"
    "v_mfma_f32_16x16x32_bf16 v[168:171], a[212:215], v[188:191], v[168:171]\n"
    "v_mfma_f32_16x16x32_bf16 v[176:179], a[216:219], v[188:191], v[176:179]\n"
    "v_mfma_f32_16x16x32_bf16 v[184:187], a[220:223], v[188:191], v[184:187]\n"
    "s_add_u32 m0, s11, 2048\n"
    "s_add_u32 s59, s49, 851968\n"
    "buffer_load_dwordx4 v236, s[32:35], s59 offen lds\n"
    "s_waitcnt vmcnt(11)\n"
    "v_mfma_f32_16x16x32_bf16 v[156:159], a[224:227], v[208:211], v[156:159]\n"
    "s_add_u32 s70, s70, 28672\n"
    "buffer_load_dwordx4 v[188:191], v113, s[12:15], s70 offen offset:512 sc1\n"
    "v_mfma_f32_16x16x32_bf16 v[164:167], a[228:231], v[208:211], v[164:167]\n"
    "v_mfma_f32_16x16x32_bf16 v[172:175], a[232:235], v[208:211], v[172:175]\n"
    "v_mfma_f32_16x16x32_bf16 v[180:183], a[236:239], v[208:211], v[180:183]\n"
    "s_add_u32 m0, s11, 3072\n"
    "s_add_u32 s59, s49, 884736\n"
    "buffer_load_dwordx4 v237, s[32:35], s59 offen lds\n"
    "s_waitcnt vmcnt(12)\n"
    "v_mfma_f32_16x16x32_bf16 v[160:163], a[224:227], v[192:195], v[160:163]\n"
    "s_add_u32 s70, s70, 28672\n"
    "buffer_load_dwordx4 v[208:211], v113, s[12:15], s70 offen offset:512 sc1\n"
    "v_mfma_f32_16x16x32_bf16 v[168:171], a[228:231], v[192:195], v[168:171]\n"
    "v_mfma_f32_16x16x32_bf16 v[176:179], a[232:235], v[192:195], v[176:179]\n"
    "v_mfma_f32_16x16x32_bf16 v[184:187], a[236:239], v[192:195], v[184:187]\n"
    "s_add_u32 m0, s11, 4096\n"
    "s_add_u32 s59, s49, 917504\n"
    "buffer_load_dwordx4 v236, s[32:35], s59 offen lds\n"
    "buffer_load_dwordx4 v[192:195], v114, s[12:15], s70 offen offset:768 sc1\n"
    "s_waitcnt vmcnt(14)\n"
    "v_mfma_f32_16x16x32_bf16 v[156:159], a[240:243], v[200:203], v[156:159]\n"
    "v_mfma_f32_16x16x32_bf16 v[164:167], a[244:247], v[200:203], v[164:167]\n"
    "v_mfma_f32_16x16x32_bf16 v[172:175], a[248:251], v[200:203], v[172:175]\n"
    "v_mfma_f32_16x16x32_bf16 v[180:183], a[252:255], v[200:203], v[180:183]\n"
    "s_add_u32 m0, s11, 5120\n"
    "s_add_u32 s59, s49, 950272\n"
    "buffer_load_dwordx4 v237, s[32:35], s59 offen lds\n"
    "s_waitcnt vmcnt(14)\n"
    "v_mfma_f32_16x16x32_bf16 v[160:163], a[240:243], v[204:207], v[160:163]\n"
    "s_add_u32 s70, s70, 28672\n"
    "buffer_load_dwordx4 v[200:203], v113, s[12:15], s70 offen offset:512 sc1\n"
    "v_mfma_f32_16x16x32_bf16 v[168:171], a[244:247], v[204:207], v[168:171]\n"
    "s_waitcnt vmcnt(14)\n"
    "v_cmp_eq_u32 vcc, -1, v251\n"
    "s_cbranch_vccnz .Lgretry_23\n"
    ".Lgok_24:\n"
    "v_mfma_f32_16x16x32_bf16 v[176:179], a[248:251], v[204:207], v[176:179]\n"
    "s_waitcnt vmcnt(12)\n"
    "v_cndmask_b32 v228, 0, v212, s[4:5]\n"
    "s_add_u32 m0, s11, 6144\n"
    "s_add_u32 s59, s49, 983040\n"
    "buffer_load_dwordx4 v236, s[32:35], s59 offen lds\n"
    "v_cndmask_b32 v229, 0, v213, s[4:5]\n"
    "v_mfma_f32_16x16x32_bf16 v[184:187], a[252:255], v[204:207], v[184:187]\n"
    "v_cndmask_b32 v230, 0, v214, s[4:5]\n"
    ".Lmark_p4out_25:\n"
    "s_waitcnt lgkmcnt(15)\n"
    "v_mfma_f32_16x16x32_bf16 v[124:127], v[220:223], v[0:3], v[124:127]\n"
    "v_cndmask_b32 v231, 0, v215, s[4:5]\n"
    "s_nop 1\n"
    "v_mfma_f32_16x16x32_bf16 v[128:131], v[228:231], v[0:3], v[128:131]\n"
    "s_add_u32 m0, s11, 7168\n"
    "s_add_u32 s59, s49, 1015808\n"
    "buffer_load_dwordx4 v237, s[32:35], s59 offen lds\n"
    "s_add_u32 s70, s70, 28672\n"
    "buffer_load_dwordx4 v[204:207], v113, s[12:15], s70 offen offset:512 sc1\n"
    "v_cmp_u_f32 vcc, v164, v168\n"
    "s_cbranch_vccnz .Lredo_30\n"
    ".Lredook_31:\n"
    ".Lback_27:\n"
    "v_mov_b32 v244, -1\n"
    "v_mov_b32 v245, -1\n"
    "v_mov_b32 v246, -1\n"
    "v_mov_b32 v247, -1\n"
    "s_lshl_b32 s74, s53, 13\n"
    "v_lshl_add_u32 v238, v112, 4, s74\n"
    "s_mov_b32 s25, 20971520\n"
    "buffer_store_dwordx4 v[244:247], v238, s[12:15], s25 offen sc1\n"
    "s_mov_b32 s25, 20975616\n"
    "buffer_store_dwordx4 v[244:247], v238, s[12:15], s25 offen sc1\n"
    "ds_write_b128 v119, v[164:167] offset:0\n"
    "ds_write_b128 v119, v[168:171] offset:1024\n"
    "ds_write_b128 v119, v[172:175] offset:2048\n"
    "ds_write_b128 v119, v[176:179] offset:3072\n"
    "ds_write_b128 v119, v[180:183] offset:4096\n"
    "ds_write_b128 v119, v[184:187] offset:5120\n"
    "v_mfma_f32_16x16x32_bf16 v[132:135], v[220:223], v[4:7], 0\n"
    "s_add_u32 m0, s11, 8192\n"
    "s_add_u32 s59, s49, 1048576\n"
    "buffer_load_dwordx4 v236, s[32:35], s59 offen lds\n"
    "v_cndmask_b32 v212, v212, 0, s[4:5]\n"
    "v_cndmask_b32 v213, v213, 0, s[4:5]\n"
    "s_waitcnt lgkmcnt(0)\n"
    "s_barrier\n"
    "ds_read_b128 v[164:167], v120 offset:0\n"
    "ds_read_b128 v[168:171], v120 offset:1024\n"
    "ds_read_b128 v[172:175], v121 offset:2048\n"
    "ds_read_b128 v[176:179], v121 offset:3072\n"
    "ds_read_b128 v[180:183], v122 offset:4096\n"
    "ds_read_b128 v[184:187], v122 offset:5120\n"
    "v_mfma_f32_16x16x32_bf16 v[136:139], v[228:231], v[4:7], 0\n"
    "s_add_u32 m0, s11, 9216\n"
    "s_add_u32 s59, s49, 1081344\n"
    "buffer_load_dwordx4 v237, s[32:35], s59 offen lds\n"
    "v_cndmask_b32 v214, v214, 0, s[4:5]\n"
    "v_cndmask_b32 v215, v215, 0, s[4:5]\n"
    "v_mfma_f32_16x16x32_bf16 v[140:143], v[220:223], v[8:11], 0\n"
    "v_mfma_f32_16x16x32_bf16 v[144:147], v[228:231], v[8:11], 0\n"
    "v_mfma_f32_16x16x32_bf16 v[148:151], v[220:223], v[12:15], 0\n"
    "s_add_u32 m0, s11, 10240\n"
    "s_add_u32 s59, s49, 1114112\n"
    "buffer_load_dwordx4 v236, s[32:35], s59 offen lds\n"
    "buffer_load_dwordx4 v[220:223], v114, s[12:15], s70 offen offset:768 sc1\n"
    "v_mfma_f32_16x16x32_bf16 v[152:155], v[228:231], v[12:15], 0\n"
    "s_waitcnt vmcnt(20)\n"
    "v_mfma_f32_16x16x32_bf16 v[124:127], v[216:219], v[16:19], v[124:127]\n"
    "s_waitcnt lgkmcnt(3)\n"
    "v_pk_add_f32 v[164:165], v[164:165], v[172:173]\n"
    "v_pk_add_f32 v[166:167], v[166:167], v[174:175]\n"
    "s_waitcnt lgkmcnt(2)\n"
    "v_pk_add_f32 v[168:169], v[168:169], v[176:177]\n"
    "s_add_u32 m0, s11, 11264\n"
    "s_add_u32 s59, s49, 1146880\n"
    "buffer_load_dwordx4 v237, s[32:35], s59 offen lds\n"
    "v_pk_add_f32 v[170:171], v[170:171], v[178:179]\n"
    "s_waitcnt lgkmcnt(1)\n"
    "v_pk_add_f32 v[156:157], v[156:157], v[180:181]\n"
    "v_pk_add_f32 v[158:159], v[158:159], v[182:183]\n"
    "s_waitcnt lgkmcnt(0)\n"
    "v_pk_add_f32 v[160:161], v[160:161], v[184:185]\n"
    "v_pk_add_f32 v[162:163], v[162:163], v[186:187]\n"
    "v_pk_add_f32 v[156:157], v[156:157], v[164:165]\n"
    "v_pk_add_f32 v[158:159], v[158:159], v[166:167]\n"
    "v_pk_add_f32 v[160:161], v[160:161], v[168:169]\n"
    "v_pk_add_f32 v[162:163], v[162:163], v[170:171]\n"
    "v_lshlrev_b32 v172, 16, v252\n"
    "v_and_b32 v173, 0xffff0000, v252\n"
    "v_lshlrev_b32 v174, 16, v253\n"
    "s_add_u32 m0, s11, 12288\n"
    "s_add_u32 s59, s49, 1179648\n"
    "buffer_load_dwordx4 v236, s[32:35], s59 offen lds\n"
    "v_and_b32 v175, 0xffff0000, v253\n"
    "v_lshlrev_b32 v176, 16, v254\n"
    "v_and_b32 v177, 0xffff0000, v254\n"
    "v_lshlrev_b32 v178, 16, v255\n"
    "v_and_b32 v179, 0xffff0000, v255\n"
    "v_mov_b32 v184, v248\n"
    "v_add_u32 v185, -384, v250\n"
    "v_permlane16_swap_b32 v156, v160\n"
    "v_permlane16_swap_b32 v157, v161\n"
    "v_permlane16_swap_b32 v158, v162\n"
    "v_permlane16_swap_b32 v159, v163\n"
    "v_pk_add_f32 v[156:157], v[156:157], v[172:173]\n"
    "s_add_u32 m0, s11, 13312\n"
    "s_add_u32 s59, s49, 1212416\n"
    "buffer_load_dwordx4 v237, s[32:35], s59 offen lds\n"
    "v_pk_add_f32 v[158:159], v[158:159], v[174:175]\n"
    "v_pk_add_f32 v[160:161], v[160:161], v[176:177]\n"
    "v_pk_add_f32 v[162:163], v[162:163], v[178:179]\n"
    "v_pk_mul_f32 v[164:165], v[156:157], v[156:157]\n"
    "v_pk_mul_f32 v[166:167], v[158:159], v[158:159]\n"
    "v_pk_mul_f32 v[168:169], v[160:161], v[160:161]\n"
    "v_pk_mul_f32 v[170:171], v[162:163], v[162:163]\n"
    "v_pk_fma_f32 v[164:165], v[164:165], s[64:65], s[64:65] op_sel:[0,0,1] op_sel_hi:[1,0,1]\n"
    "v_pk_fma_f32 v[166:167], v[166:167], s[64:65], s[64:65] op_sel:[0,0,1] op_sel_hi:[1,0,1]\n"
    "v_pk_fma_f32 v[168:169], v[168:169], s[64:65], s[64:65] op_sel:[0,0,1] op_sel_hi:[1,0,1]\n"
    "v_pk_fma_f32 v[170:171], v[170:171], s[64:65], s[64:65] op_sel:[0,0,1] op_sel_hi:[1,0,1]\n"
    "v_pk_mul_f32 v[164:165], v[164:165], v[156:157]\n"
    "s_add_u32 m0, s11, 14336\n"
    "s_add_u32 s59, s49, 1245184\n"
    "buffer_load_dwordx4 v236, s[32:35], s59 offen lds\n"
    "v_pk_mul_f32 v[166:167], v[166:167], v[158:159]\n"
    "v_pk_mul_f32 v[168:169], v[168:169], v[160:161]\n"
    "v_pk_mul_f32 v[170:171], v[170:171], v[162:163]\n"
    "v_exp_f32 v164, v164\n"
    "v_exp_f32 v165, v165\n"
    "v_exp_f32 v166, v166\n"
    "v_exp_f32 v167, v167\n"
    "v_exp_f32 v168, v168\n"
    "s_add_u32 m0, s11, 15360\n"
    "s_add_u32 s59, s49, 1277952\n"
    "buffer_load_dwordx4 v237, s[32:35], s59 offen lds\n"
    "v_exp_f32 v169, v169\n"
    "v_exp_f32 v170, v170\n"
    "v_exp_f32 v171, v171\n"
    "v_pk_add_f32 v[164:165], v[164:165], 1.0 op_sel_hi:[1,0]\n"
    "v_pk_add_f32 v[166:167], v[166:167], 1.0 op_sel_hi:[1,0]\n"
    "v_pk_add_f32 v[168:169], v[168:169], 1.0 op_sel_hi:[1,0]\n"
    "v_pk_add_f32 v[170:171], v[170:171], 1.0 op_sel_hi:[1,0]\n"
    "v_rcp_f32 v164, v164\n"
    "s_add_u32 m0, s11, 16384\n"
    "s_add_u32 s59, s49, 1310720\n"
    "buffer_load_dwordx4 v236, s[32:35], s59 offen lds\n"
    "v_rcp_f32 v165, v165\n"
    "v_rcp_f32 v166, v166\n"
    "v_rcp_f32 v167, v167\n"
    "v_rcp_f32 v168, v168\n"
    "v_rcp_f32 v169, v169\n"
    "v_rcp_f32 v170, v170\n"
    "s_add_u32 m0, s11, 17408\n"
    "s_add_u32 s59, s49, 1343488\n"
    "buffer_load_dwordx4 v237, s[32:35], s59 offen lds\n"
    "v_rcp_f32 v171, v171\n"
    "v_pk_mul_f32 v[164:165], v[164:165], v[156:157]\n"
    "v_pk_mul_f32 v[166:167], v[166:167], v[158:159]\n"
    "v_pk_mul_f32 v[168:169], v[168:169], v[160:161]\n"
    "v_pk_mul_f32 v[170:171], v[170:171], v[162:163]\n"
    "v_cvt_pk_bf16_f32 v180, v164, v165\n"
    "v_cvt_pk_bf16_f32 v181, v166, v167\n"
    "v_cvt_pk_bf16_f32 v182, v168, v169\n"
    "v_cvt_pk_bf16_f32 v183, v170, v171\n"
    "buffer_store_dwordx4 v[180:183], v184, s[12:15], 0 offen offset:0 sc1\n"
    "v_mov_b32 v176, v236\n"
    "s_add_u32 m0, s11, 18432\n"
    "s_add_u32 s59, s49, 1376256\n"
    "buffer_load_dwordx4 v236, s[32:35], s59 offen lds\n"
    "v_mov_b32 v177, v237\n"
    "v_mov_b32 v156, v123\n"
    "v_mov_b32 v157, v123\n"
    "v_mov_b32 v158, v123\n"
    "v_mov_b32 v159, v123\n"
    "v_mov_b32 v160, v123\n"
    "v_mov_b32 v161, v123\n"
    "v_mov_b32 v162, v123\n"
    "v_mfma_f32_16x16x32_bf16 v[128:131], v[212:215], v[16:19], v[128:131]\n"
    "s_add_u32 m0, s11, 19456\n"
    "s_add_u32 s59, s49, 1409024\n"
    "buffer_load_dwordx4 v177, s[32:35], s59 offen lds\n"
    "v_mov_b32 v163, v123\n"
    "s_waitcnt lgkmcnt(0)\n"
    "s_barrier\n"
    "v_mfma_f32_16x16x32_bf16 v[132:135], v[216:219], v[20:23], v[132:135]\n"
    "v_mfma_f32_16x16x32_bf16 v[136:139], v[212:215], v[20:23], v[136:139]\n"
    "v_mfma_f32_16x16x32_bf16 v[140:143], v[216:219], v[24:27], v[140:143]\n"
    "s_add_u32 m0, s11, 20480\n"
    "s_add_u32 s59, s49, 1441792\n"
    "buffer_load_dwordx4 v176, s[32:35], s59 offen lds\n"
    "v_mfma_f32_16x16x32_bf16 v[144:147], v[212:215], v[24:27], v[144:147]\n"
    "v_mfma_f32_16x16x32_bf16 v[148:151], v[216:219], v[28:31], v[148:151]\n"
    "s_add_u32 s70, s70, 28672\n"
    "buffer_load_dwordx4 v[216:219], v113, s[12:15], s70 offen offset:512 sc1\n"
    "v_mfma_f32_16x16x32_bf16 v[152:155], v[212:215], v[28:31], v[152:155]\n"
    "s_add_u32 m0, s11, 21504\n"
    "s_add_u32 s59, s49, 1474560\n"
    "buffer_load_dwordx4 v177, s[32:35], s59 offen lds\n"
    "s_waitcnt vmcnt(30)\n"
    "v_cndmask_b32 v228, 0, v224, s[4:5]\n"
    "v_mfma_f32_16x16x32_bf16 v[124:127], v[196:199], v[32:35], v[124:127]\n"
    "v_cndmask_b32 v229, 0, v225, s[4:5]\n"
    "v_cndmask_b32 v230, 0, v226, s[4:5]\n"
    "v_cndmask_b32 v231, 0, v227, s[4:5]\n"
    "s_nop 1\n"
    "v_mfma_f32_16x16x32_bf16 v[128:131], v[228:231], v[32:35], v[128:131]\n"
    "s_add_u32 m0, s11, 22528\n"
    "s_add_u32 s59, s49, 1507328\n"
    "buffer_load_dwordx4 v176, s[32:35], s59 offen lds\n"
    "s_add_u32 s70, s70, 28672\n"
    "buffer_load_dwordx4 v[212:215], v113, s[12:15], s70 offen offset:512 sc1\n"
    "v_mfma_f32_16x16x32_bf16 v[132:135], v[196:199], v[36:39], v[132:135]\n"
    "v_cndmask_b32 v224, v224, 0, s[4:5]\n"
    "v_cndmask_b32 v225, v225, 0, s[4:5]\n"
    "v_mfma_f32_16x16x32_bf16 v[136:139], v[228:231], v[36:39], v[136:139]\n"
    "s_add_u32 m0, s11, 23552\n"
    "s_add_u32 s59, s49, 1540096\n"
    "buffer_load_dwordx4 v177, s[32:35], s59 offen lds\n"
    "v_cndmask_b32 v226, v226, 0, s[4:5]\n"
    "v_cndmask_b32 v227, v227, 0, s[4:5]\n"
    "v_mfma_f32_16x16x32_bf16 v[140:143], v[196:199], v[40:43], v[140:143]\n"
    "v_mfma_f32_16x16x32_bf16 v[144:147], v[228:231], v[40:43], v[144:147]\n"
    "v_mfma_f32_16x16x32_bf16 v[148:151], v[196:199], v[44:47], v[148:151]\n"
    "s_add_u32 m0, s11, 24576\n"
    "s_add_u32 s59, s49, 1572864\n"
    "buffer_load_dwordx4 v176, s[32:35], s59 offen lds\n"
    "buffer_load_dwordx4 v[196:199], v114, s[12:15], s70 offen offset:768 sc1\n"
    "v_mfma_f32_16x16x32_bf16 v[152:155], v[228:231], v[44:47], v[152:155]\n"
    "s_waitcnt vmcnt(33)\n"
    "v_mfma_f32_16x16x32_bf16 v[124:127], v[188:191], v[48:51], v[124:127]\n"
    "v_mfma_f32_16x16x32_bf16 v[128:131], v[224:227], v[48:51], v[128:131]\n"
    "s_add_u32 m0, s11, 25600\n"
    "s_add_u32 s59, s49, 1605632\n"
    "buffer_load_dwordx4 v177, s[32:35], s59 offen lds\n"
    "v_mfma_f32_16x16x32_bf16 v[132:135], v[188:191], v[52:55], v[132:135]\n"
    "v_mfma_f32_16x16x32_bf16 v[136:139], v[224:227], v[52:55], v[136:139]\n"
    "v_mfma_f32_16x16x32_bf16 v[140:143], v[188:191], v[56:59], v[140:143]\n"
    "s_add_u32 m0, s11, 26624\n"
    "s_add_u32 s59, s49, 1638400\n"
    "buffer_load_dwordx4 v176, s[32:35], s59 offen lds\n"
    "v_mfma_f32_16x16x32_bf16 v[144:147], v[224:227], v[56:59], v[144:147]\n"
    "v_mfma_f32_16x16x32_bf16 v[148:151], v[188:191], v[60:63], v[148:151]\n"
    "s_add_u32 s70, s70, 28672\n"
    "buffer_load_dwordx4 v[188:191], v113, s[12:15], s70 offen offset:512 sc1\n"
    "v_mfma_f32_16x16x32_bf16 v[152:155], v[224:227], v[60:63], v[152:155]\n"
    "s_add_u32 m0, s11, 27648\n"
    "s_add_u32 s59, s49, 1671168\n"
    "buffer_load_dwordx4 v177, s[32:35], s59 offen lds\n"
    "s_waitcnt vmcnt(33)\n"
    "v_cndmask_b32 v228, 0, v192, s[4:5]\n"
    "v_mfma_f32_16x16x32_bf16 v[124:127], v[208:211], v[64:67], v[124:127]\n"
    "v_cndmask_b32 v229, 0, v193, s[4:5]\n"
    "v_cndmask_b32 v230, 0, v194, s[4:5]\n"
    "v_cndmask_b32 v231, 0, v195, s[4:5]\n"
    "s_nop 1\n"
    "v_mfma_f32_16x16x32_bf16 v[128:131], v[228:231], v[64:67], v[128:131]\n"
    "s_waitcnt lgkmcnt(0)\n"
    "s_add_u32 s70, s70, 28672\n"
    "buffer_load_dwordx4 v[224:227], v113, s[12:15], s70 offen offset:512 sc1\n"
    "v_mfma_f32_16x16x32_bf16 v[132:135], v[208:211], v[68:71], v[132:135]\n"
    "v_cndmask_b32 v192, v192, 0, s[4:5]\n"
    "v_cndmask_b32 v193, v193, 0, s[4:5]\n"
    "v_mfma_f32_16x16x32_bf16 v[136:139], v[228:231], v[68:71], v[136:139]\n"
    "s_add_u32 m0, s11, 28672\n"
    "s_add_u32 s59, s49, 1703936\n"
    "buffer_load_dwordx4 v176, s[32:35], s59 offen lds\n"
    "v_cndmask_b32 v194, v194, 0, s[4:5]\n"
    "v_cndmask_b32 v195, v195, 0, s[4:5]\n"
    "v_mfma_f32_16x16x32_bf16 v[140:143], v[208:211], v[72:75], v[140:143]\n"
    "v_mfma_f32_16x16x32_bf16 v[144:147], v[228:231], v[72:75], v[144:147]\n"
    "v_mfma_f32_16x16x32_bf16 v[148:151], v[208:211], v[76:79], v[148:151]\n"
    "s_add_u32 m0, s11, 29696\n"
    "s_add_u32 s59, s49, 1736704\n"
    "buffer_load_dwordx4 v177, s[32:35], s59 offen lds\n"
    "ds_read_b64_tr_b16 v[236:237], v115 offset:0\n"
    "buffer_load_dwordx4 v[208:211], v114, s[12:15], s70 offen offset:768 sc1\n"
    "v_mfma_f32_16x16x32_bf16 v[152:155], v[228:231], v[76:79], v[152:155]\n"
    "ds_read_b64_tr_b16 v[238:239], v115 offset:512\n"
    "s_waitcnt vmcnt(35)\n"
    "v_mfma_f32_16x16x32_bf16 v[124:127], v[200:203], v[80:83], v[124:127]\n"
    "ds_read_b64_tr_b16 v[240:241], v116 offset:0\n"
    "v_mfma_f32_16x16x32_bf16 v[128:131], v[192:195], v[80:83], v[128:131]\n"
    "s_add_u32 m0, s11, 30720\n"
    "s_add_u32 s59, s49, 1769472\n"
    "buffer_load_dwordx4 v176, s[32:35], s59 offen lds\n"
    "ds_read_b64_tr_b16 v[242:243], v116 offset:512\n"
    "v_mfma_f32_16x16x32_bf16 v[132:135], v[200:203], v[84:87], v[132:135]\n"
    "ds_read_b64_tr_b16 v[244:245], v117 offset:0\n"
    "v_mfma_f32_16x16x32_bf16 v[136:139], v[192:195], v[84:87], v[136:139]\n"
    "ds_read_b64_tr_b16 v[246:247], v117 offset:512\n"
    "v_mfma_f32_16x16x32_bf16 v[140:143], v[200:203], v[88:91], v[140:143]\n"
    "s_add_u32 m0, s11, 31744\n"
    "s_add_u32 s59, s49, 1802240\n"
    "buffer_load_dwordx4 v177, s[32:35], s59 offen lds\n"
    "ds_read_b64_tr_b16 v[248:249], v118 offset:0\n"
    "v_mfma_f32_16x16x32_bf16 v[144:147], v[192:195], v[88:91], v[144:147]\n"
    "ds_read_b64_tr_b16 v[250:251], v118 offset:512\n"
    "v_mfma_f32_16x16x32_bf16 v[148:151], v[200:203], v[92:95], v[148:151]\n"
    "s_waitcnt vmcnt(35)\n"
    "ds_read_b64_tr_b16 v[252:253], v115 offset:4096\n"
    "s_waitcnt vmcnt(28)\n"
    "v_cndmask_b32 v228, 0, v220, s[4:5]\n"
    "v_mfma_f32_16x16x32_bf16 v[152:155], v[192:195], v[92:95], v[152:155]\n"
    "s_waitcnt lgkmcnt(7)\n"
    "v_mfma_f32_16x16x32_bf16 v[124:127], v[204:207], v[236:239], v[124:127]\n"
    "s_add_u32 s70, s70, 28672\n"
    "buffer_load_dwordx4 v[200:203], v113, s[12:15], s70 offen offset:512 sc1\n"
    "v_cndmask_b32 v229, 0, v221, s[4:5]\n"
    "v_cndmask_b32 v230, 0, v222, s[4:5]\n"
    "v_cndmask_b32 v231, 0, v223, s[4:5]\n"
    "s_nop 1\n"
    "v_mfma_f32_16x16x32_bf16 v[128:131], v[228:231], v[236:239], v[128:131]\n"
    "ds_read_b64_tr_b16 v[254:255], v115 offset:4608\n"
    "s_add_u32 s70, s70, 28672\n"
    "buffer_load_dwordx4 v[192:195], v113, s[12:15], s70 offen offset:512 sc1\n"
    "s_waitcnt lgkmcnt(6)\n"
    "v_mfma_f32_16x16x32_bf16 v[132:135], v[204:207], v[240:243], v[132:135]\n"
    "ds_read_b64_tr_b16 v[232:233], v116 offset:4096\n"
    "ds_read_b64_tr_b16 v[234:235], v116 offset:4608\n"
    "v_mfma_f32_16x16x32_bf16 v[136:139], v[228:231], v[240:243], v[136:139]\n"
    "ds_read_b64_tr_b16 v[236:237], v117 offset:4096\n"
    "s_waitcnt lgkmcnt(7)\n"
    "v_mfma_f32_16x16x32_bf16 v[140:143], v[204:207], v[244:247], v[140:143]\n"
    "ds_read_b64_tr_b16 v[238:239], v117 offset:4608\n"
    "ds_read_b64_tr_b16 v[240:241], v118 offset:4096\n"
    "v_mfma_f32_16x16x32_bf16 v[144:147], v[228:231], v[244:247], v[144:147]\n"
    "v_cndmask_b32 v220, v220, 0, s[4:5]\n"
    "s_waitcnt lgkmcnt(7)\n"
    "v_mfma_f32_16x16x32_bf16 v[148:151], v[204:207], v[248:251], v[148:151]\n"
    "v_cndmask_b32 v221, v221, 0, s[4:5]\n"
    "v_cndmask_b32 v222, v222, 0, s[4:5]\n"
    "v_mfma_f32_16x16x32_bf16 v[152:155], v[228:231], v[248:251], v[152:155]\n"
    "v_cndmask_b32 v223, v223, 0, s[4:5]\n"
    "ds_read_b64_tr_b16 v[242:243], v118 offset:4608\n"
    "s_waitcnt vmcnt(18) lgkmcnt(6)\n"
    "v_mfma_f32_16x16x32_bf16 v[124:127], v[216:219], v[252:255], v[124:127]\n"
    "ds_read_b64_tr_b16 v[244:245], v115 offset:8192\n"
    "buffer_load_dwordx4 v[204:207], v114, s[12:15], s70 offen offset:768 sc1\n"
    "v_mfma_f32_16x16x32_bf16 v[128:131], v[220:223], v[252:255], v[128:131]\n"
    "ds_read_b64_tr_b16 v[246:247], v115 offset:8704\n"
    "s_waitcnt lgkmcnt(6)\n"
    "v_mfma_f32_16x16x32_bf16 v[132:135], v[216:219], v[232:235], v[132:135]\n"
    "ds_read_b64_tr_b16 v[248:249], v116 offset:8192\n"
    "ds_read_b64_tr_b16 v[250:251], v116 offset:8704\n"
    "v_mfma_f32_16x16x32_bf16 v[136:139], v[220:223], v[232:235], v[136:139]\n"
    "ds_read_b64_tr_b16 v[252:253], v117 offset:8192\n"
    "s_waitcnt lgkmcnt(7)\n"
    "v_mfma_f32_16x16x32_bf16 v[140:143], v[216:219], v[236:239], v[140:143]\n"
    "ds_read_b64_tr_b16 v[254:255], v117 offset:8704\n"
    "ds_read_b64_tr_b16 v[232:233], v118 offset:8192\n"
    "v_mfma_f32_16x16x32_bf16 v[144:147], v[220:223], v[236:239], v[144:147]\n"
    "ds_read_b64_tr_b16 v[234:235], v118 offset:8704\n"
    "s_waitcnt lgkmcnt(8)\n"
    "v_mfma_f32_16x16x32_bf16 v[148:151], v[216:219], v[240:243], v[148:151]\n"
    "ds_read_b64_tr_b16 v[236:237], v115 offset:12288\n"
    "s_waitcnt vmcnt(13)\n"
    "v_cndmask_b32 v228, 0, v196, s[4:5]\n"
    "v_mfma_f32_16x16x32_bf16 v[152:155], v[220:223], v[240:243], v[152:155]\n"
    "s_waitcnt lgkmcnt(7)\n"
    "v_mfma_f32_16x16x32_bf16 v[124:127], v[212:215], v[244:247], v[124:127]\n"
    "s_add_u32 s70, s70, 28672\n"
    "buffer_load_dwordx4 v[216:219], v113, s[12:15], s70 offen offset:512 sc1\n"
    "v_cndmask_b32 v229, 0, v197, s[4:5]\n"
    "v_cndmask_b32 v230, 0, v198, s[4:5]\n"
    "v_cndmask_b32 v231, 0, v199, s[4:5]\n"
    "s_nop 1\n"
    "v_mfma_f32_16x16x32_bf16 v[128:131], v[228:231], v[244:247], v[128:131]\n"
    "ds_read_b64_tr_b16 v[238:239], v115 offset:12800\n"
    "s_waitcnt lgkmcnt(6)\n"
    "v_mfma_f32_16x16x32_bf16 v[132:135], v[212:215], v[248:251], v[132:135]\n"
    "ds_read_b64_tr_b16 v[240:241], v116 offset:12288\n"
    "ds_read_b64_tr_b16 v[242:243], v116 offset:12800\n"
    "v_mfma_f32_16x16x32_bf16 v[136:139], v[228:231], v[248:251], v[136:139]\n"
    "ds_read_b64_tr_b16 v[244:245], v117 offset:12288\n"
    "s_waitcnt lgkmcnt(7)\n"
    "v_mfma_f32_16x16x32_bf16 v[140:143], v[212:215], v[252:255], v[140:143]\n"
    "ds_read_b64_tr_b16 v[246:247], v117 offset:12800\n"
    "ds_read_b64_tr_b16 v[248:249], v118 offset:12288\n"
    "v_mfma_f32_16x16x32_bf16 v[144:147], v[228:231], v[252:255], v[144:147]\n"
    "v_cndmask_b32 v196, v196, 0, s[4:5]\n"
    "s_waitcnt lgkmcnt(7)\n"
    "v_mfma_f32_16x16x32_bf16 v[148:151], v[212:215], v[232:235], v[148:151]\n"
    "v_cndmask_b32 v197, v197, 0, s[4:5]\n"
    "v_cndmask_b32 v198, v198, 0, s[4:5]\n"
    "v_mfma_f32_16x16x32_bf16 v[152:155], v[228:231], v[232:235], v[152:155]\n"
    "v_cndmask_b32 v199, v199, 0, s[4:5]\n"
    "ds_read_b64_tr_b16 v[250:251], v118 offset:12800\n"
    "s_waitcnt vmcnt(11) lgkmcnt(6)\n"
    "v_mfma_f32_16x16x32_bf16 v[124:127], v[188:191], v[236:239], v[124:127]\n"
    "ds_read_b64_tr_b16 v[252:253], v115 offset:16384\n"
    "ds_read_b64_tr_b16 v[254:255], v115 offset:16896\n"
    "v_mfma_f32_16x16x32_bf16 v[128:131], v[196:199], v[236:239], v[128:131]\n"
    "ds_read_b64_tr_b16 v[232:233], v116 offset:16384\n"
    "s_waitcnt lgkmcnt(7)\n"
    "v_mfma_f32_16x16x32_bf16 v[132:135], v[188:191], v[240:243], v[132:135]\n"
    "buffer_load_dword v186, v185, s[12:15], s82 offen sc1\n"
    "s_add_u32 s70, s82, 0\n"
    "buffer_load_dwordx4 v[220:223], v113, s[12:15], s70 offen offset:0 sc1\n"
    "buffer_load_dwordx4 v[212:215], v113, s[12:15], s70 offen offset:256 sc1\n"
    "v_mfma_f32_16x16x32_bf16 v[136:139], v[196:199], v[240:243], v[136:139]\n"
    "ds_read_b64_tr_b16 v[234:235], v116 offset:16896\n"
    "s_waitcnt lgkmcnt(6)\n"
    "v_mfma_f32_16x16x32_bf16 v[140:143], v[188:191], v[244:247], v[140:143]\n"
    "ds_read_b64_tr_b16 v[236:237], v117 offset:16384\n"
    "ds_read_b64_tr_b16 v[238:239], v117 offset:16896\n"
    "v_mfma_f32_16x16x32_bf16 v[144:147], v[196:199], v[244:247], v[144:147]\n"
    "ds_read_b64_tr_b16 v[240:241], v118 offset:16384\n"
    "s_waitcnt lgkmcnt(7)\n"
    "v_mfma_f32_16x16x32_bf16 v[148:151], v[188:191], v[248:251], v[148:151]\n"
    "ds_read_b64_tr_b16 v[242:243], v118 offset:16896\n"
    "ds_read_b64_tr_b16 v[244:245], v115 offset:20480\n"
    "v_mfma_f32_16x16x32_bf16 v[152:155], v[196:199], v[248:251], v[152:155]\n"
    "s_waitcnt vmcnt(12) lgkmcnt(7)\n"
    "v_mfma_f32_16x16x32_bf16 v[124:127], v[224:227], v[252:255], v[124:127]\n"
    "s_add_u32 s70, s70, 28672\n"
    "buffer_load_dwordx4 v[188:191], v113, s[12:15], s70 offen offset:0 sc1\n"
    "s_waitcnt vmcnt(10)\n"
    "v_cndmask_b32 v228, 0, v208, s[4:5]\n"
    "v_cndmask_b32 v229, 0, v209, s[4:5]\n"
    "v_cndmask_b32 v230, 0, v210, s[4:5]\n"
    "v_cndmask_b32 v231, 0, v211, s[4:5]\n"
    "s_nop 1\n"
    "v_mfma_f32_16x16x32_bf16 v[128:131], v[228:231], v[252:255], v[128:131]\n"
    "ds_read_b64_tr_b16 v[246:247], v115 offset:20992\n"
    "buffer_load_dwordx4 v[196:199], v113, s[12:15], s70 offen offset:256 sc1\n"
    "s_waitcnt lgkmcnt(6)\n"
    "v_mfma_f32_16x16x32_bf16 v[132:135], v[224:227], v[232:235], v[132:135]\n"
    "ds_read_b64_tr_b16 v[248:249], v116 offset:20480\n"
    "ds_read_b64_tr_b16 v[250:251], v116 offset:20992\n"
    "v_mfma_f32_16x16x32_bf16 v[136:139], v[228:231], v[232:235], v[136:139]\n"
    "ds_read_b64_tr_b16 v[252:253], v117 offset:20480\n"
    "s_waitcnt lgkmcnt(7)\n"
    "v_mfma_f32_16x16x32_bf16 v[140:143], v[224:227], v[236:239], v[140:143]\n"
    "ds_read_b64_tr_b16 v[254:255], v117 offset:20992\n"
    "ds_read_b64_tr_b16 v[232:233], v118 offset:20480\n"
    "v_mfma_f32_16x16x32_bf16 v[144:147], v[228:231], v[236:239], v[144:147]\n"
    "v_cndmask_b32 v208, v208, 0, s[4:5]\n"
    "s_waitcnt lgkmcnt(7)\n"
    "v_mfma_f32_16x16x32_bf16 v[148:151], v[224:227], v[240:243], v[148:151]\n"
    "v_cndmask_b32 v209, v209, 0, s[4:5]\n"
    "v_cndmask_b32 v210, v210, 0, s[4:5]\n"
    "v_mfma_f32_16x16x32_bf16 v[152:155], v[228:231], v[240:243], v[152:155]\n"
    "v_cndmask_b32 v211, v211, 0, s[4:5]\n"
    "ds_read_b64_tr_b16 v[234:235], v118 offset:20992\n"
    "s_waitcnt vmcnt(8) lgkmcnt(6)\n"
    "v_mfma_f32_16x16x32_bf16 v[124:127], v[200:203], v[244:247], v[124:127]\n"
    "ds_read_b64_tr_b16 v[236:237], v115 offset:24576\n"
    "ds_read_b64_tr_b16 v[238:239], v115 offset:25088\n"
    "s_add_u32 s70, s70, 28672\n"
    "buffer_load_dwordx4 v[224:227], v113, s[12:15], s70 offen offset:0 sc1\n"
    "v_mfma_f32_16x16x32_bf16 v[128:131], v[208:211], v[244:247], v[128:131]\n"
    "ds_read_b64_tr_b16 v[240:241], v116 offset:24576\n"
    "s_waitcnt lgkmcnt(7)\n"
    "v_mfma_f32_16x16x32_bf16 v[132:135], v[200:203], v[248:251], v[132:135]\n"
    "ds_read_b64_tr_b16 v[242:243], v116 offset:25088\n"
    "ds_read_b64_tr_b16 v[244:245], v117 offset:24576\n"
    "v_mfma_f32_16x16x32_bf16 v[136:139], v[208:211], v[248:251], v[136:139]\n"
    "ds_read_b64_tr_b16 v[246:247], v117 offset:25088\n"
    "s_waitcnt lgkmcnt(8)\n"
    "v_mfma_f32_16x16x32_bf16 v[140:143], v[200:203], v[252:255], v[140:143]\n"
    "ds_read_b64_tr_b16 v[248:249], v118 offset:24576\n"
    "v_mfma_f32_16x16x32_bf16 v[144:147], v[208:211], v[252:255], v[144:147]\n"
    "ds_read_b64_tr_b16 v[250:251], v118 offset:25088\n"
    "s_waitcnt lgkmcnt(8)\n"
    "v_mfma_f32_16x16x32_bf16 v[148:151], v[200:203], v[232:235], v[148:151]\n"
    "ds_read_b64_tr_b16 v[252:253], v115 offset:28672\n"
    "buffer_load_dwordx4 v[200:203], v113, s[12:15], s70 offen offset:256 sc1\n"
    "v_mfma_f32_16x16x32_bf16 v[152:155], v[208:211], v[232:235], v[152:155]\n"
    "s_waitcnt vmcnt(9) lgkmcnt(7)\n"
    "v_mfma_f32_16x16x32_bf16 v[124:127], v[192:195], v[236:239], v[124:127]\n"
    "s_waitcnt vmcnt(8)\n"
    "v_cndmask_b32 v228, 0, v204, s[4:5]\n"
    "v_cndmask_b32 v229, 0, v205, s[4:5]\n"
    "v_cndmask_b32 v230, 0, v206, s[4:5]\n"
    "v_cndmask_b32 v231, 0, v207, s[4:5]\n"
    "s_nop 1\n"
    "v_mfma_f32_16x16x32_bf16 v[128:131], v[228:231], v[236:239], v[128:131]\n"
    "ds_read_b64_tr_b16 v[254:255], v115 offset:29184\n"
    "s_add_u32 s70, s70, 28672\n"
    "buffer_load_dwordx4 v[208:211], v113, s[12:15], s70 offen offset:0 sc1\n"
    "s_waitcnt lgkmcnt(6)\n"
    "v_mfma_f32_16x16x32_bf16 v[132:135], v[192:195], v[240:243], v[132:135]\n"
    "ds_read_b64_tr_b16 v[232:233], v116 offset:28672\n"
    "ds_read_b64_tr_b16 v[234:235], v116 offset:29184\n"
    "v_mfma_f32_16x16x32_bf16 v[136:139], v[228:231], v[240:243], v[136:139]\n"
    "ds_read_b64_tr_b16 v[236:237], v117 offset:28672\n"
    "s_waitcnt lgkmcnt(7)\n"
    "v_mfma_f32_16x16x32_bf16 v[140:143], v[192:195], v[244:247], v[140:143]\n"
    "ds_read_b64_tr_b16 v[238:239], v117 offset:29184\n"
    "ds_read_b64_tr_b16 v[240:241], v118 offset:28672\n"
    "v_mfma_f32_16x16x32_bf16 v[144:147], v[228:231], v[244:247], v[144:147]\n"
    "v_cndmask_b32 v204, v204, 0, s[4:5]\n"
    "s_waitcnt lgkmcnt(7)\n"
    "v_mfma_f32_16x16x32_bf16 v[148:151], v[192:195], v[248:251], v[148:151]\n"
    "v_cndmask_b32 v205, v205, 0, s[4:5]\n"
    "v_cndmask_b32 v206, v206, 0, s[4:5]\n"
    "v_mfma_f32_16x16x32_bf16 v[152:155], v[228:231], v[248:251], v[152:155]\n"
    "v_cndmask_b32 v207, v207, 0, s[4:5]\n"
    "ds_read_b64_tr_b16 v[242:243], v118 offset:29184\n"
    "s_waitcnt vmcnt(8) lgkmcnt(6)\n"
    "v_mfma_f32_16x16x32_bf16 v[124:127], v[216:219], v[252:255], v[124:127]\n"
    "buffer_load_dwordx4 v[192:195], v113, s[12:15], s70 offen offset:256 sc1\n"
    "v_mfma_f32_16x16x32_bf16 v[128:131], v[204:207], v[252:255], v[128:131]\n"
    "s_waitcnt lgkmcnt(4)\n"
    "v_mfma_f32_16x16x32_bf16 v[132:135], v[216:219], v[232:235], v[132:135]\n"
    "v_mfma_f32_16x16x32_bf16 v[136:139], v[204:207], v[232:235], v[136:139]\n"
    "s_waitcnt lgkmcnt(2)\n"
    "v_mfma_f32_16x16x32_bf16 v[140:143], v[216:219], v[236:239], v[140:143]\n"
    "v_mfma_f32_16x16x32_bf16 v[144:147], v[204:207], v[236:239], v[144:147]\n"
    "s_waitcnt lgkmcnt(0)\n"
    "v_mfma_f32_16x16x32_bf16 v[148:151], v[216:219], v[240:243], v[148:151]\n"
    "s_add_u32 s70, s70, 28672\n"
    "buffer_load_dwordx4 v[216:219], v113, s[12:15], s70 offen offset:0 sc1\n"
    "v_mfma_f32_16x16x32_bf16 v[152:155], v[204:207], v[240:243], v[152:155]\n"
    "buffer_load_dwordx4 v[204:207], v113, s[12:15], s70 offen offset:256 sc1\n"
    ".Lmark_p5in_32:\n"
    "s_waitcnt vmcnt(10)\n"
    "v_cmp_eq_u32 vcc, -1, v186\n"
    "s_cbranch_vccnz .Lgretry_33\n"
    ".Lgok_34:\n"
    "s_waitcnt vmcnt(9)\n"
    "v_mfma_f32_16x16x32_bf16 v[156:159], v[220:223], v[0:3], v[156:159]\n"
    "s_waitcnt vmcnt(8)\n"
    "v_mfma_f32_16x16x32_bf16 v[160:163], v[212:215], v[0:3], v[160:163]\n"
    "v_pk_add_f32 v[240:241], v[132:133], v[134:135]\n"
    "v_pk_add_f32 v[242:243], v[136:137], v[138:139]\n"
    "v_pk_add_f32 v[240:241], v[240:241], v[242:243]\n"
    "v_cmp_u_f32 vcc, v240, v241\n"
    "s_cbranch_vccnz .Lredo_39\n"
    ".Lredook_40:\n"
    ".Lback_36:\n"
    "v_mov_b32 v244, -1\n"
    "v_mov_b32 v245, -1\n"
    "v_mov_b32 v246, -1\n"
    "v_mov_b32 v247, -1\n"
    "s_lshl_b32 s74, s53, 13\n"
    "v_lshl_add_u32 v238, v112, 4, s74\n"
    "s_mov_b32 s25, 23068672\n"
    "buffer_store_dwordx4 v[244:247], v238, s[12:15], s25 offen sc1\n"
    "s_mov_b32 s25, 23072768\n"
    "buffer_store_dwordx4 v[244:247], v238, s[12:15], s25 offen sc1\n"
    "ds_write_b128 v119, v[132:135] offset:0\n"
    "ds_write_b128 v119, v[136:139] offset:1024\n"
    "ds_write_b128 v119, v[140:143] offset:2048\n"
    "ds_write_b128 v119, v[144:147] offset:3072\n"
    "ds_write_b128 v119, v[148:151] offset:4096\n"
    "ds_write_b128 v119, v[152:155] offset:5120\n"
    "v_mfma_f32_16x16x32_bf16 v[164:167], v[220:223], v[4:7], 0\n"
    "s_waitcnt lgkmcnt(0)\n"
    "s_barrier\n"
    "ds_read_b128 v[132:135], v120 offset:0\n"
    "ds_read_b128 v[136:139], v120 offset:1024\n"
    "ds_read_b128 v[140:143], v121 offset:2048\n"
    "ds_read_b128 v[144:147], v121 offset:3072\n"
    "ds_read_b128 v[148:151], v122 offset:4096\n"
    "ds_read_b128 v[152:155], v122 offset:5120\n"
    "v_mfma_f32_16x16x32_bf16 v[168:171], v[212:215], v[4:7], 0\n"
    "v_mfma_f32_16x16x32_bf16 v[172:175], v[220:223], v[8:11], 0\n"
    "v_mfma_f32_16x16x32_bf16 v[176:179], v[212:215], v[8:11], 0\n"
    "v_mfma_f32_16x16x32_bf16 v[180:183], v[220:223], v[12:15], 0\n"
    "s_add_u32 s70, s70, 28672\n"
    "buffer_load_dwordx4 v[220:223], v113, s[12:15], s70 offen offset:0 sc1\n"
    "v_mfma_f32_16x16x32_bf16 v[184:187], v[212:215], v[12:15], 0\n"
    "buffer_load_dwordx4 v[212:215], v113, s[12:15], s70 offen offset:256 sc1\n"
    "s_waitcnt vmcnt(11)\n"
    "v_mfma_f32_16x16x32_bf16 v[156:159], v[188:191], v[16:19], v[156:159]\n"
    "s_waitcnt lgkmcnt(3)\n"
    "v_pk_add_f32 v[132:133], v[132:133], v[140:141]\n"
    "v_pk_add_f32 v[134:135], v[134:135], v[142:143]\n"
    "s_waitcnt lgkmcnt(2)\n"
    "v_pk_add_f32 v[136:137], v[136:137], v[144:145]\n"
    "v_pk_add_f32 v[138:139], v[138:139], v[146:147]\n"
    "s_waitcnt lgkmcnt(1)\n"
    "v_pk_add_f32 v[124:125], v[124:125], v[148:149]\n"
    "v_pk_add_f32 v[126:127], v[126:127], v[150:151]\n"
    "s_waitcnt lgkmcnt(0)\n"
    "v_pk_add_f32 v[128:129], v[128:129], v[152:153]\n"
    "v_pk_add_f32 v[130:131], v[130:131], v[154:155]\n"
    "v_pk_add_f32 v[124:125], v[124:125], v[132:133]\n"
    "v_pk_add_f32 v[126:127], v[126:127], v[134:135]\n"
    "v_pk_add_f32 v[128:129], v[128:129], v[136:137]\n"
    "v_pk_add_f32 v[130:131], v[130:131], v[138:139]\n"
    "v_and_b32 v132, 15, v112\n"
    "v_bfe_u32 v133, v112, 4, 2\n"
    "v_bfrev_b32 v134, v133\n"
    "v_lshrrev_b32 v134, 26, v134\n"
    "v_mov_b32 v135, 1073741824\n"
    "s_lshl_b32 s74, s6, 3\n"
    "s_add_u32 s74, s74, s56\n"
    "s_lshl_b32 s74, s74, 2\n"
    "s_add_u32 s74, s74, s52\n"
    "s_mul_i32 s74, s74, 1792\n"
    "s_add_u32 s74, s74, 14680064\n"
    "v_lshl_add_u32 v140, v133, 4, s74\n"
    "v_lshl_add_u32 v140, v132, 6, v140\n"
    "s_add_u32 s74, s74, 1024\n"
    "v_add_u32 v141, s74, v134\n"
    "v_mad_u32_u24 v141, v132, 48, v141\n"
    "v_cmp_ne_u32 vcc, 3, v133\n"
    "s_nop 1\n"
    "v_cndmask_b32 v141, v135, v141, vcc\n"
    "v_mov_b32 v132, v128\n"
    "v_mov_b32 v133, v129\n"
    "v_mov_b32 v134, v130\n"
    "v_mov_b32 v135, v131\n"
    "v_permlane32_swap_b32 v128, v132\n"
    "v_permlane32_swap_b32 v129, v133\n"
    "v_permlane32_swap_b32 v130, v134\n"
    "v_permlane32_swap_b32 v131, v135\n"
    "v_pk_add_f32 v[128:129], v[128:129], v[132:133]\n"
    "v_pk_add_f32 v[130:131], v[130:131], v[134:135]\n"
    "s_nop 0\n"
    "v_permlane16_swap_b32 v124, v128\n"
    "v_permlane16_swap_b32 v125, v129\n"
    "v_permlane16_swap_b32 v126, v130\n"
    "v_permlane16_swap_b32 v127, v131\n"
    "v_cvt_pk_bf16_f32 v136, v124, v125\n"
    "v_cvt_pk_bf16_f32 v137, v126, v127\n"
    "v_cvt_pk_bf16_f32 v138, v128, v129\n"
    "v_cvt_pk_bf16_f32 v139, v130, v131\n"
    "buffer_store_dwordx4 v[136:139], v141, s[12:15], 0 offen sc1\n"
    "s_waitcnt lgkmcnt(0)\n"
    "s_barrier\n"
    "s_waitcnt vmcnt(11)\n"
    "v_mfma_f32_16x16x32_bf16 v[160:163], v[196:199], v[16:19], v[160:163]\n"
    "v_mfma_f32_16x16x32_bf16 v[164:167], v[188:191], v[20:23], v[164:167]\n"
    "v_mfma_f32_16x16x32_bf16 v[168:171], v[196:199], v[20:23], v[168:171]\n"
    "v_mfma_f32_16x16x32_bf16 v[172:175], v[188:191], v[24:27], v[172:175]\n"
    "v_mfma_f32_16x16x32_bf16 v[176:179], v[196:199], v[24:27], v[176:179]\n"
    "v_mfma_f32_16x16x32_bf16 v[180:183], v[188:191], v[28:31], v[180:183]\n"
    "s_add_u32 s70, s70, 28672\n"
    "buffer_load_dwordx4 v[188:191], v113, s[12:15], s70 offen offset:0 sc1\n"
    "v_mfma_f32_16x16x32_bf16 v[184:187], v[196:199], v[28:31], v[184:187]\n"
    "buffer_load_dwordx4 v[196:199], v113, s[12:15], s70 offen offset:256 sc1\n"
    "s_waitcnt vmcnt(12)\n"
    "v_mfma_f32_16x16x32_bf16 v[156:159], v[224:227], v[32:35], v[156:159]\n"
    "s_waitcnt vmcnt(11)\n"
    "v_mfma_f32_16x16x32_bf16 v[160:163], v[200:203], v[32:35], v[160:163]\n"
    "v_mfma_f32_16x16x32_bf16 v[164:167], v[224:227], v[36:39], v[164:167]\n"
    "v_mfma_f32_16x16x32_bf16 v[168:171], v[200:203], v[36:39], v[168:171]\n"
    "v_mfma_f32_16x16x32_bf16 v[172:175], v[224:227], v[40:43], v[172:175]\n"
    "v_mfma_f32_16x16x32_bf16 v[176:179], v[200:203], v[40:43], v[176:179]\n"
    "v_mfma_f32_16x16x32_bf16 v[180:183], v[224:227], v[44:47], v[180:183]\n"
    "s_add_u32 s70, s70, 28672\n"
    "buffer_load_dwordx4 v[224:227], v113, s[12:15], s70 offen offset:0 sc1\n"
    "v_mfma_f32_16x16x32_bf16 v[184:187], v[200:203], v[44:47], v[184:187]\n"
    "buffer_load_dwordx4 v[200:203], v113, s[12:15], s70 offen offset:256 sc1\n"
    "s_waitcnt vmcnt(12)\n"
    "v_mfma_f32_16x16x32_bf16 v[156:159], v[208:211], v[48:51], v[156:159]\n"
    "s_waitcnt vmcnt(11)\n"
    "v_mfma_f32_16x16x32_bf16 v[160:163], v[192:195], v[48:51], v[160:163]\n"
    "v_mfma_f32_16x16x32_bf16 v[164:167], v[208:211], v[52:55], v[164:167]\n"
    "v_mfma_f32_16x16x32_bf16 v[168:171], v[192:195], v[52:55], v[168:171]\n"
    "v_mfma_f32_16x16x32_bf16 v[172:175], v[208:211], v[56:59], v[172:175]\n"
    "v_mfma_f32_16x16x32_bf16 v[176:179], v[192:195], v[56:59], v[176:179]\n"
    "v_mfma_f32_16x16x32_bf16 v[180:183], v[208:211], v[60:63], v[180:183]\n"
    "s_add_u32 s70, s70, 28672\n"
    "buffer_load_dwordx4 v[208:211], v113, s[12:15], s70 offen offset:0 sc1\n"
    "v_mfma_f32_16x16x32_bf16 v[184:187], v[192:195], v[60:63], v[184:187]\n"
    "buffer_load_dwordx4 v[192:195], v113, s[12:15], s70 offen offset:256 sc1\n"
    "s_waitcnt vmcnt(12)\n"
    "v_mfma_f32_16x16x32_bf16 v[156:159], v[216:219], v[64:67], v[156:159]\n"
    "s_waitcnt vmcnt(11)\n"
    "v_mfma_f32_16x16x32_bf16 v[160:163], v[204:207], v[64:67], v[160:163]\n"
    "v_mfma_f32_16x16x32_bf16 v[164:167], v[216:219], v[68:71], v[164:167]\n"
    "v_mfma_f32_16x16x32_bf16 v[168:171], v[204:207], v[68:71], v[168:171]\n"
    "v_mfma_f32_16x16x32_bf16 v[172:175], v[216:219], v[72:75], v[172:175]\n"
    "v_mfma_f32_16x16x32_bf16 v[176:179], v[204:207], v[72:75], v[176:179]\n"
    "v_mfma_f32_16x16x32_bf16 v[180:183], v[216:219], v[76:79], v[180:183]\n"
    "ds_read_b64_tr_b16 v[244:245], v115 offset:0\n"
    "v_mfma_f32_16x16x32_bf16 v[184:187], v[204:207], v[76:79], v[184:187]\n"
    "ds_read_b64_tr_b16 v[246:247], v115 offset:512\n"
    "s_waitcnt vmcnt(8)\n"
    "v_mfma_f32_16x16x32_bf16 v[156:159], v[220:223], v[80:83], v[156:159]\n"
    "s_add_u32 s70, s70, 28672\n"
    "buffer_load_dwordx4 v[216:219], v113, s[12:15], s70 offen offset:0 sc1\n"
    "s_waitcnt vmcnt(8)\n"
    "v_mfma_f32_16x16x32_bf16 v[160:163], v[212:215], v[80:83], v[160:163]\n"
    "buffer_load_dwordx4 v[204:207], v113, s[12:15], s70 offen offset:256 sc1\n"
    "ds_read_b64_tr_b16 v[248:249], v116 offset:0\n"
    "v_mfma_f32_16x16x32_bf16 v[164:167], v[220:223], v[84:87], v[164:167]\n"
    "ds_read_b64_tr_b16 v[250:251], v116 offset:512\n"
    "ds_read_b64_tr_b16 v[252:253], v117 offset:0\n"
    "v_mfma_f32_16x16x32_bf16 v[168:171], v[212:215], v[84:87], v[168:171]\n"
    "ds_read_b64_tr_b16 v[254:255], v117 offset:512\n"
    "v_mfma_f32_16x16x32_bf16 v[172:175], v[220:223], v[88:91], v[172:175]\n"
    "ds_read_b64_tr_b16 v[232:233], v118 offset:0\n"
    "v_mfma_f32_16x16x32_bf16 v[176:179], v[212:215], v[88:91], v[176:179]\n"
    "ds_read_b64_tr_b16 v[234:235], v118 offset:512\n"
    "v_mfma_f32_16x16x32_bf16 v[180:183], v[220:223], v[92:95], v[180:183]\n"
    "ds_read_b64_tr_b16 v[236:237], v115 offset:4096\n"
    "v_mfma_f32_16x16x32_bf16 v[184:187], v[212:215], v[92:95], v[184:187]\n"
    "s_waitcnt vmcnt(7) lgkmcnt(7)\n"
    "v_mfma_f32_16x16x32_bf16 v[156:159], v[188:191], v[244:247], v[156:159]\n"
    "s_add_u32 s70, s70, 28672\n"
    "buffer_load_dwordx4 v[220:223], v113, s[12:15], s70 offen offset:0 sc1\n"
    "s_waitcnt vmcnt(7)\n"
    "v_mfma_f32_16x16x32_bf16 v[160:163], v[196:199], v[244:247], v[160:163]\n"
    "ds_read_b64_tr_b16 v[238:239], v115 offset:4608\n"
    "buffer_load_dwordx4 v[212:215], v113, s[12:15], s70 offen offset:256 sc1\n"
    "s_waitcnt lgkmcnt(6)\n"
    "v_mfma_f32_16x16x32_bf16 v[164:167], v[188:191], v[248:251], v[164:167]\n"
    "ds_read_b64_tr_b16 v[240:241], v116 offset:4096\n"
    "ds_read_b64_tr_b16 v[242:243], v116 offset:4608\n"
    "v_mfma_f32_16x16x32_bf16 v[168:171], v[196:199], v[248:251], v[168:171]\n"
    "ds_read_b64_tr_b16 v[244:245], v117 offset:4096\n"
    "s_waitcnt lgkmcnt(7)\n"
    "v_mfma_f32_16x16x32_bf16 v[172:175], v[188:191], v[252:255], v[172:175]\n"
    "ds_read_b64_tr_b16 v[246:247], v117 offset:4608\n"
    "ds_read_b64_tr_b16 v[248:249], v118 offset:4096\n"
    "v_mfma_f32_16x16x32_bf16 v[176:179], v[196:199], v[252:255], v[176:179]\n"
    "ds_read_b64_tr_b16 v[250:251], v118 offset:4608\n"
    "s_waitcnt lgkmcnt(8)\n"
    "v_mfma_f32_16x16x32_bf16 v[180:183], v[188:191], v[232:235], v[180:183]\n"
    "ds_read_b64_tr_b16 v[252:253], v115 offset:8192\n"
    "v_mfma_f32_16x16x32_bf16 v[184:187], v[196:199], v[232:235], v[184:187]\n"
    "s_waitcnt vmcnt(7) lgkmcnt(7)\n"
    "v_mfma_f32_16x16x32_bf16 v[156:159], v[224:227], v[236:239], v[156:159]\n"
    "s_add_u32 s70, s70, 28672\n"
    "buffer_load_dwordx4 v[188:191], v113, s[12:15], s70 offen offset:0 sc1\n"
    "s_waitcnt vmcnt(7)\n"
    "v_mfma_f32_16x16x32_bf16 v[160:163], v[200:203], v[236:239], v[160:163]\n"
    "ds_read_b64_tr_b16 v[254:255], v115 offset:8704\n"
    "buffer_load_dwordx4 v[196:199], v113, s[12:15], s70 offen offset:256 sc1\n"
    "s_waitcnt lgkmcnt(6)\n"
    "v_mfma_f32_16x16x32_bf16 v[164:167], v[224:227], v[240:243], v[164:167]\n"
    "ds_read_b64_tr_b16 v[232:233], v116 offset:8192\n"
    "ds_read_b64_tr_b16 v[234:235], v116 offset:8704\n"
    "v_mfma_f32_16x16x32_bf16 v[168:171], v[200:203], v[240:243], v[168:171]\n"
    "ds_read_b64_tr_b16 v[236:237], v117 offset:8192\n"
    "s_waitcnt lgkmcnt(7)\n"
    "v_mfma_f32_16x16x32_bf16 v[172:175], v[224:227], v[244:247], v[172:175]\n"
    "ds_read_b64_tr_b16 v[238:239], v117 offset:8704\n"
    "ds_read_b64_tr_b16 v[240:241], v118 offset:8192\n"
    "v_mfma_f32_16x16x32_bf16 v[176:179], v[200:203], v[244:247], v[176:179]\n"
    "ds_read_b64_tr_b16 v[242:243], v118 offset:8704\n"
    "s_waitcnt lgkmcnt(8)\n"
    "v_mfma_f32_16x16x32_bf16 v[180:183], v[224:227], v[248:251], v[180:183]\n"
    "ds_read_b64_tr_b16 v[244:245], v115 offset:12288\n"
    "v_mfma_f32_16x16x32_bf16 v[184:187], v[200:203], v[248:251], v[184:187]\n"
    "s_waitcnt vmcnt(7) lgkmcnt(7)\n"
    "v_mfma_f32_16x16x32_bf16 v[156:159], v[208:211], v[252:255], v[156:159]\n"
    "s_add_u32 s70, s70, 28672\n"
    "buffer_load_dwordx4 v[224:227], v113, s[12:15], s70 offen offset:0 sc1\n"
    "s_waitcnt vmcnt(7)\n"
    "v_mfma_f32_16x16x32_bf16 v[160:163], v[192:195], v[252:255], v[160:163]\n"
    "ds_read_b64_tr_b16 v[246:247], v115 offset:12800\n"
    "buffer_load_dwordx4 v[200:203], v113, s[12:15], s70 offen offset:256 sc1\n"
    "s_waitcnt lgkmcnt(6)\n"
    "v_mfma_f32_16x16x32_bf16 v[164:167], v[208:211], v[232:235], v[164:167]\n"
    "ds_read_b64_tr_b16 v[248:249], v116 offset:12288\n"
    "ds_read_b64_tr_b16 v[250:251], v116 offset:12800\n"
    "v_mfma_f32_16x16x32_bf16 v[168:171], v[192:195], v[232:235], v[168:171]\n"
    "ds_read_b64_tr_b16 v[252:253], v117 offset:12288\n"
    "s_waitcnt lgkmcnt(7)\n"
    "v_mfma_f32_16x16x32_bf16 v[172:175], v[208:211], v[236:239], v[172:175]\n"
    "ds_read_b64_tr_b16 v[254:255], v117 offset:12800\n"
    "ds_read_b64_tr_b16 v[232:233], v118 offset:12288\n"
    "v_mfma_f32_16x16x32_bf16 v[176:179], v[192:195], v[236:239], v[176:179]\n"
    "ds_read_b64_tr_b16 v[234:235], v118 offset:12800\n"
    "s_waitcnt lgkmcnt(8)\n"
    "v_mfma_f32_16x16x32_bf16 v[180:183], v[208:211], v[240:243], v[180:183]\n"
    "ds_read_b64_tr_b16 v[236:237], v115 offset:16384\n"
    "v_mfma_f32_16x16x32_bf16 v[184:187], v[192:195], v[240:243], v[184:187]\n"
    "s_waitcnt vmcnt(7) lgkmcnt(7)\n"
    "v_mfma_f32_16x16x32_bf16 v[156:159], v[216:219], v[244:247], v[156:159]\n"
    "s_add_u32 s70, s70, 28672\n"
    "buffer_load_dwordx4 v[208:211], v113, s[12:15], s70 offen offset:0 sc1\n"
    "s_waitcnt vmcnt(7)\n"
    "v_mfma_f32_16x16x32_bf16 v[160:163], v[204:207], v[244:247], v[160:163]\n"
    "ds_read_b64_tr_b16 v[238:239], v115 offset:16896\n"
    "buffer_load_dwordx4 v[192:195], v113, s[12:15], s70 offen offset:256 sc1\n"
    "s_waitcnt lgkmcnt(6)\n"
    "v_mfma_f32_16x16x32_bf16 v[164:167], v[216:219], v[248:251], v[164:167]\n"
    "ds_read_b64_tr_b16 v[240:241], v116 offset:16384\n"
    "ds_read_b64_tr_b16 v[242:243], v116 offset:16896\n"
    "v_mfma_f32_16x16x32_bf16 v[168:171], v[204:207], v[248:251], v[168:171]\n"
    "v_mov_b32 v144, -1\n"
    "v_mov_b32 v145, -1\n"
    "v_mov_b32 v146, -1\n"
    "v_mov_b32 v147, -1\n"
    "s_lshl_b32 s74, s53, 13\n"
    "v_lshl_add_u32 v142, v112, 4, s74\n"
    "s_mov_b32 s25, 25165824\n"
    "buffer_store_dwordx4 v[144:147], v142, s[12:15], s25 offen sc1\n"
    "s_mov_b32 s25, 25169920\n"
    "buffer_store_dwordx4 v[144:147], v142, s[12:15], s25 offen sc1\n"
    "s_waitcnt lgkmcnt(6)\n"
    "v_mfma_f32_16x16x32_bf16 v[172:175], v[216:219], v[252:255], v[172:175]\n"
    "ds_read_b64_tr_b16 v[244:245], v117 offset:16384\n"
    "ds_read_b64_tr_b16 v[246:247], v117 offset:16896\n"
    "v_mfma_f32_16x16x32_bf16 v[176:179], v[204:207], v[252:255], v[176:179]\n"
    "ds_read_b64_tr_b16 v[248:249], v118 offset:16384\n"
    "s_waitcnt lgkmcnt(7)\n"
    "v_mfma_f32_16x16x32_bf16 v[180:183], v[216:219], v[232:235], v[180:183]\n"
    "ds_read_b64_tr_b16 v[250:251], v118 offset:16896\n"
    "ds_read_b64_tr_b16 v[252:253], v115 offset:20480\n"
    "v_mfma_f32_16x16x32_bf16 v[184:187], v[204:207], v[232:235], v[184:187]\n"
    "s_waitcnt vmcnt(9) lgkmcnt(7)\n"
    "v_mfma_f32_16x16x32_bf16 v[156:159], v[220:223], v[236:239], v[156:159]\n"
    "ds_read_b64_tr_b16 v[254:255], v115 offset:20992\n"
    "s_waitcnt vmcnt(8)\n"
    "v_mfma_f32_16x16x32_bf16 v[160:163], v[212:215], v[236:239], v[160:163]\n"
    "ds_read_b64_tr_b16 v[232:233], v116 offset:20480\n"
    "s_waitcnt lgkmcnt(7)\n"
    "v_mfma_f32_16x16x32_bf16 v[164:167], v[220:223], v[240:243], v[164:167]\n"
    "ds_read_b64_tr_b16 v[234:235], v116 offset:20992\n"
    "ds_read_b64_tr_b16 v[236:237], v117 offset:20480\n"
    "v_mfma_f32_16x16x32_bf16 v[168:171], v[212:215], v[240:243], v[168:171]\n"
    "ds_read_b64_tr_b16 v[238:239], v117 offset:20992\n"
    "s_waitcnt lgkmcnt(8)\n"
    "v_mfma_f32_16x16x32_bf16 v[172:175], v[220:223], v[244:247], v[172:175]\n"
    "ds_read_b64_tr_b16 v[240:241], v118 offset:20480\n"
    "v_mfma_f32_16x16x32_bf16 v[176:179], v[212:215], v[244:247], v[176:179]\n"
    "ds_read_b64_tr_b16 v[242:243], v118 offset:20992\n"
    "s_waitcnt lgkmcnt(8)\n"
    "v_mfma_f32_16x16x32_bf16 v[180:183], v[220:223], v[248:251], v[180:183]\n"
    "ds_read_b64_tr_b16 v[244:245], v116 offset:24576\n"
    "v_mfma_f32_16x16x32_bf16 v[184:187], v[212:215], v[248:251], v[184:187]\n"
    "s_getpc_b64 s[84:85]\n"
    ".Lpc_41:\n"
    "s_and_b32 s85, s85, 0xffff\n"
    "s_mov_b32 s86, .Lend-.Lpc_41\n"
    "s_mov_b32 s87, 0x20000\n"
    "s_and_b32 s25, s55, 7\n"
    "s_lshl_b32 s25, s25, 12\n"
    "v_lshlrev_b32 v152, 4, v112\n"
    "buffer_load_dwordx4 v[152:155], v152, s[84:87], s25 offen\n"
    "s_waitcnt vmcnt(8) lgkmcnt(7)\n"
    "v_mfma_f32_16x16x32_bf16 v[156:159], v[188:191], v[252:255], v[156:159]\n"
    "ds_read_b64_tr_b16 v[246:247], v116 offset:25088\n"
    "s_waitcnt vmcnt(7)\n"
    "v_mfma_f32_16x16x32_bf16 v[160:163], v[196:199], v[252:255], v[160:163]\n"
    "ds_read_b64_tr_b16 v[248:249], v117 offset:24576\n"
    "s_waitcnt lgkmcnt(7)\n"
    "v_mfma_f32_16x16x32_bf16 v[164:167], v[188:191], v[232:235], v[164:167]\n"
    "ds_read_b64_tr_b16 v[250:251], v117 offset:25088\n"
    "ds_read_b64_tr_b16 v[252:253], v118 offset:24576\n"
    "v_mfma_f32_16x16x32_bf16 v[168:171], v[196:199], v[232:235], v[168:171]\n"
    "ds_read_b64_tr_b16 v[254:255], v118 offset:25088\n"
    "s_waitcnt lgkmcnt(8)\n"
    "v_mfma_f32_16x16x32_bf16 v[172:175], v[188:191], v[236:239], v[172:175]\n"
    "ds_read_b64_tr_b16 v[232:233], v116 offset:28672\n"
    "v_mfma_f32_16x16x32_bf16 v[176:179], v[196:199], v[236:239], v[176:179]\n"
    "ds_read_b64_tr_b16 v[234:235], v116 offset:29184\n"
    "s_waitcnt lgkmcnt(8)\n"
    "v_mfma_f32_16x16x32_bf16 v[180:183], v[188:191], v[240:243], v[180:183]\n"
    "ds_read_b64_tr_b16 v[236:237], v117 offset:28672\n"
    "v_mfma_f32_16x16x32_bf16 v[184:187], v[196:199], v[240:243], v[184:187]\n"
    "s_waitcnt vmcnt(6) lgkmcnt(7)\n"
    "v_mfma_f32_16x16x32_bf16 v[164:167], v[224:227], v[244:247], v[164:167]\n"
    "ds_read_b64_tr_b16 v[238:239], v117 offset:29184\n"
    "s_waitcnt vmcnt(5)\n"
    "v_mfma_f32_16x16x32_bf16 v[168:171], v[200:203], v[244:247], v[168:171]\n"
    "ds_read_b64_tr_b16 v[240:241], v118 offset:28672\n"
    "s_waitcnt lgkmcnt(7)\n"
    "v_mfma_f32_16x16x32_bf16 v[172:175], v[224:227], v[248:251], v[172:175]\n"
    "ds_read_b64_tr_b16 v[242:243], v118 offset:29184\n"
    "ds_read_b64_tr_b16 v[244:245], v115 offset:24576\n"
    "v_mfma_f32_16x16x32_bf16 v[176:179], v[200:203], v[248:251], v[176:179]\n"
    "ds_read_b64_tr_b16 v[246:247], v115 offset:25088\n"
    "s_waitcnt lgkmcnt(8)\n"
    "v_mfma_f32_16x16x32_bf16 v[180:183], v[224:227], v[252:255], v[180:183]\n"
    "ds_read_b64_tr_b16 v[248:249], v115 offset:28672\n"
    "v_mfma_f32_16x16x32_bf16 v[184:187], v[200:203], v[252:255], v[184:187]\n"
    "s_waitcnt vmcnt(4) lgkmcnt(7)\n"
    "v_mfma_f32_16x16x32_bf16 v[164:167], v[208:211], v[232:235], v[164:167]\n"
    "ds_read_b64_tr_b16 v[250:251], v115 offset:29184\n"
    "s_waitcnt vmcnt(3)\n"
    "v_mfma_f32_16x16x32_bf16 v[168:171], v[192:195], v[232:235], v[168:171]\n"
    "s_waitcnt lgkmcnt(6)\n"
    "v_mfma_f32_16x16x32_bf16 v[172:175], v[208:211], v[236:239], v[172:175]\n"
    "v_mfma_f32_16x16x32_bf16 v[176:179], v[192:195], v[236:239], v[176:179]\n"
    "v_pk_add_f32 v[148:149], v[164:165], v[166:167]\n"
    "v_pk_add_f32 v[150:151], v[168:169], v[170:171]\n"
    "v_pk_add_f32 v[148:149], v[148:149], v[150:151]\n"
    "v_cmp_u_f32 vcc, v148, v149\n"
    "s_waitcnt lgkmcnt(4)\n"
    "v_mfma_f32_16x16x32_bf16 v[180:183], v[208:211], v[240:243], v[180:183]\n"
    "ds_write_b128 v119, v[164:167] offset:0\n"
    "v_mfma_f32_16x16x32_bf16 v[184:187], v[192:195], v[240:243], v[184:187]\n"
    "s_waitcnt lgkmcnt(3)\n"
    "v_mfma_f32_16x16x32_bf16 v[156:159], v[224:227], v[244:247], v[156:159]\n"
    "ds_write_b128 v119, v[168:171] offset:1024\n"
    "v_mfma_f32_16x16x32_bf16 v[160:163], v[200:203], v[244:247], v[160:163]\n"
    "s_waitcnt lgkmcnt(2)\n"
    "v_mfma_f32_16x16x32_bf16 v[156:159], v[208:211], v[248:251], v[156:159]\n"
    "ds_write_b128 v119, v[172:175] offset:2048\n"
    "v_mfma_f32_16x16x32_bf16 v[160:163], v[192:195], v[248:251], v[160:163]\n"
    "ds_write_b128 v119, v[176:179] offset:3072\n"
    ".Lmark_p5k_42:\n"
    "ds_write_b128 v119, v[180:183] offset:4096\n"
    "ds_write_b128 v119, v[184:187] offset:5120\n"
    "s_cbranch_vccnz .Lredo_47\n"
    ".Lredook_48:\n"
    ".Lback_44:\n"
    "s_waitcnt vmcnt(1)\n"
    "s_waitcnt lgkmcnt(0)\n"
    "s_barrier\n"
    "ds_read_b128 v[164:167], v120 offset:0\n"
    "ds_read_b128 v[168:171], v120 offset:1024\n"
    "ds_read_b128 v[172:175], v121 offset:2048\n"
    "ds_read_b128 v[176:179], v121 offset:3072\n"
    "ds_read_b128 v[180:183], v122 offset:4096\n"
    "ds_read_b128 v[184:187], v122 offset:5120\n"
    "s_waitcnt lgkmcnt(3)\n"
    "v_pk_add_f32 v[164:165], v[164:165], v[172:173]\n"
    "v_pk_add_f32 v[166:167], v[166:167], v[174:175]\n"
    "s_waitcnt lgkmcnt(2)\n"
    "v_pk_add_f32 v[168:169], v[168:169], v[176:177]\n"
    "v_pk_add_f32 v[170:171], v[170:171], v[178:179]\n"
    "s_waitcnt lgkmcnt(1)\n"
    "v_pk_add_f32 v[156:157], v[156:157], v[180:181]\n"
    "v_pk_add_f32 v[158:159], v[158:159], v[182:183]\n"
    "s_waitcnt lgkmcnt(0)\n"
    "v_pk_add_f32 v[160:161], v[160:161], v[184:185]\n"
    "v_pk_add_f32 v[162:163], v[162:163], v[186:187]\n"
    "v_pk_add_f32 v[156:157], v[156:157], v[164:165]\n"
    "v_pk_add_f32 v[158:159], v[158:159], v[166:167]\n"
    "v_pk_add_f32 v[160:161], v[160:161], v[168:169]\n"
    "v_pk_add_f32 v[162:163], v[162:163], v[170:171]\n"
    "v_cvt_pk_bf16_f32 v168, v156, v157\n"
    "v_cvt_pk_bf16_f32 v169, v158, v159\n"
    "v_cvt_pk_bf16_f32 v170, v160, v161\n"
    "v_cvt_pk_bf16_f32 v171, v162, v163\n"
    "buffer_store_dwordx4 v[168:171], v140, s[12:15], 0 offen sc1\n"
    ".Lmark_p5out_49:\n"
    "s_waitcnt lgkmcnt(0)\n"
    "s_barrier\n"
    "v_and_b32 v238, 15, v112\n"
    "v_bfe_u32 v239, v112, 4, 2\n"
    "v_mad_u32_u24 v240, v239, 14, v238\n"
    "v_mul_u32_u24 v241, 37, v240\n"
    "v_lshrrev_b32 v241, 8, v241\n"
    "v_mul_u32_u24 v242, 7, v241\n"
    "v_sub_u32 v242, v240, v242\n"
    "v_mul_u32_u24 v243, 7168, v241\n"
    "s_and_b32 s74, s55, 15\n"
    "s_lshl_b32 s75, s74, 6\n"
    "s_mul_i32 s76, s74, 48\n"
    "s_add_u32 s76, s76, 960\n"
    "v_mov_b32 v241, s75\n"
    "v_mov_b32 v240, s76\n"
    "v_cmp_gt_u32 vcc, 4, v242\n"
    "v_lshl_add_u32 v243, v242, 4, v243\n"
    "s_nop 0\n"
    "v_cndmask_b32 v241, v240, v241, vcc\n"
    "v_add_u32 v243, v243, v241\n"
    "v_mov_b32 v240, 1073741824\n"
    "v_cmp_gt_u32 vcc, 14, v238\n"
    "s_lshl_b32 s74, s54, 2\n"
    "s_add_u32 s74, s74, s52\n"
    "s_lshl_b32 s74, s74, 5\n"
    "s_lshr_b32 s75, s55, 4\n"
    "s_add_u32 s74, s74, s75\n"
    "s_mul_i32 s74, s74, 1792\n"
    "s_add_u32 s70, s74, 14680064\n"
    "s_add_u32 s71, s70, 3584\n"
    "v_cndmask_b32 v243, v240, v243, vcc\n"
    "s_mov_b32 s64, 0x00003f80\n"
    "s_mov_b32 s65, 0x3f800000\n"
    "v_mov_b32 v245, v243\n"
    "v_and_b32 v238, 15, v112\n"
    "v_bfe_u32 v239, v112, 4, 2\n"
    "v_cvt_f32_u32 v240, v238\n"
    "v_lshlrev_b32 v241, 2, v239\n"
    "v_cvt_f32_u32 v241, v241\n"
    "v_mul_f32 v242, 0x3d000000, v240\n"
    "v_mul_f32 v190, v241, v242\n"
    "v_add_f32 v191, v190, v242\n"
    "v_add_f32 v192, v191, v242\n"
    "v_add_f32 v193, v192, v242\n"
    "v_sin_f32 v198, v190\n"
    "v_sin_f32 v199, v191\n"
    "v_sin_f32 v200, v192\n"
    "v_sin_f32 v201, v193\n"
    "v_cos_f32 v194, v190\n"
    "v_cos_f32 v195, v191\n"
    "v_cos_f32 v196, v192\n"
    "v_cos_f32 v197, v193\n"
    "v_and_b32 v243, 1, v238\n"
    "v_mul_u32_u24 v243, 0x8000, v243\n"
    "v_lshl_or_b32 v243, v243, 16, v243\n"
    "v_cvt_pk_bf16_f32 v114, v194, v195\n"
    "v_cvt_pk_bf16_f32 v118, -v198, -v199\n"
    "v_cvt_pk_bf16_f32 v115, v196, v197\n"
    "v_cvt_pk_bf16_f32 v119, -v200, -v201\n"
    "v_xor_b32 v116, v114, v243\n"
    "v_xor_b32 v117, v115, v243\n"
    "v_xor_b32 v120, v118, v243\n"
    "v_xor_b32 v121, v119, v243\n"
    "v_xor_b32 v122, 0x80000000, v114\n"
    "v_xor_b32 v123, 0x80000000, v115\n"
    "v_xor_b32 v124, 0x80000000, v116\n"
    "v_xor_b32 v125, 0x80000000, v117\n"
    "v_xor_b32 v126, 0x80000000, v118\n"
    "v_xor_b32 v127, 0x80000000, v119\n"
    "v_xor_b32 v128, 0x80000000, v120\n"
    "v_xor_b32 v129, 0x80000000, v121\n"
    "v_mul_f32 v242, 0x3b124925, v240\n"
    "v_mul_f32 v190, v241, v242\n"
    "v_add_f32 v191, v190, v242\n"
    "v_add_f32 v192, v191, v242\n"
    "v_add_f32 v193, v192, v242\n"
    "v_sin_f32 v138, v190\n"
    "v_sin_f32 v139, v191\n"
    "v_sin_f32 v140, v192\n"
    "v_sin_f32 v141, v193\n"
    "v_cos_f32 v130, v190\n"
    "v_cos_f32 v131, v191\n"
    "v_cos_f32 v132, v192\n"
    "v_cos_f32 v133, v193\n"
    "v_add_f32 v242, 0x41800000, v240\n"
    "v_mul_f32 v242, 0x3b124925, v242\n"
    "v_mul_f32 v190, v241, v242\n"
    "v_add_f32 v191, v190, v242\n"
    "v_add_f32 v192, v191, v242\n"
    "v_add_f32 v193, v192, v242\n"
    "v_sin_f32 v142, v190\n"
    "v_sin_f32 v143, v191\n"
    "v_sin_f32 v144, v192\n"
    "v_sin_f32 v145, v193\n"
    "v_cos_f32 v134, v190\n"
    "v_cos_f32 v135, v191\n"
    "v_cos_f32 v136, v192\n"
    "v_cos_f32 v137, v193\n"
    "v_lshrrev_b32 v242, 1, v238\n"
    "v_cvt_f32_u32 v242, v242\n"
    "v_mul_f32 v242, 0x3d924925, v242\n"
    "v_and_b32 v190, 1, v238\n"
    "v_cvt_f32_u32 v190, v190\n"
    "v_mul_f32 v190, 0x3e800000, v190\n"
    "v_fmac_f32 v190, v241, v242\n"
    "v_add_f32 v191, v190, v242\n"
    "v_add_f32 v192, v191, v242\n"
    "v_add_f32 v193, v192, v242\n"
    "v_sin_f32 v198, v190\n"
    "v_sin_f32 v199, v191\n"
    "v_sin_f32 v200, v192\n"
    "v_sin_f32 v201, v193\n"
    "v_cos_f32 v194, v190\n"
    "v_cos_f32 v195, v191\n"
    "v_cos_f32 v196, v192\n"
    "v_cos_f32 v197, v193\n"
    "v_cmp_ne_u32 vcc, 3, v239\n"
    "v_cvt_pk_bf16_f32 v146, v194, v198\n"
    "v_cvt_pk_bf16_f32 v147, v195, v199\n"
    "v_cvt_pk_bf16_f32 v148, v196, v200\n"
    "v_cvt_pk_bf16_f32 v149, v197, v201\n"
    "v_cndmask_b32 v148, 0, v148, vcc\n"
    "v_cndmask_b32 v149, 0, v149, vcc\n"
    "v_lshlrev_b32 v150, 16, v96\n"
    "v_and_b32 v151, 0xffff0000, v96\n"
    "s_waitcnt vmcnt(1)\n"
    "v_lshlrev_b32 v152, 16, v97\n"
    "v_and_b32 v153, 0xffff0000, v97\n"
    "v_lshlrev_b32 v154, 16, v98\n"
    "v_and_b32 v155, 0xffff0000, v98\n"
    "v_lshlrev_b32 v156, 16, v99\n"
    "v_and_b32 v157, 0xffff0000, v99\n"
    "v_lshlrev_b32 v158, 16, v100\n"
    "v_and_b32 v159, 0xffff0000, v100\n"
    "v_lshlrev_b32 v160, 16, v101\n"
    "v_and_b32 v161, 0xffff0000, v101\n"
    "v_lshlrev_b32 v162, 16, v102\n"
    "v_and_b32 v163, 0xffff0000, v102\n"
    "v_lshlrev_b32 v164, 16, v103\n"
    "v_and_b32 v165, 0xffff0000, v103\n"
    "v_and_b32 v246, 15, v112\n"
    "v_bfe_u32 v249, v112, 4, 2\n"
    "s_mul_i32 s74, s52, 2112\n"
    "s_add_u32 s74, s74, 131072\n"
    "v_lshl_add_u32 v246, v249, 6, v246\n"
    "v_lshl_add_u32 v246, v246, 3, s74\n"
    "v_and_b32 v247, 1, v112\n"
    "v_mul_u32_u24 v247, 4224, v247\n"
    "v_lshrrev_b32 v248, 1, v112\n"
    "v_lshl_add_u32 v247, v248, 3, v247\n"
    "v_add_u32 v247, 131072, v247\n"
    "s_lshl_b32 s74, s53, 13\n"
    "s_add_u32 s74, s74, 16777216\n"
    "s_waitcnt vmcnt(0)\n"
    "buffer_load_dwordx4 v[166:169], v245, s[12:15], s70 offen sc1\n"
    "buffer_load_dwordx4 v[170:173], v245, s[12:15], s71 offen sc1\n"
    "v_lshl_add_u32 v248, v112, 4, s74\n"
    "s_mov_b32 s79, 4096\n"
    "s_mov_b32 s80, 8192\n"
    "s_mov_b32 s81, 12288\n"
    "v_cmp_gt_u32 vcc, 194, v112\n"
    "v_mov_b32 v249, 1073741824\n"
    "s_nop 0\n"
    "v_cndmask_b32 v249, v249, v248, vcc\n"
    "s_waitcnt vmcnt(1)\n"
    "v_max3_u32 v174, v166, v167, v168\n"
    "s_waitcnt vmcnt(0)\n"
    "v_max3_u32 v174, v174, v169, v170\n"
    "v_max3_u32 v174, v174, v171, v172\n"
    "v_max_u32 v174, v174, v173\n"
    "v_cmp_eq_u32 vcc, -1, v174\n"
    "s_cbranch_vccnz .Lretry_50\n"
    ".Ly_51:\n"
    "v_dot2_f32_bf16 v150, v166, s64, v150\n"
    "v_dot2_f32_bf16 v151, v166, s65, v151\n"
    "v_dot2_f32_bf16 v152, v167, s64, v152\n"
    "v_dot2_f32_bf16 v153, v167, s65, v153\n"
    "v_dot2_f32_bf16 v154, v168, s64, v154\n"
    "v_dot2_f32_bf16 v155, v168, s65, v155\n"
    "v_dot2_f32_bf16 v156, v169, s64, v156\n"
    "v_dot2_f32_bf16 v157, v169, s65, v157\n"
    "v_dot2_f32_bf16 v158, v170, s64, v158\n"
    "v_dot2_f32_bf16 v159, v170, s65, v159\n"
    "v_dot2_f32_bf16 v160, v171, s64, v160\n"
    "v_dot2_f32_bf16 v161, v171, s65, v161\n"
    "v_dot2_f32_bf16 v162, v172, s64, v162\n"
    "v_dot2_f32_bf16 v163, v172, s65, v163\n"
    "v_dot2_f32_bf16 v164, v173, s64, v164\n"
    "v_dot2_f32_bf16 v165, v173, s65, v165\n"
    ".Lmark_p1in_52:\n"
    "v_pk_add_f32 v[174:175], v[150:151], v[152:153]\n"
    "v_pk_mul_f32 v[176:177], v[150:151], v[150:151]\n"
    "v_pk_add_f32 v[178:179], v[158:159], v[160:161]\n"
    "v_pk_mul_f32 v[180:181], v[158:159], v[158:159]\n"
    "v_pk_add_f32 v[174:175], v[174:175], v[154:155]\n"
    "v_pk_add_f32 v[178:179], v[178:179], v[162:163]\n"
    "v_pk_add_f32 v[174:175], v[174:175], v[156:157]\n"
    "v_pk_add_f32 v[178:179], v[178:179], v[164:165]\n"
    "v_pk_fma_f32 v[176:177], v[152:153], v[152:153], v[176:177]\n"
    "v_pk_fma_f32 v[180:181], v[160:161], v[160:161], v[180:181]\n"
    "v_pk_fma_f32 v[176:177], v[154:155], v[154:155], v[176:177]\n"
    "v_pk_fma_f32 v[180:181], v[162:163], v[162:163], v[180:181]\n"
    "v_pk_fma_f32 v[176:177], v[156:157], v[156:157], v[176:177]\n"
    "v_pk_fma_f32 v[180:181], v[164:165], v[164:165], v[180:181]\n"
    "v_add_f32 v174, v174, v175\n"
    "v_add_f32 v178, v178, v179\n"
    "v_add_f32 v176, v176, v177\n"
    "v_add_f32 v180, v180, v181\n"
    "s_nop 0\n"
    "v_permlane32_swap_b32 v174, v176\n"
    "v_permlane32_swap_b32 v178, v180\n"
    "v_add_f32 v174, v174, v176\n"
    "v_add_f32 v178, v178, v180\n"
    "s_nop 1\n"
    "v_permlane16_swap_b32 v174, v178\n"
    "v_add_f32 v174, v174, v178\n"
    "s_nop 1\n"
    "v_add_f32_dpp v174, v174, v174 row_ror:8 row_mask:0xf bank_mask:0xf\n"
    "s_nop 1\n"
    "v_add_f32_dpp v174, v174, v174 row_ror:4 row_mask:0xf bank_mask:0xf\n"
    "s_nop 1\n"
    "v_add_f32_dpp v174, v174, v174 row_ror:2 row_mask:0xf bank_mask:0xf\n"
    "s_nop 1\n"
    "v_add_f32_dpp v174, v174, v174 row_ror:1 row_mask:0xf bank_mask:0xf\n"
    "v_mul_f32 v174, 0x3b124925, v174\n"
    "v_mov_b32 v175, v174\n"
    "s_nop 1\n"
    "v_permlane32_swap_b32 v174, v175\n"
    "v_fma_f32 v175, -v174, v174, v175\n"
    "v_max_f32 v175, 0, v175\n"
    "v_add_f32 v176, 0x3727c5ac, v175\n"
    "v_rsq_f32 v176, v176\n"
    "s_nop 0\n"
    "v_mul_f32 v174, -v174, v176\n"
    "v_readlane_b32 s64, v176, 0\n"
    "v_readlane_b32 s66, v176, 16\n"
    "v_readlane_b32 s65, v174, 0\n"
    "v_readlane_b32 s67, v174, 16\n"
    "s_nop 0\n"
    "v_pk_fma_f32 v[190:191], v[150:151], s[64:65], s[64:65] op_sel:[0,0,1] op_sel_hi:[1,0,1]\n"
    "v_pk_fma_f32 v[192:193], v[152:153], s[64:65], s[64:65] op_sel:[0,0,1] op_sel_hi:[1,0,1]\n"
    "v_pk_fma_f32 v[194:195], v[154:155], s[64:65], s[64:65] op_sel:[0,0,1] op_sel_hi:[1,0,1]\n"
    "v_pk_fma_f32 v[196:197], v[156:157], s[64:65], s[64:65] op_sel:[0,0,1] op_sel_hi:[1,0,1]\n"
    "v_pk_fma_f32 v[198:199], v[158:159], s[66:67], s[66:67] op_sel:[0,0,1] op_sel_hi:[1,0,1]\n"
    "v_pk_fma_f32 v[200:201], v[160:161], s[66:67], s[66:67] op_sel:[0,0,1] op_sel_hi:[1,0,1]\n"
    "v_pk_fma_f32 v[202:203], v[162:163], s[66:67], s[66:67] op_sel:[0,0,1] op_sel_hi:[1,0,1]\n"
    "v_pk_fma_f32 v[204:205], v[164:165], s[66:67], s[66:67] op_sel:[0,0,1] op_sel_hi:[1,0,1]\n"
    "v_cvt_pk_bf16_f32 v182, v190, v191\n"
    "v_cvt_pk_bf16_f32 v183, v192, v193\n"
    "v_cvt_pk_bf16_f32 v184, v194, v195\n"
    "v_cvt_pk_bf16_f32 v185, v196, v197\n"
    "v_cvt_pk_bf16_f32 v186, v198, v199\n"
    "v_cvt_pk_bf16_f32 v187, v200, v201\n"
    "v_cvt_pk_bf16_f32 v188, v202, v203\n"
    "v_cvt_pk_bf16_f32 v189, v204, v205\n"
    "v_mfma_f32_16x16x32_bf16 v[190:193], v[182:185], v[114:117], 0\n"
    "v_mfma_f32_16x16x32_bf16 v[194:197], v[182:185], v[118:121], 0\n"
    "v_mfma_f32_16x16x32_bf16 v[198:201], v[182:185], v[122:125], 0\n"
    "v_mfma_f32_16x16x32_bf16 v[202:205], v[182:185], v[126:129], 0\n"
    "v_mfma_f32_16x16x32_bf16 v[206:209], v[186:189], v[114:117], 0\n"
    "v_mfma_f32_16x16x32_bf16 v[210:213], v[186:189], v[118:121], 0\n"
    "v_mfma_f32_16x16x32_bf16 v[214:217], v[186:189], v[122:125], 0\n"
    "v_mfma_f32_16x16x32_bf16 v[218:221], v[186:189], v[126:129], 0\n"
    "v_pk_mul_f32 v[166:167], v[190:191], v[130:131]\n"
    "v_pk_mul_f32 v[170:171], v[194:195], v[130:131]\n"
    "v_pk_mul_f32 v[168:169], v[192:193], v[132:133]\n"
    "v_pk_mul_f32 v[172:173], v[196:197], v[132:133]\n"
    "v_pk_fma_f32 v[166:167], v[194:195], v[138:139], v[166:167]\n"
    "v_pk_fma_f32 v[170:171], v[190:191], v[138:139], v[170:171] neg_lo:[1,0,0] neg_hi:[1,0,0]\n"
    "v_pk_fma_f32 v[168:169], v[196:197], v[140:141], v[168:169]\n"
    "v_pk_fma_f32 v[172:173], v[192:193], v[140:141], v[172:173] neg_lo:[1,0,0] neg_hi:[1,0,0]\n"
    "v_cvt_pk_bf16_f32 v222, v166, v170\n"
    "v_cvt_pk_bf16_f32 v223, v167, v171\n"
    "v_cvt_pk_bf16_f32 v224, v168, v172\n"
    "v_cvt_pk_bf16_f32 v225, v169, v173\n"
    "v_pk_mul_f32 v[174:175], v[198:199], v[134:135]\n"
    "v_pk_mul_f32 v[178:179], v[202:203], v[134:135]\n"
    "v_pk_mul_f32 v[176:177], v[200:201], v[136:137]\n"
    "v_pk_mul_f32 v[180:181], v[204:205], v[136:137]\n"
    "v_pk_fma_f32 v[174:175], v[202:203], v[142:143], v[174:175]\n"
    "v_pk_fma_f32 v[178:179], v[198:199], v[142:143], v[178:179] neg_lo:[1,0,0] neg_hi:[1,0,0]\n"
    "v_pk_fma_f32 v[176:177], v[204:205], v[144:145], v[176:177]\n"
    "v_pk_fma_f32 v[180:181], v[200:201], v[144:145], v[180:181] neg_lo:[1,0,0] neg_hi:[1,0,0]\n"
    "v_cvt_pk_bf16_f32 v226, v174, v178\n"
    "v_cvt_pk_bf16_f32 v227, v175, v179\n"
    "v_cvt_pk_bf16_f32 v228, v176, v180\n"
    "v_cvt_pk_bf16_f32 v229, v177, v181\n"
    "v_pk_mul_f32 v[174:175], v[206:207], v[130:131]\n"
    "v_pk_mul_f32 v[178:179], v[210:211], v[130:131]\n"
    "v_pk_mul_f32 v[176:177], v[208:209], v[132:133]\n"
    "v_pk_mul_f32 v[180:181], v[212:213], v[132:133]\n"
    "v_pk_fma_f32 v[174:175], v[210:211], v[138:139], v[174:175]\n"
    "v_pk_fma_f32 v[178:179], v[206:207], v[138:139], v[178:179] neg_lo:[1,0,0] neg_hi:[1,0,0]\n"
    "v_pk_fma_f32 v[176:177], v[212:213], v[140:141], v[176:177]\n"
    "v_pk_fma_f32 v[180:181], v[208:209], v[140:141], v[180:181] neg_lo:[1,0,0] neg_hi:[1,0,0]\n"
    "v_cvt_pk_bf16_f32 v230, v174, v178\n"
    "v_cvt_pk_bf16_f32 v231, v175, v179\n"
    "v_cvt_pk_bf16_f32 v232, v176, v180\n"
    "v_cvt_pk_bf16_f32 v233, v177, v181\n"
    "v_pk_mul_f32 v[166:167], v[214:215], v[134:135]\n"
    "v_pk_mul_f32 v[170:171], v[218:219], v[134:135]\n"
    "v_pk_mul_f32 v[168:169], v[216:217], v[136:137]\n"
    "v_pk_mul_f32 v[172:173], v[220:221], v[136:137]\n"
    "v_pk_fma_f32 v[166:167], v[218:219], v[142:143], v[166:167]\n"
    "v_pk_fma_f32 v[170:171], v[214:215], v[142:143], v[170:171] neg_lo:[1,0,0] neg_hi:[1,0,0]\n"
    "v_pk_fma_f32 v[168:169], v[220:221], v[144:145], v[168:169]\n"
    "v_pk_fma_f32 v[172:173], v[216:217], v[144:145], v[172:173] neg_lo:[1,0,0] neg_hi:[1,0,0]\n"
    "v_cvt_pk_bf16_f32 v234, v166, v170\n"
    "v_cvt_pk_bf16_f32 v235, v167, v171\n"
    "v_cvt_pk_bf16_f32 v236, v168, v172\n"
    "v_cvt_pk_bf16_f32 v237, v169, v173\n"
    "v_mfma_f32_16x16x32_bf16 v[190:193], v[146:149], v[222:225], 0\n"
    "v_mfma_f32_16x16x32_bf16 v[198:201], v[146:149], v[226:229], 0\n"
    "v_mfma_f32_16x16x32_bf16 v[194:197], v[146:149], v[230:233], 0\n"
    "v_mfma_f32_16x16x32_bf16 v[202:205], v[146:149], v[234:237], 0\n"
    "v_cvt_pk_bf16_f32 v222, v190, v191\n"
    "s_nop 2\n"
    "v_cvt_pk_bf16_f32 v223, v194, v195\n"
    "v_cvt_pk_bf16_f32 v224, v192, v193\n"
    "v_cvt_pk_bf16_f32 v225, v196, v197\n"
    "v_cvt_pk_bf16_f32 v226, v198, v199\n"
    "v_cvt_pk_bf16_f32 v227, v202, v203\n"
    "v_cvt_pk_bf16_f32 v228, v200, v201\n"
    "v_cvt_pk_bf16_f32 v229, v204, v205\n"
    "ds_write_b64 v246, v[222:223] offset:0\n"
    "ds_write_b64 v246, v[224:225] offset:256\n"
    "ds_write_b64 v246, v[226:227] offset:128\n"
    "ds_write_b64 v246, v[228:229] offset:384\n"
    "s_waitcnt lgkmcnt(0)\n"
    "s_barrier\n"
    "ds_read_b64 v[190:191], v247 offset:0\n"
    "ds_read_b64 v[192:193], v247 offset:2112\n"
    "ds_read_b64 v[194:195], v247 offset:1024\n"
    "ds_read_b64 v[196:197], v247 offset:3136\n"
    "s_waitcnt lgkmcnt(2)\n"
    "buffer_store_dwordx4 v[190:193], v248, s[12:15], 0 offen sc1\n"
    "s_waitcnt lgkmcnt(0)\n"
    "buffer_store_dwordx4 v[194:197], v249, s[12:15], s79 offen sc1\n"
    ".Lmark_p1out_53:\n"
    "v_cvt_pk_bf16_f32 v96, v150, v151\n"
    "v_cvt_pk_bf16_f32 v97, v152, v153\n"
    "v_cvt_pk_bf16_f32 v98, v154, v155\n"
    "v_cvt_pk_bf16_f32 v99, v156, v157\n"
    "v_cvt_pk_bf16_f32 v100, v158, v159\n"
    "v_cvt_pk_bf16_f32 v101, v160, v161\n"
    "v_cvt_pk_bf16_f32 v102, v162, v163\n"
    "v_cvt_pk_bf16_f32 v103, v164, v165\n"
    "v_and_b32 v113, 63, v112\n"
    "v_lshlrev_b32 v113, 4, v113\n"
    "v_add_u32 v113, 139776, v113\n"
    "s_lshl_b32 s74, s52, 11\n"
    "s_add_u32 s74, s74, 8192\n"
    "v_add_u32 v255, s74, v113\n"
    "v_and_b32 v253, 15, v112\n"
    "v_lshlrev_b32 v253, 5, v253\n"
    "v_bfe_u32 v248, v112, 4, 1\n"
    "v_lshl_add_u32 v253, v248, 4, v253\n"
    "v_bfe_u32 v248, v112, 5, 3\n"
    "v_lshl_add_u32 v253, v248, 10, v253\n"
    "s_lshl_b32 s70, s53, 13\n"
    "s_add_u32 s70, s70, 18874368\n"
    "s_cmp_eq_u32 s53, s8\n"
    "s_cselect_b32 s70, s70, 1073741824\n"
    "v_bfe_u32 v114, v112, 1, 5\n"
    "v_lshlrev_b32 v114, 13, v114\n"
    "v_and_b32 v252, 1, v112\n"
    "v_lshl_add_u32 v114, v252, 4, v114\n"
    "v_bfe_u32 v115, v112, 1, 4\n"
    "v_lshlrev_b32 v115, 4, v115\n"
    "v_lshl_add_u32 v115, v252, 8, v115\n"
    "v_bfe_u32 v252, v112, 5, 1\n"
    "v_lshl_add_u32 v115, v252, 10, v115\n"
    "s_lshl_b32 s74, s52, 11\n"
    "s_add_u32 s74, s74, 139776\n"
    "v_add_u32 v115, s74, v115\n"
    "s_lshl_b32 s74, s52, 19\n"
    "s_lshl_b32 s75, s8, 5\n"
    "s_add_u32 s74, s74, s75\n"
    "s_add_u32 s78, s74, 16777216\n"
    "s_add_u32 s79, s78, 262144\n"
    "v_and_b32 v212, 15, v112\n"
    "v_bfe_u32 v213, v112, 4, 2\n"
    "v_cvt_f32_u32 v214, v212\n"
    "v_lshlrev_b32 v215, 2, v213\n"
    "v_cvt_f32_u32 v215, v215\n"
    "v_and_b32 v217, 1, v212\n"
    "v_mul_u32_u24 v217, 0x8000, v217\n"
    "v_lshl_or_b32 v217, v217, 16, v217\n"
    "s_lshl_b32 s71, s52, 4\n"
    "v_add_u32 v218, s71, v212\n"
    "v_cvt_f32_u32 v218, v218\n"
    "v_mul_f32 v216, 0x3c800000, v218\n"
    "v_mul_f32 v236, v215, v216\n"
    "v_add_f32 v237, v236, v216\n"
    "v_add_f32 v238, v237, v216\n"
    "v_add_f32 v239, v238, v216\n"
    "v_sin_f32 v244, v236\n"
    "v_sin_f32 v245, v237\n"
    "v_sin_f32 v246, v238\n"
    "v_sin_f32 v247, v239\n"
    "v_cos_f32 v240, v236\n"
    "v_cos_f32 v241, v237\n"
    "v_cos_f32 v242, v238\n"
    "v_cos_f32 v243, v239\n"
    "v_cvt_pk_bf16_f32 v116, v240, v244\n"
    "v_cvt_pk_bf16_f32 v120, -v244, v240\n"
    "v_cvt_pk_bf16_f32 v117, v241, v245\n"
    "v_cvt_pk_bf16_f32 v121, -v245, v241\n"
    "v_cvt_pk_bf16_f32 v118, v242, v246\n"
    "v_cvt_pk_bf16_f32 v122, -v246, v242\n"
    "v_cvt_pk_bf16_f32 v119, v243, v247\n"
    "v_cvt_pk_bf16_f32 v123, -v247, v243\n"
    "v_mul_f32 v236, v215, v216\n"
    "v_fmamk_f32 v236, v216, 0x41800000, v236\n"
    "v_add_f32 v237, v236, v216\n"
    "v_add_f32 v238, v237, v216\n"
    "v_add_f32 v239, v238, v216\n"
    "v_sin_f32 v244, v236\n"
    "v_sin_f32 v245, v237\n"
    "v_sin_f32 v246, v238\n"
    "v_sin_f32 v247, v239\n"
    "v_cos_f32 v240, v236\n"
    "v_cos_f32 v241, v237\n"
    "v_cos_f32 v242, v238\n"
    "v_cos_f32 v243, v239\n"
    "v_cvt_pk_bf16_f32 v124, v240, v244\n"
    "v_cvt_pk_bf16_f32 v128, -v244, v240\n"
    "v_cvt_pk_bf16_f32 v125, v241, v245\n"
    "v_cvt_pk_bf16_f32 v129, -v245, v241\n"
    "v_cvt_pk_bf16_f32 v126, v242, v246\n"
    "v_cvt_pk_bf16_f32 v130, -v246, v242\n"
    "v_cvt_pk_bf16_f32 v127, v243, v247\n"
    "v_cvt_pk_bf16_f32 v131, -v247, v243\n"
    "v_xor_b32 v132, v116, v217\n"
    "v_xor_b32 v133, v117, v217\n"
    "v_xor_b32 v134, v118, v217\n"
    "v_xor_b32 v135, v119, v217\n"
    "v_xor_b32 v136, v120, v217\n"
    "v_xor_b32 v137, v121, v217\n"
    "v_xor_b32 v138, v122, v217\n"
    "v_xor_b32 v139, v123, v217\n"
    "v_xor_b32 v140, v124, v217\n"
    "v_xor_b32 v141, v125, v217\n"
    "v_xor_b32 v142, v126, v217\n"
    "v_xor_b32 v143, v127, v217\n"
    "v_xor_b32 v144, v128, v217\n"
    "v_xor_b32 v145, v129, v217\n"
    "v_xor_b32 v146, v130, v217\n"
    "v_xor_b32 v147, v131, v217\n"
    "v_mul_f32 v216, 0x3d000000, v214\n"
    "v_mul_f32 v236, v215, v216\n"
    "v_add_f32 v237, v236, v216\n"
    "v_add_f32 v238, v237, v216\n"
    "v_add_f32 v239, v238, v216\n"
    "v_sin_f32 v244, v236\n"
    "v_sin_f32 v245, v237\n"
    "v_sin_f32 v246, v238\n"
    "v_sin_f32 v247, v239\n"
    "v_cos_f32 v240, v236\n"
    "v_cos_f32 v241, v237\n"
    "v_cos_f32 v242, v238\n"
    "v_cos_f32 v243, v239\n"
    "v_cvt_pk_bf16_f32 v148, v240, v244\n"
    "v_cvt_pk_bf16_f32 v152, -v244, v240\n"
    "v_cvt_pk_bf16_f32 v149, v241, v245\n"
    "v_cvt_pk_bf16_f32 v153, -v245, v241\n"
    "v_cvt_pk_bf16_f32 v150, v242, v246\n"
    "v_cvt_pk_bf16_f32 v154, -v246, v242\n"
    "v_cvt_pk_bf16_f32 v151, v243, v247\n"
    "v_cvt_pk_bf16_f32 v155, -v247, v243\n"
    "v_xor_b32 v156, v148, v217\n"
    "v_xor_b32 v157, v149, v217\n"
    "v_xor_b32 v158, v150, v217\n"
    "v_xor_b32 v159, v151, v217\n"
    "v_xor_b32 v160, v152, v217\n"
    "v_xor_b32 v161, v153, v217\n"
    "v_xor_b32 v162, v154, v217\n"
    "v_xor_b32 v163, v155, v217\n"
    "v_mov_b32 v164, v148\n"
    "v_xor_b32 v165, s73, v149\n"
    "v_mov_b32 v166, v150\n"
    "v_xor_b32 v167, s73, v151\n"
    "v_mov_b32 v168, v152\n"
    "v_xor_b32 v169, s73, v153\n"
    "v_mov_b32 v170, v154\n"
    "v_xor_b32 v171, s73, v155\n"
    "v_mov_b32 v172, v156\n"
    "v_xor_b32 v173, s73, v157\n"
    "v_mov_b32 v174, v158\n"
    "v_xor_b32 v175, s73, v159\n"
    "v_mov_b32 v176, v160\n"
    "v_xor_b32 v177, s73, v161\n"
    "v_mov_b32 v178, v162\n"
    "v_xor_b32 v179, s73, v163\n"
    "v_mul_f32 v216, 0x3a000000, v218\n"
    "v_mul_f32 v236, v215, v216\n"
    "v_add_f32 v237, v236, v216\n"
    "v_add_f32 v238, v237, v216\n"
    "v_add_f32 v239, v238, v216\n"
    "v_sin_f32 v188, v236\n"
    "v_sin_f32 v189, v237\n"
    "v_sin_f32 v190, v238\n"
    "v_sin_f32 v191, v239\n"
    "s_waitcnt vmcnt(0)\n"
    "buffer_load_dwordx4 v[220:223], v114, s[12:15], s78 offen sc1\n"
    "buffer_load_dwordx4 v[224:227], v114, s[12:15], s79 offen sc1\n"
    "v_cos_f32 v180, v236\n"
    "v_cos_f32 v181, v237\n"
    "v_cos_f32 v182, v238\n"
    "v_cos_f32 v183, v239\n"
    "v_mul_f32 v236, v215, v216\n"
    "v_fmamk_f32 v236, v216, 0x41800000, v236\n"
    "v_add_f32 v237, v236, v216\n"
    "v_add_f32 v238, v237, v216\n"
    "v_add_f32 v239, v238, v216\n"
    "v_sin_f32 v192, v236\n"
    "v_sin_f32 v193, v237\n"
    "v_sin_f32 v194, v238\n"
    "v_sin_f32 v195, v239\n"
    "v_cos_f32 v184, v236\n"
    "v_cos_f32 v185, v237\n"
    "v_cos_f32 v186, v238\n"
    "v_cos_f32 v187, v239\n"
    "v_cvt_f32_u32 v218, s71\n"
    "v_add_f32 v218, v218, v215\n"
    "v_mul_f32 v216, 0x3a000000, v214\n"
    "v_mul_f32 v236, v218, v216\n"
    "v_add_f32 v237, v236, v216\n"
    "v_add_f32 v238, v237, v216\n"
    "v_add_f32 v239, v238, v216\n"
    "v_sin_f32 v204, v236\n"
    "v_sin_f32 v205, v237\n"
    "v_sin_f32 v206, v238\n"
    "v_sin_f32 v207, v239\n"
    "v_cos_f32 v196, v236\n"
    "v_cos_f32 v197, v237\n"
    "v_cos_f32 v198, v238\n"
    "v_cos_f32 v199, v239\n"
    "v_add_f32 v214, 0x41800000, v214\n"
    "v_mul_f32 v216, 0x3a000000, v214\n"
    "v_mul_f32 v236, v218, v216\n"
    "v_add_f32 v237, v236, v216\n"
    "v_add_f32 v238, v237, v216\n"
    "v_add_f32 v239, v238, v216\n"
    "v_sin_f32 v208, v236\n"
    "v_sin_f32 v209, v237\n"
    "v_sin_f32 v210, v238\n"
    "v_sin_f32 v211, v239\n"
    "v_cos_f32 v200, v236\n"
    "v_cos_f32 v201, v237\n"
    "v_cos_f32 v202, v238\n"
    "v_cos_f32 v203, v239\n"
    "v_pk_mul_f32 v[196:197], v[196:197], s[72:73] op_sel_hi:[1,0]\n"
    "v_pk_mul_f32 v[198:199], v[198:199], s[72:73] op_sel_hi:[1,0]\n"
    "v_pk_mul_f32 v[200:201], v[200:201], s[72:73] op_sel_hi:[1,0]\n"
    "v_pk_mul_f32 v[202:203], v[202:203], s[72:73] op_sel_hi:[1,0]\n"
    "v_pk_mul_f32 v[204:205], v[204:205], s[72:73] op_sel_hi:[1,0]\n"
    "v_pk_mul_f32 v[206:207], v[206:207], s[72:73] op_sel_hi:[1,0]\n"
    "v_pk_mul_f32 v[208:209], v[208:209], s[72:73] op_sel_hi:[1,0]\n"
    "v_pk_mul_f32 v[210:211], v[210:211], s[72:73] op_sel_hi:[1,0]\n"
    "s_waitcnt vmcnt(1)\n"
    "v_max3_u32 v236, v220, v221, v222\n"
    "s_waitcnt vmcnt(0)\n"
    "v_max3_u32 v236, v236, v223, v224\n"
    "v_max3_u32 v236, v236, v225, v226\n"
    "v_max_u32 v236, v236, v227\n"
    "v_cmp_eq_u32 vcc, -1, v236\n"
    "s_cbranch_vccnz .Lretry_54\n"
    ".Lx1_55:\n"
    "ds_write_b128 v115, v[220:223] offset:0\n"
    "ds_write_b128 v115, v[224:227] offset:512\n"
    ".Lmark_p2in_56:\n"
    "s_waitcnt lgkmcnt(0)\n"
    "s_barrier\n"
    "ds_read_b128 v[212:215], v113 offset:0\n"
    "ds_read_b128 v[216:219], v113 offset:2048\n"
    "ds_read_b128 v[220:223], v113 offset:4096\n"
    "ds_read_b128 v[224:227], v113 offset:6144\n"
    "ds_read_b128 v[228:231], v113 offset:1024\n"
    "ds_read_b128 v[232:235], v113 offset:3072\n"
    "s_waitcnt lgkmcnt(5)\n"
    "v_mfma_f32_16x16x32_bf16 v[236:239], v[212:215], v[116:119], 0\n"
    "v_mfma_f32_16x16x32_bf16 v[240:243], v[212:215], v[120:123], 0\n"
    "ds_read_b128 v[212:215], v113 offset:5120\n"
    "s_waitcnt lgkmcnt(5)\n"
    "v_mfma_f32_16x16x32_bf16 v[236:239], v[216:219], v[124:127], v[236:239]\n"
    "v_mfma_f32_16x16x32_bf16 v[240:243], v[216:219], v[128:131], v[240:243]\n"
    "ds_read_b128 v[216:219], v113 offset:7168\n"
    "s_waitcnt lgkmcnt(5)\n"
    "v_mfma_f32_16x16x32_bf16 v[236:239], v[220:223], v[132:135], v[236:239]\n"
    "v_mfma_f32_16x16x32_bf16 v[240:243], v[220:223], v[136:139], v[240:243]\n"
    "s_waitcnt lgkmcnt(4)\n"
    "v_mfma_f32_16x16x32_bf16 v[236:239], v[224:227], v[140:143], v[236:239]\n"
    "v_mfma_f32_16x16x32_bf16 v[240:243], v[224:227], v[144:147], v[240:243]\n"
    "s_waitcnt lgkmcnt(3)\n"
    "v_mfma_f32_16x16x32_bf16 v[244:247], v[228:231], v[116:119], 0\n"
    "v_mfma_f32_16x16x32_bf16 v[248:251], v[228:231], v[120:123], 0\n"
    "s_waitcnt lgkmcnt(2)\n"
    "v_mfma_f32_16x16x32_bf16 v[244:247], v[232:235], v[124:127], v[244:247]\n"
    "v_mfma_f32_16x16x32_bf16 v[248:251], v[232:235], v[128:131], v[248:251]\n"
    "s_waitcnt lgkmcnt(1)\n"
    "v_mfma_f32_16x16x32_bf16 v[244:247], v[212:215], v[132:135], v[244:247]\n"
    "v_mfma_f32_16x16x32_bf16 v[248:251], v[212:215], v[136:139], v[248:251]\n"
    "s_waitcnt lgkmcnt(0)\n"
    "v_mfma_f32_16x16x32_bf16 v[244:247], v[216:219], v[140:143], v[244:247]\n"
    "v_mfma_f32_16x16x32_bf16 v[248:251], v[216:219], v[144:147], v[248:251]\n"
    "v_pk_mul_f32 v[220:221], v[236:237], v[180:181]\n"
    "v_pk_mul_f32 v[224:225], v[240:241], v[180:181]\n"
    "v_pk_mul_f32 v[222:223], v[238:239], v[182:183]\n"
    "v_pk_mul_f32 v[226:227], v[242:243], v[182:183]\n"
    "v_pk_fma_f32 v[220:221], v[240:241], v[188:189], v[220:221]\n"
    "v_pk_fma_f32 v[224:225], v[236:237], v[188:189], v[224:225] neg_lo:[1,0,0] neg_hi:[1,0,0]\n"
    "v_pk_fma_f32 v[222:223], v[242:243], v[190:191], v[222:223]\n"
    "v_pk_fma_f32 v[226:227], v[238:239], v[190:191], v[226:227] neg_lo:[1,0,0] neg_hi:[1,0,0]\n"
    "v_cvt_pk_bf16_f32 v220, v220, v224\n"
    "v_cvt_pk_bf16_f32 v221, v221, v225\n"
    "v_cvt_pk_bf16_f32 v222, v222, v226\n"
    "v_cvt_pk_bf16_f32 v223, v223, v227\n"
    "v_pk_mul_f32 v[228:229], v[244:245], v[184:185]\n"
    "v_pk_mul_f32 v[232:233], v[248:249], v[184:185]\n"
    "v_pk_mul_f32 v[230:231], v[246:247], v[186:187]\n"
    "v_pk_mul_f32 v[234:235], v[250:251], v[186:187]\n"
    "v_pk_fma_f32 v[228:229], v[248:249], v[192:193], v[228:229]\n"
    "v_pk_fma_f32 v[232:233], v[244:245], v[192:193], v[232:233] neg_lo:[1,0,0] neg_hi:[1,0,0]\n"
    "v_pk_fma_f32 v[230:231], v[250:251], v[194:195], v[230:231]\n"
    "v_pk_fma_f32 v[234:235], v[246:247], v[194:195], v[234:235] neg_lo:[1,0,0] neg_hi:[1,0,0]\n"
    "v_cvt_pk_bf16_f32 v228, v228, v232\n"
    "v_cvt_pk_bf16_f32 v229, v229, v233\n"
    "v_cvt_pk_bf16_f32 v230, v230, v234\n"
    "v_cvt_pk_bf16_f32 v231, v231, v235\n"
    "v_mfma_f32_16x16x32_bf16 v[236:239], v[148:151], v[220:223], 0\n"
    "v_mfma_f32_16x16x32_bf16 v[240:243], v[152:155], v[220:223], 0\n"
    "v_mfma_f32_16x16x32_bf16 v[236:239], v[156:159], v[228:231], v[236:239]\n"
    "v_mfma_f32_16x16x32_bf16 v[240:243], v[160:163], v[228:231], v[240:243]\n"
    "v_mfma_f32_16x16x32_bf16 v[244:247], v[164:167], v[220:223], 0\n"
    "v_mfma_f32_16x16x32_bf16 v[248:251], v[168:171], v[220:223], 0\n"
    "v_mfma_f32_16x16x32_bf16 v[244:247], v[172:175], v[228:231], v[244:247]\n"
    "v_mfma_f32_16x16x32_bf16 v[248:251], v[176:179], v[228:231], v[248:251]\n"
    "v_lshlrev_b32 v180, 16, v104\n"
    "v_and_b32 v188, 0xffff0000, v104\n"
    "v_lshlrev_b32 v181, 16, v105\n"
    "v_and_b32 v189, 0xffff0000, v105\n"
    "v_lshlrev_b32 v182, 16, v106\n"
    "v_and_b32 v190, 0xffff0000, v106\n"
    "v_lshlrev_b32 v183, 16, v107\n"
    "v_and_b32 v191, 0xffff0000, v107\n"
    "v_lshlrev_b32 v184, 16, v108\n"
    "v_and_b32 v192, 0xffff0000, v108\n"
    "v_lshlrev_b32 v185, 16, v109\n"
    "v_and_b32 v193, 0xffff0000, v109\n"
    "v_lshlrev_b32 v186, 16, v110\n"
    "v_and_b32 v194, 0xffff0000, v110\n"
    "v_lshlrev_b32 v187, 16, v111\n"
    "v_and_b32 v195, 0xffff0000, v111\n"
    "v_pk_mul_f32 v[212:213], v[236:237], v[180:181]\n"
    "v_pk_mul_f32 v[216:217], v[240:241], v[180:181]\n"
    "v_pk_mul_f32 v[214:215], v[238:239], v[182:183]\n"
    "v_pk_mul_f32 v[218:219], v[242:243], v[182:183]\n"
    "v_pk_fma_f32 v[212:213], v[240:241], v[188:189], v[212:213] neg_lo:[1,0,0] neg_hi:[1,0,0]\n"
    "v_pk_fma_f32 v[216:217], v[236:237], v[188:189], v[216:217]\n"
    "v_pk_fma_f32 v[214:215], v[242:243], v[190:191], v[214:215] neg_lo:[1,0,0] neg_hi:[1,0,0]\n"
    "v_pk_fma_f32 v[218:219], v[238:239], v[190:191], v[218:219]\n"
    "v_cvt_pk_bf16_f32 v212, v212, -v216\n"
    "v_cvt_pk_bf16_f32 v213, v213, -v217\n"
    "v_cvt_pk_bf16_f32 v214, v214, -v218\n"
    "v_cvt_pk_bf16_f32 v215, v215, -v219\n"
    "v_pk_mul_f32 v[220:221], v[244:245], v[184:185]\n"
    "v_pk_mul_f32 v[224:225], v[248:249], v[184:185]\n"
    "v_pk_mul_f32 v[222:223], v[246:247], v[186:187]\n"
    "v_pk_mul_f32 v[226:227], v[250:251], v[186:187]\n"
    "v_pk_fma_f32 v[220:221], v[248:249], v[192:193], v[220:221] neg_lo:[1,0,0] neg_hi:[1,0,0]\n"
    "v_pk_fma_f32 v[224:225], v[244:245], v[192:193], v[224:225]\n"
    "v_pk_fma_f32 v[222:223], v[250:251], v[194:195], v[222:223] neg_lo:[1,0,0] neg_hi:[1,0,0]\n"
    "v_pk_fma_f32 v[226:227], v[246:247], v[194:195], v[226:227]\n"
    "v_cvt_pk_bf16_f32 v220, v220, -v224\n"
    "v_cvt_pk_bf16_f32 v221, v221, -v225\n"
    "v_cvt_pk_bf16_f32 v222, v222, -v226\n"
    "v_cvt_pk_bf16_f32 v223, v223, -v227\n"
    "v_mfma_f32_16x16x32_bf16 v[236:239], v[212:215], v[148:151], 0\n"
    "v_mfma_f32_16x16x32_bf16 v[240:243], v[212:215], v[152:155], 0\n"
    "v_mfma_f32_16x16x32_bf16 v[236:239], v[220:223], v[156:159], v[236:239]\n"
    "v_mfma_f32_16x16x32_bf16 v[240:243], v[220:223], v[160:163], v[240:243]\n"
    "v_mfma_f32_16x16x32_bf16 v[244:247], v[212:215], v[164:167], 0\n"
    "v_mfma_f32_16x16x32_bf16 v[248:251], v[212:215], v[168:171], 0\n"
    "v_mfma_f32_16x16x32_bf16 v[244:247], v[220:223], v[172:175], v[244:247]\n"
    "v_mfma_f32_16x16x32_bf16 v[248:251], v[220:223], v[176:179], v[248:251]\n"
    "v_pk_mul_f32 v[212:213], v[236:237], v[196:197]\n"
    "v_pk_mul_f32 v[216:217], v[240:241], v[196:197]\n"
    "v_pk_mul_f32 v[214:215], v[238:239], v[198:199]\n"
    "v_pk_mul_f32 v[218:219], v[242:243], v[198:199]\n"
    "v_pk_fma_f32 v[212:213], v[240:241], v[204:205], v[212:213]\n"
    "v_pk_fma_f32 v[216:217], v[236:237], v[204:205], v[216:217] neg_lo:[1,0,0] neg_hi:[1,0,0]\n"
    "v_pk_fma_f32 v[214:215], v[242:243], v[206:207], v[214:215]\n"
    "v_pk_fma_f32 v[218:219], v[238:239], v[206:207], v[218:219] neg_lo:[1,0,0] neg_hi:[1,0,0]\n"
    "v_cvt_pk_bf16_f32 v212, v212, v216\n"
    "v_cvt_pk_bf16_f32 v213, v213, v217\n"
    "v_cvt_pk_bf16_f32 v214, v214, v218\n"
    "v_cvt_pk_bf16_f32 v215, v215, v219\n"
    "ds_write_b128 v255, v[212:215] offset:0\n"
    "v_pk_mul_f32 v[220:221], v[244:245], v[200:201]\n"
    "v_pk_mul_f32 v[224:225], v[248:249], v[200:201]\n"
    "v_pk_mul_f32 v[222:223], v[246:247], v[202:203]\n"
    "v_pk_mul_f32 v[226:227], v[250:251], v[202:203]\n"
    "v_pk_fma_f32 v[220:221], v[248:249], v[208:209], v[220:221]\n"
    "v_pk_fma_f32 v[224:225], v[244:245], v[208:209], v[224:225] neg_lo:[1,0,0] neg_hi:[1,0,0]\n"
    "v_pk_fma_f32 v[222:223], v[250:251], v[210:211], v[222:223]\n"
    "v_pk_fma_f32 v[226:227], v[246:247], v[210:211], v[226:227] neg_lo:[1,0,0] neg_hi:[1,0,0]\n"
    "v_cvt_pk_bf16_f32 v220, v220, v224\n"
    "v_cvt_pk_bf16_f32 v221, v221, v225\n"
    "v_cvt_pk_bf16_f32 v222, v222, v226\n"
    "v_cvt_pk_bf16_f32 v223, v223, v227\n"
    "ds_write_b128 v255, v[220:223] offset:1024\n"
    "s_waitcnt lgkmcnt(0)\n"
    "s_barrier\n"
    "ds_read_b128 v[180:183], v113 offset:8192\n"
    "ds_read_b128 v[184:187], v113 offset:10240\n"
    "ds_read_b128 v[188:191], v113 offset:12288\n"
    "ds_read_b128 v[192:195], v113 offset:14336\n"
    "ds_read_b128 v[196:199], v113 offset:9216\n"
    "ds_read_b128 v[200:203], v113 offset:11264\n"
    "ds_read_b128 v[204:207], v113 offset:13312\n"
    "ds_read_b128 v[208:211], v113 offset:15360\n"
    "s_waitcnt lgkmcnt(7)\n"
    "v_mfma_f32_16x16x32_bf16 v[236:239], v[116:119], v[180:183], 0\n"
    "v_mfma_f32_16x16x32_bf16 v[240:243], v[120:123], v[180:183], 0\n"
    "s_waitcnt lgkmcnt(6)\n"
    "v_mfma_f32_16x16x32_bf16 v[236:239], v[124:127], v[184:187], v[236:239]\n"
    "v_mfma_f32_16x16x32_bf16 v[240:243], v[128:131], v[184:187], v[240:243]\n"
    "s_waitcnt lgkmcnt(5)\n"
    "v_mfma_f32_16x16x32_bf16 v[236:239], v[132:135], v[188:191], v[236:239]\n"
    "v_mfma_f32_16x16x32_bf16 v[240:243], v[136:139], v[188:191], v[240:243]\n"
    "s_waitcnt lgkmcnt(4)\n"
    "v_mfma_f32_16x16x32_bf16 v[236:239], v[140:143], v[192:195], v[236:239]\n"
    "v_mfma_f32_16x16x32_bf16 v[240:243], v[144:147], v[192:195], v[240:243]\n"
    "s_waitcnt lgkmcnt(3)\n"
    "v_mfma_f32_16x16x32_bf16 v[244:247], v[116:119], v[196:199], 0\n"
    "v_mfma_f32_16x16x32_bf16 v[248:251], v[120:123], v[196:199], 0\n"
    "s_waitcnt lgkmcnt(2)\n"
    "v_mfma_f32_16x16x32_bf16 v[244:247], v[124:127], v[200:203], v[244:247]\n"
    "v_mfma_f32_16x16x32_bf16 v[248:251], v[128:131], v[200:203], v[248:251]\n"
    "s_waitcnt lgkmcnt(1)\n"
    "v_mfma_f32_16x16x32_bf16 v[244:247], v[132:135], v[204:207], v[244:247]\n"
    "v_mfma_f32_16x16x32_bf16 v[248:251], v[136:139], v[204:207], v[248:251]\n"
    "s_waitcnt lgkmcnt(0)\n"
    "v_mfma_f32_16x16x32_bf16 v[244:247], v[140:143], v[208:211], v[244:247]\n"
    "v_mfma_f32_16x16x32_bf16 v[248:251], v[144:147], v[208:211], v[248:251]\n"
    "v_cvt_pk_bf16_f32 v212, v236, -v240\n"
    "v_cvt_pk_bf16_f32 v213, v237, -v241\n"
    "v_cvt_pk_bf16_f32 v214, v238, -v242\n"
    "v_cvt_pk_bf16_f32 v215, v239, -v243\n"
    "buffer_store_dwordx4 v[212:215], v253, s[12:15], s70 offen offset:0 sc1\n"
    "s_nop 2\n"
    "v_cvt_pk_bf16_f32 v216, v244, -v248\n"
    "v_cvt_pk_bf16_f32 v217, v245, -v249\n"
    "v_cvt_pk_bf16_f32 v218, v246, -v250\n"
    "v_cvt_pk_bf16_f32 v219, v247, -v251\n"
    "buffer_store_dwordx4 v[216:219], v253, s[12:15], s70 offen offset:512 sc1\n"
    ".Lmark_p2out_57:\n"
    "v_and_b32 v113, 15, v112\n"
    "v_bfe_u32 v114, v112, 4, 2\n"
    "v_bfe_u32 v115, v112, 1, 5\n"
    "v_lshlrev_b32 v116, 5, v115\n"
    "v_lshlrev_b32 v115, 13, v115\n"
    "v_and_b32 v248, 1, v112\n"
    "v_lshl_add_u32 v115, v248, 4, v115\n"
    "v_cmp_gt_u32 vcc, 194, v112\n"
    "v_mov_b32 v123, 1073741824\n"
    "v_bfe_u32 v249, v112, 4, 1\n"
    "v_xor_b32 v248, v248, v249\n"
    "v_cndmask_b32 v123, v123, v115, vcc\n"
    "v_lshl_add_u32 v116, v248, 4, v116\n"
    "s_lshl_b32 s74, s52, 11\n"
    "s_add_u32 s74, s74, 139776\n"
    "v_add_u32 v116, s74, v116\n"
    "s_lshl_b32 s74, s52, 19\n"
    "s_lshl_b32 s75, s53, 5\n"
    "s_add_u32 s74, s74, s75\n"
    "s_add_u32 s70, s74, 18874368\n"
    "s_add_u32 s71, s70, 262144\n"
    "v_cvt_f32_u32 v244, v113\n"
    "v_lshlrev_b32 v245, 2, v114\n"
    "v_cvt_f32_u32 v245, v245\n"
    "v_and_b32 v247, 1, v113\n"
    "v_mul_u32_u24 v247, 0x8000, v247\n"
    "v_lshl_or_b32 v247, v247, 16, v247\n"
    "v_mul_f32 v246, 0x3d924925, v244\n"
    "v_mul_f32 v196, v245, v246\n"
    "v_mul_f32 v196, 0.5, v196\n"
    "v_add_f32 v197, v196, v246\n"
    "v_sin_f32 v204, v196\n"
    "v_cos_f32 v200, v196\n"
    "v_sin_f32 v205, v197\n"
    "v_cos_f32 v201, v197\n"
    "v_cmp_gt_u32 vcc, 14, v113\n"
    "v_mov_b32 v124, 0\n"
    "v_mov_b32 v125, 0\n"
    "v_mov_b32 v126, 0\n"
    "v_mov_b32 v127, 0\n"
    "v_mov_b32 v128, 0\n"
    "v_mov_b32 v129, 0\n"
    "v_mov_b32 v130, 0\n"
    "v_mov_b32 v131, 0\n"
    "v_mov_b32 v132, 0\n"
    "v_mov_b32 v133, 0\n"
    "v_mov_b32 v134, 0\n"
    "v_mov_b32 v135, 0\n"
    "v_mov_b32 v136, 0\n"
    "v_mov_b32 v137, 0\n"
    "v_mov_b32 v138, 0\n"
    "v_mov_b32 v139, 0\n"
    "v_cvt_pk_bf16_f32 v198, v200, -v204\n"
    "v_cvt_pk_bf16_f32 v199, v204, v200\n"
    "v_cndmask_b32 v124, 0, v198, vcc\n"
    "v_cndmask_b32 v128, 0, v199, vcc\n"
    "v_cndmask_b32 v133, 0, v198, vcc\n"
    "v_cndmask_b32 v137, 0, v199, vcc\n"
    "v_cvt_pk_bf16_f32 v198, v201, -v205\n"
    "v_cvt_pk_bf16_f32 v199, v205, v201\n"
    "v_cndmask_b32 v126, 0, v198, vcc\n"
    "v_cndmask_b32 v130, 0, v199, vcc\n"
    "v_cndmask_b32 v135, 0, v198, vcc\n"
    "v_cndmask_b32 v139, 0, v199, vcc\n"
    "v_mul_f32 v246, 0x3b124925, v244\n"
    "v_mul_f32 v196, v245, v246\n"
    "v_add_f32 v197, v196, v246\n"
    "v_add_f32 v198, v197, v246\n"
    "v_add_f32 v199, v198, v246\n"
    "v_sin_f32 v148, v196\n"
    "v_sin_f32 v149, v197\n"
    "v_sin_f32 v150, v198\n"
    "v_sin_f32 v151, v199\n"
    "v_cos_f32 v140, v196\n"
    "v_cos_f32 v141, v197\n"
    "v_cos_f32 v142, v198\n"
    "v_cos_f32 v143, v199\n"
    "v_mul_f32 v196, v245, v246\n"
    "v_fmamk_f32 v196, v246, 0x41800000, v196\n"
    "v_add_f32 v197, v196, v246\n"
    "v_add_f32 v198, v197, v246\n"
    "v_add_f32 v199, v198, v246\n"
    "v_sin_f32 v152, v196\n"
    "v_sin_f32 v153, v197\n"
    "v_sin_f32 v154, v198\n"
    "v_sin_f32 v155, v199\n"
    "v_cos_f32 v144, v196\n"
    "v_cos_f32 v145, v197\n"
    "v_cos_f32 v146, v198\n"
    "v_cos_f32 v147, v199\n"
    "v_mul_f32 v246, 0x3d000000, v244\n"
    "v_mul_f32 v196, v245, v246\n"
    "v_add_f32 v197, v196, v246\n"
    "v_add_f32 v198, v197, v246\n"
    "v_add_f32 v199, v198, v246\n"
    "v_sin_f32 v204, v196\n"
    "v_sin_f32 v205, v197\n"
    "v_sin_f32 v206, v198\n"
    "v_sin_f32 v207, v199\n"
    "v_cos_f32 v200, v196\n"
    "v_cos_f32 v201, v197\n"
    "v_cos_f32 v202, v198\n"
    "v_cos_f32 v203, v199\n"
    "v_cvt_pk_bf16_f32 v156, v200, -v204\n"
    "v_cvt_pk_bf16_f32 v157, v201, -v205\n"
    "v_cvt_pk_bf16_f32 v158, v202, -v206\n"
    "v_cvt_pk_bf16_f32 v159, v203, -v207\n"
    "v_xor_b32 v160, v156, v247\n"
    "v_xor_b32 v161, v157, v247\n"
    "v_xor_b32 v162, v158, v247\n"
    "v_xor_b32 v163, v159, v247\n"
    "v_mov_b32 v164, v156\n"
    "v_xor_b32 v165, s73, v157\n"
    "v_mov_b32 v166, v158\n"
    "v_xor_b32 v167, s73, v159\n"
    "v_mov_b32 v168, v160\n"
    "v_xor_b32 v169, s73, v161\n"
    "v_mov_b32 v170, v162\n"
    "v_xor_b32 v171, s73, v163\n"
    "v_lshlrev_b32 v172, 16, v96\n"
    "v_and_b32 v173, 0xffff0000, v96\n"
    "v_lshlrev_b32 v174, 16, v97\n"
    "v_and_b32 v175, 0xffff0000, v97\n"
    "v_lshlrev_b32 v176, 16, v98\n"
    "v_and_b32 v177, 0xffff0000, v98\n"
    "v_lshlrev_b32 v178, 16, v99\n"
    "v_and_b32 v179, 0xffff0000, v99\n"
    "v_lshlrev_b32 v180, 16, v100\n"
    "v_and_b32 v181, 0xffff0000, v100\n"
    "v_lshlrev_b32 v182, 16, v101\n"
    "v_and_b32 v183, 0xffff0000, v101\n"
    "v_lshlrev_b32 v184, 16, v102\n"
    "v_and_b32 v185, 0xffff0000, v102\n"
    "v_lshlrev_b32 v186, 16, v103\n"
    "v_and_b32 v187, 0xffff0000, v103\n"
    "v_lshl_add_u32 v117, v114, 6, v113\n"
    "v_lshlrev_b32 v117, 5, v117\n"
    "v_lshrrev_b32 v248, 3, v113\n"
    "v_lshlrev_b32 v248, 1, v248\n"
    "v_xor_b32 v248, s52, v248\n"
    "v_lshl_add_u32 v117, v248, 3, v117\n"
    "v_add_u32 v117, 139776, v117\n"
    "v_lshrrev_b32 v248, 4, v112\n"
    "s_waitcnt vmcnt(0)\n"
    "buffer_load_dwordx4 v[188:191], v115, s[12:15], s70 offen sc1\n"
    "buffer_load_dwordx4 v[192:195], v123, s[12:15], s71 offen sc1\n"
    "v_and_b32 v249, 3, v112\n"
    "v_lshl_add_u32 v248, v248, 2, v249\n"
    "v_mul_u32_u24 v249, 147, v248\n"
    "v_lshrrev_b32 v249, 11, v249\n"
    "v_mul_u32_u24 v250, 14, v249\n"
    "v_sub_u32 v250, v248, v250\n"
    "v_mul_u32_u24 v119, 112, v249\n"
    "v_and_b32 v248, 3, v250\n"
    "v_lshlrev_b32 v248, 4, v248\n"
    "v_lshrrev_b32 v249, 2, v250\n"
    "v_lshl_add_u32 v248, v249, 3, v248\n"
    "v_lshlrev_b32 v249, 3, v250\n"
    "v_cmp_gt_u32 vcc, 8, v250\n"
    "s_nop 1\n"
    "v_cndmask_b32 v248, v249, v248, vcc\n"
    "v_add_u32 v119, v119, v248\n"
    "v_bfe_u32 v248, v112, 2, 2\n"
    "v_mul_u32_u24 v248, 1088, v248\n"
    "v_add_u32 v119, v119, v248\n"
    "v_add_u32 v119, 147968, v119\n"
    "v_lshrrev_b32 v248, 4, v112\n"
    "v_and_b32 v249, 3, v112\n"
    "v_lshl_add_u32 v248, v248, 2, v249\n"
    "v_add_u32 v248, 64, v248\n"
    "v_min_u32 v248, 111, v248\n"
    "v_mul_u32_u24 v249, 147, v248\n"
    "v_lshrrev_b32 v249, 11, v249\n"
    "v_mul_u32_u24 v250, 14, v249\n"
    "v_sub_u32 v250, v248, v250\n"
    "v_mul_u32_u24 v120, 112, v249\n"
    "v_and_b32 v248, 3, v250\n"
    "v_lshlrev_b32 v248, 4, v248\n"
    "v_lshrrev_b32 v249, 2, v250\n"
    "v_lshl_add_u32 v248, v249, 3, v248\n"
    "v_lshlrev_b32 v249, 3, v250\n"
    "v_cmp_gt_u32 vcc, 8, v250\n"
    "s_nop 1\n"
    "v_cndmask_b32 v248, v249, v248, vcc\n"
    "v_add_u32 v120, v120, v248\n"
    "v_bfe_u32 v248, v112, 2, 2\n"
    "v_mul_u32_u24 v248, 1088, v248\n"
    "v_add_u32 v120, v120, v248\n"
    "v_add_u32 v120, 147968, v120\n"
    "s_mul_i32 s78, s53, 7168\n"
    "s_add_u32 s78, s78, 20971520\n"
    "v_lshl_add_u32 v121, v112, 4, s78\n"
    "v_add_u32 v122, 4096, v121\n"
    "v_mov_b32 v248, 1073741824\n"
    "v_cmp_gt_u32 vcc, 192, v112\n"
    "s_mul_i32 s79, s52, 2176\n"
    "s_add_u32 s79, s79, 147968\n"
    "v_cndmask_b32 v122, v248, v122, vcc\n"
    "v_mul_u32_u24 v118, 112, v114\n"
    "v_lshl_add_u32 v118, v113, 1, v118\n"
    "v_add_u32 v118, s79, v118\n"
    "s_waitcnt vmcnt(1)\n"
    "v_max3_u32 v244, v188, v189, v190\n"
    "s_waitcnt vmcnt(0)\n"
    "v_max3_u32 v244, v244, v191, v192\n"
    "v_max3_u32 v244, v244, v193, v194\n"
    "v_max_u32 v244, v244, v195\n"
    "v_cmp_eq_u32 vcc, -1, v244\n"
    "s_cbranch_vccnz .Lretry_58\n"
    ".Lz_59:\n"
    "ds_write_b128 v116, v[188:191] offset:0\n"
    "ds_write_b128 v116, v[192:195] offset:1024\n"
    ".Lmark_p3in_60:\n"
    "s_waitcnt lgkmcnt(0)\n"
    "s_barrier\n"
    "ds_read_b64 v[188:189], v117 offset:0\n"
    "ds_read_b64 v[190:191], v117 offset:1024\n"
    "ds_read_b64 v[192:193], v117 offset:512\n"
    "ds_read_b64 v[194:195], v117 offset:1536\n"
    "s_waitcnt lgkmcnt(2)\n"
    "v_mfma_f32_16x16x32_bf16 v[196:199], v[188:191], v[124:127], 0\n"
    "v_mfma_f32_16x16x32_bf16 v[200:203], v[188:191], v[128:131], 0\n"
    "s_waitcnt lgkmcnt(0)\n"
    "v_mfma_f32_16x16x32_bf16 v[204:207], v[192:195], v[124:127], 0\n"
    "v_mfma_f32_16x16x32_bf16 v[208:211], v[192:195], v[128:131], 0\n"
    "v_mfma_f32_16x16x32_bf16 v[212:215], v[188:191], v[132:135], 0\n"
    "v_mfma_f32_16x16x32_bf16 v[216:219], v[188:191], v[136:139], 0\n"
    "v_mfma_f32_16x16x32_bf16 v[220:223], v[192:195], v[132:135], 0\n"
    "v_mfma_f32_16x16x32_bf16 v[224:227], v[192:195], v[136:139], 0\n"
    "v_pk_mul_f32 v[188:189], v[196:197], v[140:141]\n"
    "v_pk_mul_f32 v[192:193], v[200:201], v[140:141]\n"
    "v_pk_mul_f32 v[190:191], v[198:199], v[142:143]\n"
    "v_pk_mul_f32 v[194:195], v[202:203], v[142:143]\n"
    "v_pk_fma_f32 v[188:189], v[200:201], v[148:149], v[188:189] neg_lo:[1,0,0] neg_hi:[1,0,0]\n"
    "v_pk_fma_f32 v[192:193], v[196:197], v[148:149], v[192:193]\n"
    "v_pk_fma_f32 v[190:191], v[202:203], v[150:151], v[190:191] neg_lo:[1,0,0] neg_hi:[1,0,0]\n"
    "v_pk_fma_f32 v[194:195], v[198:199], v[150:151], v[194:195]\n"
    "v_cvt_pk_bf16_f32 v228, v188, v192\n"
    "v_cvt_pk_bf16_f32 v229, v189, v193\n"
    "v_cvt_pk_bf16_f32 v230, v190, v194\n"
    "v_cvt_pk_bf16_f32 v231, v191, v195\n"
    "v_pk_mul_f32 v[244:245], v[204:205], v[144:145]\n"
    "v_pk_mul_f32 v[248:249], v[208:209], v[144:145]\n"
    "v_pk_mul_f32 v[246:247], v[206:207], v[146:147]\n"
    "v_pk_mul_f32 v[250:251], v[210:211], v[146:147]\n"
    "v_pk_fma_f32 v[244:245], v[208:209], v[152:153], v[244:245] neg_lo:[1,0,0] neg_hi:[1,0,0]\n"
    "v_pk_fma_f32 v[248:249], v[204:205], v[152:153], v[248:249]\n"
    "v_pk_fma_f32 v[246:247], v[210:211], v[154:155], v[246:247] neg_lo:[1,0,0] neg_hi:[1,0,0]\n"
    "v_pk_fma_f32 v[250:251], v[206:207], v[154:155], v[250:251]\n"
    "v_cvt_pk_bf16_f32 v232, v244, v248\n"
    "v_cvt_pk_bf16_f32 v233, v245, v249\n"
    "v_cvt_pk_bf16_f32 v234, v246, v250\n"
    "v_cvt_pk_bf16_f32 v235, v247, v251\n"
    "v_pk_mul_f32 v[244:245], v[212:213], v[140:141]\n"
    "v_pk_mul_f32 v[248:249], v[216:217], v[140:141]\n"
    "v_pk_mul_f32 v[246:247], v[214:215], v[142:143]\n"
    "v_pk_mul_f32 v[250:251], v[218:219], v[142:143]\n"
    "v_pk_fma_f32 v[244:245], v[216:217], v[148:149], v[244:245] neg_lo:[1,0,0] neg_hi:[1,0,0]\n"
    "v_pk_fma_f32 v[248:249], v[212:213], v[148:149], v[248:249]\n"
    "v_pk_fma_f32 v[246:247], v[218:219], v[150:151], v[246:247] neg_lo:[1,0,0] neg_hi:[1,0,0]\n"
    "v_pk_fma_f32 v[250:251], v[214:215], v[150:151], v[250:251]\n"
    "v_cvt_pk_bf16_f32 v236, v244, v248\n"
    "v_cvt_pk_bf16_f32 v237, v245, v249\n"
    "v_cvt_pk_bf16_f32 v238, v246, v250\n"
    "v_cvt_pk_bf16_f32 v239, v247, v251\n"
    "v_pk_mul_f32 v[188:189], v[220:221], v[144:145]\n"
    "v_pk_mul_f32 v[192:193], v[224:225], v[144:145]\n"
    "v_pk_mul_f32 v[190:191], v[222:223], v[146:147]\n"
    "v_pk_mul_f32 v[194:195], v[226:227], v[146:147]\n"
    "v_pk_fma_f32 v[188:189], v[224:225], v[152:153], v[188:189] neg_lo:[1,0,0] neg_hi:[1,0,0]\n"
    "v_pk_fma_f32 v[192:193], v[220:221], v[152:153], v[192:193]\n"
    "v_pk_fma_f32 v[190:191], v[226:227], v[154:155], v[190:191] neg_lo:[1,0,0] neg_hi:[1,0,0]\n"
    "v_pk_fma_f32 v[194:195], v[222:223], v[154:155], v[194:195]\n"
    "v_cvt_pk_bf16_f32 v240, v188, v192\n"
    "v_cvt_pk_bf16_f32 v241, v189, v193\n"
    "v_cvt_pk_bf16_f32 v242, v190, v194\n"
    "v_cvt_pk_bf16_f32 v243, v191, v195\n"
    "v_mfma_f32_16x16x32_bf16 v[172:175], v[156:159], v[228:231], v[172:175]\n"
    "v_mfma_f32_16x16x32_bf16 v[176:179], v[164:167], v[228:231], v[176:179]\n"
    "v_mfma_f32_16x16x32_bf16 v[172:175], v[160:163], v[232:235], v[172:175]\n"
    "v_mfma_f32_16x16x32_bf16 v[176:179], v[168:171], v[232:235], v[176:179]\n"
    "v_mfma_f32_16x16x32_bf16 v[180:183], v[156:159], v[236:239], v[180:183]\n"
    "v_mfma_f32_16x16x32_bf16 v[184:187], v[164:167], v[236:239], v[184:187]\n"
    "v_mfma_f32_16x16x32_bf16 v[180:183], v[160:163], v[240:243], v[180:183]\n"
    "v_mfma_f32_16x16x32_bf16 v[184:187], v[168:171], v[240:243], v[184:187]\n"
    "v_pk_add_f32 v[244:245], v[172:173], v[174:175]\n"
    "v_pk_mul_f32 v[246:247], v[172:173], v[172:173]\n"
    "s_nop 1\n"
    "v_pk_add_f32 v[248:249], v[180:181], v[182:183]\n"
    "v_pk_mul_f32 v[250:251], v[180:181], v[180:181]\n"
    "v_pk_add_f32 v[244:245], v[244:245], v[176:177]\n"
    "s_nop 0\n"
    "v_pk_add_f32 v[248:249], v[248:249], v[184:185]\n"
    "v_pk_add_f32 v[244:245], v[244:245], v[178:179]\n"
    "v_pk_add_f32 v[248:249], v[248:249], v[186:187]\n"
    "v_pk_fma_f32 v[246:247], v[174:175], v[174:175], v[246:247]\n"
    "v_pk_fma_f32 v[250:251], v[182:183], v[182:183], v[250:251]\n"
    "v_pk_fma_f32 v[246:247], v[176:177], v[176:177], v[246:247]\n"
    "v_pk_fma_f32 v[250:251], v[184:185], v[184:185], v[250:251]\n"
    "v_pk_fma_f32 v[246:247], v[178:179], v[178:179], v[246:247]\n"
    "v_pk_fma_f32 v[250:251], v[186:187], v[186:187], v[250:251]\n"
    "v_add_f32 v244, v244, v245\n"
    "v_add_f32 v248, v248, v249\n"
    "v_add_f32 v246, v246, v247\n"
    "v_add_f32 v250, v250, v251\n"
    "s_nop 0\n"
    "v_permlane32_swap_b32 v244, v246\n"
    "v_permlane32_swap_b32 v248, v250\n"
    "v_add_f32 v244, v244, v246\n"
    "v_add_f32 v248, v248, v250\n"
    "s_nop 1\n"
    "v_permlane16_swap_b32 v244, v248\n"
    "v_add_f32 v244, v244, v248\n"
    "s_nop 1\n"
    "v_add_f32_dpp v244, v244, v244 row_ror:8 row_mask:0xf bank_mask:0xf\n"
    "s_nop 1\n"
    "v_add_f32_dpp v244, v244, v244 row_ror:4 row_mask:0xf bank_mask:0xf\n"
    "s_nop 1\n"
    "v_add_f32_dpp v244, v244, v244 row_ror:2 row_mask:0xf bank_mask:0xf\n"
    "s_nop 1\n"
    "v_add_f32_dpp v244, v244, v244 row_ror:1 row_mask:0xf bank_mask:0xf\n"
    "v_mul_f32 v244, 0x3b124925, v244\n"
    "v_mov_b32 v245, v244\n"
    "s_nop 1\n"
    "v_permlane32_swap_b32 v244, v245\n"
    "v_fma_f32 v245, -v244, v244, v245\n"
    "v_max_f32 v245, 0, v245\n"
    "v_add_f32 v246, 0x3727c5ac, v245\n"
    "v_rsq_f32 v246, v246\n"
    "s_nop 0\n"
    "v_mul_f32 v247, v246, v246\n"
    "v_fmaak_f32 v247, v247, v245, 0x3727c5ac\n"
    "v_rsq_f32 v247, v247\n"
    "s_nop 0\n"
    "v_mul_f32 v246, v246, v247\n"
    "v_mul_f32 v244, -v244, v246\n"
    "v_readlane_b32 s64, v246, 0\n"
    "v_readlane_b32 s66, v246, 16\n"
    "v_readlane_b32 s65, v244, 0\n"
    "v_readlane_b32 s67, v244, 16\n"
    "s_nop 0\n"
    "v_pk_fma_f32 v[196:197], v[172:173], s[64:65], s[64:65] op_sel:[0,0,1] op_sel_hi:[1,0,1]\n"
    "v_pk_fma_f32 v[198:199], v[174:175], s[64:65], s[64:65] op_sel:[0,0,1] op_sel_hi:[1,0,1]\n"
    "v_pk_fma_f32 v[200:201], v[176:177], s[64:65], s[64:65] op_sel:[0,0,1] op_sel_hi:[1,0,1]\n"
    "v_pk_fma_f32 v[202:203], v[178:179], s[64:65], s[64:65] op_sel:[0,0,1] op_sel_hi:[1,0,1]\n"
    "v_pk_fma_f32 v[204:205], v[180:181], s[66:67], s[66:67] op_sel:[0,0,1] op_sel_hi:[1,0,1]\n"
    "v_pk_fma_f32 v[206:207], v[182:183], s[66:67], s[66:67] op_sel:[0,0,1] op_sel_hi:[1,0,1]\n"
    "v_pk_fma_f32 v[208:209], v[184:185], s[66:67], s[66:67] op_sel:[0,0,1] op_sel_hi:[1,0,1]\n"
    "v_pk_fma_f32 v[210:211], v[186:187], s[66:67], s[66:67] op_sel:[0,0,1] op_sel_hi:[1,0,1]\n"
    "v_cvt_pk_bf16_f32 v228, v196, v197\n"
    "v_cvt_pk_bf16_f32 v229, v198, v199\n"
    "v_cvt_pk_bf16_f32 v230, v200, v201\n"
    "v_cvt_pk_bf16_f32 v231, v202, v203\n"
    "v_cvt_pk_bf16_f32 v232, v204, v205\n"
    "v_cvt_pk_bf16_f32 v233, v206, v207\n"
    "v_cvt_pk_bf16_f32 v234, v208, v209\n"
    "v_cvt_pk_bf16_f32 v235, v210, v211\n"
    "s_mov_b64 exec, s[68:69]\n"
    "ds_write_b16 v118, v228 offset:0\n"
    "ds_write_b16_d16_hi v118, v228 offset:28\n"
    "ds_write_b16 v118, v229 offset:56\n"
    "ds_write_b16_d16_hi v118, v229 offset:84\n"
    "ds_write_b16 v118, v230 offset:448\n"
    "ds_write_b16_d16_hi v118, v230 offset:476\n"
    "ds_write_b16 v118, v231 offset:504\n"
    "ds_write_b16_d16_hi v118, v231 offset:532\n"
    "ds_write_b16 v118, v232 offset:1088\n"
    "ds_write_b16_d16_hi v118, v232 offset:1116\n"
    "ds_write_b16 v118, v233 offset:1144\n"
    "ds_write_b16_d16_hi v118, v233 offset:1172\n"
    "ds_write_b16 v118, v234 offset:1536\n"
    "ds_write_b16_d16_hi v118, v234 offset:1564\n"
    "ds_write_b16 v118, v235 offset:1592\n"
    "ds_write_b16_d16_hi v118, v235 offset:1620\n"
    "s_mov_b64 exec, -1\n"
    "s_waitcnt lgkmcnt(0)\n"
    "s_barrier\n"
    "ds_read_b64_tr_b16 v[196:197], v119 offset:0\n"
    "ds_read_b64_tr_b16 v[198:199], v119 offset:4352\n"
    "ds_read_b64_tr_b16 v[200:201], v120 offset:0\n"
    "ds_read_b64_tr_b16 v[202:203], v120 offset:4352\n"
    "s_waitcnt lgkmcnt(2)\n"
    "buffer_store_dwordx4 v[196:199], v121, s[12:15], 0 offen sc1\n"
    "s_waitcnt lgkmcnt(0)\n"
    "buffer_store_dwordx4 v[200:203], v122, s[12:15], 0 offen sc1\n"
    ".Lmark_p3out_61:\n"
    "v_cvt_pk_bf16_f32 v96, v172, v173\n"
    "v_cvt_pk_bf16_f32 v97, v174, v175\n"
    "v_cvt_pk_bf16_f32 v98, v176, v177\n"
    "v_cvt_pk_bf16_f32 v99, v178, v179\n"
    "v_cvt_pk_bf16_f32 v100, v180, v181\n"
    "v_cvt_pk_bf16_f32 v101, v182, v183\n"
    "v_cvt_pk_bf16_f32 v102, v184, v185\n"
    "v_cvt_pk_bf16_f32 v103, v186, v187\n"
    "s_mul_i32 s74, s52, -128\n"
    "s_add_u32 s74, s74, -16896\n"
    "v_add_u32 v118, s74, v118\n"
    "s_mov_b64 exec, s[68:69]\n"
    "ds_write_b16 v118, v96 offset:0\n"
    "ds_write_b16_d16_hi v118, v96 offset:28\n"
    "ds_write_b16 v118, v97 offset:56\n"
    "ds_write_b16_d16_hi v118, v97 offset:84\n"
    "ds_write_b16 v118, v98 offset:448\n"
    "ds_write_b16_d16_hi v118, v98 offset:476\n"
    "ds_write_b16 v118, v99 offset:504\n"
    "ds_write_b16_d16_hi v118, v99 offset:532\n"
    "ds_write_b16 v118, v100 offset:1024\n"
    "ds_write_b16_d16_hi v118, v100 offset:1052\n"
    "ds_write_b16 v118, v101 offset:1080\n"
    "ds_write_b16_d16_hi v118, v101 offset:1108\n"
    "ds_write_b16 v118, v102 offset:1472\n"
    "ds_write_b16_d16_hi v118, v102 offset:1500\n"
    "ds_write_b16 v118, v103 offset:1528\n"
    "ds_write_b16_d16_hi v118, v103 offset:1556\n"
    "s_mov_b64 exec, -1\n"
    "v_and_b32 v248, 63, v112\n"
    "v_cmp_gt_u32 vcc, 56, v248\n"
    "v_lshlrev_b32 v248, 4, v248\n"
    "s_lshl_b32 s74, s52, 11\n"
    "s_add_u32 s74, s74, 131072\n"
    "v_add_u32 v249, s74, v248\n"
    "v_mov_b32 v250, 1073741824\n"
    "v_cndmask_b32 v248, v250, v248, vcc\n"
    "s_waitcnt lgkmcnt(0)\n"
    "ds_read_b128 v[204:207], v249 offset:0\n"
    "ds_read_b128 v[208:211], v249 offset:1024\n"
    "s_add_u32 s74, s62, 25165824\n"
    "s_add_u32 s75, s63, 25165824\n"
    "s_waitcnt lgkmcnt(1)\n"
    "buffer_store_dwordx4 v[204:207], v248, s[12:15], s74 offen sc1\n"
    "s_waitcnt lgkmcnt(0)\n"
    "buffer_store_dwordx4 v[208:211], v248, s[12:15], s75 offen sc1\n"
    "s_waitcnt lgkmcnt(0)\n"
    "s_barrier\n"
    "v_and_b32 v188, 15, v112\n"
    "v_bfe_u32 v189, v112, 4, 2\n"
    "v_mul_u32_u24 v113, 7168, v189\n"
    "s_mul_i32 s75, s56, 896\n"
    "s_add_u32 s75, s75, 28672\n"
    "v_add_u32 v113, s75, v113\n"
    "v_lshrrev_b32 v114, 3, v188\n"
    "s_mul_i32 s76, s51, -57344\n"
    "s_add_u32 s76, s76, 28672\n"
    "v_mul_lo_u32 v114, s76, v114\n"
    "v_add_u32 v114, v114, v113\n"
    "v_lshl_add_u32 v113, v188, 4, v113\n"
    "v_and_b32 v188, 7, v188\n"
    "v_lshl_add_u32 v114, v188, 4, v114\n"
    "v_and_b32 v188, 15, v112\n"
    "v_bfe_u32 v190, v112, 4, 2\n"
    "v_lshlrev_b32 v115, 10, v190\n"
    "v_and_b32 v190, 1, v190\n"
    "v_lshlrev_b32 v190, 1, v190\n"
    "v_bfe_u32 v116, v112, 3, 1\n"
    "v_or_b32 v190, v190, v116\n"
    "v_xor_b32 v190, s52, v190\n"
    "v_lshl_add_u32 v115, v190, 5, v115\n"
    "v_bfe_u32 v190, v112, 2, 2\n"
    "v_lshl_add_u32 v115, v190, 7, v115\n"
    "v_and_b32 v190, 3, v112\n"
    "v_lshl_add_u32 v115, v190, 3, v115\n"
    "v_add_u32 v115, s11, v115\n"
    "v_xor_b32 v116, 32, v115\n"
    "v_xor_b32 v117, 64, v115\n"
    "v_xor_b32 v118, 96, v115\n"
    "v_and_b32 v119, 63, v112\n"
    "v_mul_u32_u24 v250, 7168, v119\n"
    "v_cmp_gt_u32 vcc, 56, v119\n"
    "v_lshlrev_b32 v119, 4, v119\n"
    "s_lshl_b32 s74, s52, 13\n"
    "s_add_u32 s74, s74, 131072\n"
    "v_add_u32 v119, s74, v119\n"
    "v_xor_b32 v120, 8192, v119\n"
    "v_xor_b32 v121, 16384, v119\n"
    "v_xor_b32 v122, 24576, v119\n"
    "s_add_u32 s75, s75, 892\n"
    "v_add_u32 v125, s75, v250\n"
    "v_mov_b32 v190, 1073741824\n"
    "v_cndmask_b32 v250, v190, v125, vcc\n"
    "s_mul_i32 s71, s52, 458752\n"
    "s_add_u32 s71, s71, 20942848\n"
    "s_mul_i32 s82, s52, 401408\n"
    "s_add_u32 s82, s82, 23040000\n"
    "s_lshl_b32 s74, s57, 2\n"
    "s_add_u32 s74, s74, s52\n"
    "s_lshl_b32 s75, s74, 5\n"
    "v_lshrrev_b32 v191, 1, v189\n"
    "v_lshl_add_u32 v252, v191, 4, s75\n"
    "s_mul_i32 s74, s74, 14336\n"
    "s_mul_i32 s75, s56, 896\n"
    "s_add_u32 s74, s74, s75\n"
    "s_add_u32 s74, s74, 23068672\n"
    "s_add_u32 s74, s74, s7\n"
    "v_mul_u32_u24 v248, 7168, v191\n"
    "v_and_b32 v191, 31, v112\n"
    "v_lshl_add_u32 v248, v191, 4, v248\n"
    "v_add_u32 v248, s74, v248\n"
    "v_cmp_gt_u32 vcc, 24, v191\n"
    "s_lshl_b32 s74, s6, 7\n"
    "s_lshl_b32 s75, s52, 5\n"
    "s_add_u32 s74, s74, s75\n"
    "v_cndmask_b32 v249, v190, v248, vcc\n"
    "v_lshl_add_u32 v123, v188, 1, s74\n"
    "s_mov_b32 s64, 0xbdd2d3e8\n"
    "s_mov_b32 s65, 0xc0135761\n"
    "s_lshl_b32 s74, s6, 6\n"
    "s_lshl_b32 s75, s52, 4\n"
    "s_add_u32 s74, s74, s75\n"
    "v_add_u32 v104, s74, v188\n"
    "v_mul_u32_u24 v104, 896, v104\n"
    "s_mul_i32 s74, s56, 112\n"
    "s_add_u32 s74, s74, 25165824\n"
    "v_add_u32 v104, s74, v104\n"
    "v_lshl_add_u32 v105, v189, 3, v104\n"
    "v_lshl_add_u32 v104, v189, 4, v104\n"
    "v_mov_b32 v106, 1073741824\n"
    "v_cmp_gt_u32 vcc, 2, v189\n"
    "s_nop 1\n"
    "v_cndmask_b32 v106, v106, v105, vcc\n"
    "s_waitcnt vmcnt(0)\n"
    ".Lg4top_62:\n"
    "buffer_load_dword v124, v125, s[12:15], s71 offen sc1\n"
    "s_waitcnt vmcnt(0)\n"
    "v_cmp_eq_u32 vcc, -1, v124\n"
    "s_cbranch_vccnz .Lg4retry_63\n"
    ".Lg4ok_64:\n"
    "s_add_u32 s70, s71, 0\n"
    "buffer_load_dwordx4 v[188:191], v113, s[12:15], s70 offen offset:512 sc1\n"
    "buffer_load_dwordx4 v[192:195], v114, s[12:15], s70 offen offset:768 sc1\n"
    "s_add_u32 s70, s70, 28672\n"
    "buffer_load_dwordx4 v[196:199], v113, s[12:15], s70 offen offset:512 sc1\n"
    "s_add_u32 s70, s70, 28672\n"
    "buffer_load_dwordx4 v[200:203], v113, s[12:15], s70 offen offset:512 sc1\n"
    "buffer_load_dwordx4 v[204:207], v114, s[12:15], s70 offen offset:768 sc1\n"
    "s_add_u32 s70, s70, 28672\n"
    "buffer_load_dwordx4 v[208:211], v113, s[12:15], s70 offen offset:512 sc1\n"
    "s_add_u32 s70, s70, 28672\n"
    "buffer_load_dwordx4 v[212:215], v113, s[12:15], s70 offen offset:512 sc1\n"
    "buffer_load_dwordx4 v[216:219], v114, s[12:15], s70 offen offset:768 sc1\n"
    ".Lmark_p4in_65:\n"
    "s_waitcnt vmcnt(7)\n"
    "v_mfma_f32_16x16x32_bf16 v[124:127], a[0:3], v[188:191], 0\n"
    "s_add_u32 s70, s70, 28672\n"
    "buffer_load_dwordx4 v[220:223], v113, s[12:15], s70 offen offset:512 sc1\n"
    "v_mfma_f32_16x16x32_bf16 v[132:135], a[4:7], v[188:191], 0\n"
    "s_waitcnt vmcnt(7)\n"
    "v_cndmask_b32 v228, 0, v192, s[4:5]\n"
    "v_cndmask_b32 v229, 0, v193, s[4:5]\n"
    "v_mfma_f32_16x16x32_bf16 v[140:143], a[8:11], v[188:191], 0\n"
    "v_cndmask_b32 v230, 0, v194, s[4:5]\n"
    "v_cndmask_b32 v231, 0, v195, s[4:5]\n"
    "v_mfma_f32_16x16x32_bf16 v[148:151], a[12:15], v[188:191], 0\n"
    "s_add_u32 s70, s70, 28672\n"
    "buffer_load_dwordx4 v[224:227], v113, s[12:15], s70 offen offset:512 sc1\n"
    "v_mfma_f32_16x16x32_bf16 v[128:131], a[0:3], v[228:231], 0\n"
    "buffer_load_dwordx4 v[108:111], v114, s[12:15], s70 offen offset:768 sc1\n"
    "v_cndmask_b32 v192, v192, 0, s[4:5]\n"
    "v_mfma_f32_16x16x32_bf16 v[136:139], a[4:7], v[228:231], 0\n"
    "s_add_u32 s70, s70, 28672\n"
    "buffer_load_dwordx4 v[232:235], v113, s[12:15], s70 offen offset:512 sc1\n"
    "v_mfma_f32_16x16x32_bf16 v[144:147], a[8:11], v[228:231], 0\n"
    "s_add_u32 s70, s70, 28672\n"
    "buffer_load_dwordx4 v[236:239], v113, s[12:15], s70 offen offset:512 sc1\n"
    "buffer_load_dwordx4 v[188:191], v114, s[12:15], s70 offen offset:768 sc1\n"
    "v_mfma_f32_16x16x32_bf16 v[152:155], a[12:15], v[228:231], 0\n"
    "v_cndmask_b32 v193, v193, 0, s[4:5]\n"
    "v_cndmask_b32 v194, v194, 0, s[4:5]\n"
    "v_cndmask_b32 v195, v195, 0, s[4:5]\n"
    "s_waitcnt vmcnt(11)\n"
    "v_mfma_f32_16x16x32_bf16 v[124:127], a[16:19], v[196:199], v[124:127]\n"
    "buffer_load_dwordx4 v[252:255], v252, s[36:39], 0 offen\n"
    "buffer_load_ushort v123, v123, s[40:43], 0 offen\n"
    "v_mfma_f32_16x16x32_bf16 v[132:135], a[20:23], v[196:199], v[132:135]\n"
    "v_mfma_f32_16x16x32_bf16 v[140:143], a[24:27], v[196:199], v[140:143]\n"
    "v_mfma_f32_16x16x32_bf16 v[148:151], a[28:31], v[196:199], v[148:151]\n"
    "s_add_u32 s70, s70, 28672\n"
    "buffer_load_dwordx4 v[196:199], v113, s[12:15], s70 offen offset:512 sc1\n"
    "v_mfma_f32_16x16x32_bf16 v[128:131], a[16:19], v[192:195], v[128:131]\n"
    "v_mfma_f32_16x16x32_bf16 v[136:139], a[20:23], v[192:195], v[136:139]\n"
    "v_mfma_f32_16x16x32_bf16 v[144:147], a[24:27], v[192:195], v[144:147]\n"
    "s_waitcnt vmcnt(12)\n"
    "v_cndmask_b32 v228, 0, v204, s[4:5]\n"
    "v_cndmask_b32 v229, 0, v205, s[4:5]\n"
    "v_mfma_f32_16x16x32_bf16 v[152:155], a[28:31], v[192:195], v[152:155]\n"
    "v_cndmask_b32 v230, 0, v206, s[4:5]\n"
    "v_mfma_f32_16x16x32_bf16 v[124:127], a[32:35], v[200:203], v[124:127]\n"
    "s_add_u32 s70, s70, 28672\n"
    "buffer_load_dwordx4 v[192:195], v113, s[12:15], s70 offen offset:512 sc1\n"
    "v_mfma_f32_16x16x32_bf16 v[132:135], a[36:39], v[200:203], v[132:135]\n"
    "v_cndmask_b32 v231, 0, v207, s[4:5]\n"
    "v_mfma_f32_16x16x32_bf16 v[140:143], a[40:43], v[200:203], v[140:143]\n"
    "v_mfma_f32_16x16x32_bf16 v[148:151], a[44:47], v[200:203], v[148:151]\n"
    "buffer_load_dwordx4 v[200:203], v114, s[12:15], s70 offen offset:768 sc1\n"
    "v_mfma_f32_16x16x32_bf16 v[128:131], a[32:35], v[228:231], v[128:131]\n"
    "v_cndmask_b32 v204, v204, 0, s[4:5]\n"
    "v_cndmask_b32 v205, v205, 0, s[4:5]\n"
    "v_mfma_f32_16x16x32_bf16 v[136:139], a[36:39], v[228:231], v[136:139]\n"
    "v_cndmask_b32 v206, v206, 0, s[4:5]\n"
    "v_cndmask_b32 v207, v207, 0, s[4:5]\n"
    "v_mfma_f32_16x16x32_bf16 v[144:147], a[40:43], v[228:231], v[144:147]\n"
    "v_mfma_f32_16x16x32_bf16 v[152:155], a[44:47], v[228:231], v[152:155]\n"
    "s_waitcnt vmcnt(13)\n"
    "v_mfma_f32_16x16x32_bf16 v[124:127], a[48:51], v[208:211], v[124:127]\n"
    "v_mfma_f32_16x16x32_bf16 v[132:135], a[52:55], v[208:211], v[132:135]\n"
    "v_mfma_f32_16x16x32_bf16 v[140:143], a[56:59], v[208:211], v[140:143]\n"
    "v_mfma_f32_16x16x32_bf16 v[148:151], a[60:63], v[208:211], v[148:151]\n"
    "s_add_u32 s70, s70, 28672\n"
    "buffer_load_dwordx4 v[208:211], v113, s[12:15], s70 offen offset:512 sc1\n"
    "v_mfma_f32_16x16x32_bf16 v[128:131], a[48:51], v[204:207], v[128:131]\n"
    "v_mfma_f32_16x16x32_bf16 v[136:139], a[52:55], v[204:207], v[136:139]\n"
    "v_mfma_f32_16x16x32_bf16 v[144:147], a[56:59], v[204:207], v[144:147]\n"
    "s_waitcnt vmcnt(12)\n"
    "v_cndmask_b32 v228, 0, v216, s[4:5]\n"
    "v_cndmask_b32 v229, 0, v217, s[4:5]\n"
    "v_mfma_f32_16x16x32_bf16 v[152:155], a[60:63], v[204:207], v[152:155]\n"
    "v_cndmask_b32 v230, 0, v218, s[4:5]\n"
    "v_mfma_f32_16x16x32_bf16 v[124:127], a[64:67], v[212:215], v[124:127]\n"
    "s_add_u32 s70, s70, 28672\n"
    "buffer_load_dwordx4 v[204:207], v113, s[12:15], s70 offen offset:512 sc1\n"
    "v_mfma_f32_16x16x32_bf16 v[132:135], a[68:71], v[212:215], v[132:135]\n"
    "v_cndmask_b32 v231, 0, v219, s[4:5]\n"
    "v_mfma_f32_16x16x32_bf16 v[140:143], a[72:75], v[212:215], v[140:143]\n"
    "v_mfma_f32_16x16x32_bf16 v[148:151], a[76:79], v[212:215], v[148:151]\n"
    "buffer_load_dwordx4 v[212:215], v114, s[12:15], s70 offen offset:768 sc1\n"
    "v_mfma_f32_16x16x32_bf16 v[128:131], a[64:67], v[228:231], v[128:131]\n"
    "v_cndmask_b32 v216, v216, 0, s[4:5]\n"
    "v_cndmask_b32 v217, v217, 0, s[4:5]\n"
    "v_mfma_f32_16x16x32_bf16 v[136:139], a[68:71], v[228:231], v[136:139]\n"
    "v_cndmask_b32 v218, v218, 0, s[4:5]\n"
    "v_cndmask_b32 v219, v219, 0, s[4:5]\n"
    "v_mfma_f32_16x16x32_bf16 v[144:147], a[72:75], v[228:231], v[144:147]\n"
    "v_mfma_f32_16x16x32_bf16 v[152:155], a[76:79], v[228:231], v[152:155]\n"
    "s_waitcnt vmcnt(13)\n"
    "v_mfma_f32_16x16x32_bf16 v[124:127], a[80:83], v[220:223], v[124:127]\n"
    "v_mfma_f32_16x16x32_bf16 v[132:135], a[84:87], v[220:223], v[132:135]\n"
    "v_mfma_f32_16x16x32_bf16 v[140:143], a[88:91], v[220:223], v[140:143]\n"
    "v_mfma_f32_16x16x32_bf16 v[148:151], a[92:95], v[220:223], v[148:151]\n"
    "s_add_u32 s70, s70, 28672\n"
    "buffer_load_dwordx4 v[220:223], v113, s[12:15], s70 offen offset:512 sc1\n"
    "v_mfma_f32_16x16x32_bf16 v[128:131], a[80:83], v[216:219], v[128:131]\n"
    "v_mfma_f32_16x16x32_bf16 v[136:139], a[84:87], v[216:219], v[136:139]\n"
    "v_mfma_f32_16x16x32_bf16 v[144:147], a[88:91], v[216:219], v[144:147]\n"
    "s_waitcnt vmcnt(12)\n"
    "v_cndmask_b32 v228, 0, v108, s[4:5]\n"
    "v_cndmask_b32 v229, 0, v109, s[4:5]\n"
    "v_mfma_f32_16x16x32_bf16 v[152:155], a[92:95], v[216:219], v[152:155]\n"
    "v_cndmask_b32 v230, 0, v110, s[4:5]\n"
    "v_mfma_f32_16x16x32_bf16 v[124:127], a[96:99], v[224:227], v[124:127]\n"
    "s_add_u32 s70, s70, 28672\n"
    "buffer_load_dwordx4 v[216:219], v113, s[12:15], s70 offen offset:512 sc1\n"
    "v_mfma_f32_16x16x32_bf16 v[132:135], a[100:103], v[224:227], v[132:135]\n"
    "v_cndmask_b32 v231, 0, v111, s[4:5]\n"
    "v_mfma_f32_16x16x32_bf16 v[140:143], a[104:107], v[224:227], v[140:143]\n"
    "v_mfma_f32_16x16x32_bf16 v[148:151], a[108:111], v[224:227], v[148:151]\n"
    "buffer_load_dwordx4 v[224:227], v114, s[12:15], s70 offen offset:768 sc1\n"
    "v_mfma_f32_16x16x32_bf16 v[128:131], a[96:99], v[228:231], v[128:131]\n"
    "v_cndmask_b32 v108, v108, 0, s[4:5]\n"
    "v_cndmask_b32 v109, v109, 0, s[4:5]\n"
    "v_mfma_f32_16x16x32_bf16 v[136:139], a[100:103], v[228:231], v[136:139]\n"
    "v_cndmask_b32 v110, v110, 0, s[4:5]\n"
    "v_cndmask_b32 v111, v111, 0, s[4:5]\n"
    "v_mfma_f32_16x16x32_bf16 v[144:147], a[104:107], v[228:231], v[144:147]\n"
    "v_mfma_f32_16x16x32_bf16 v[152:155], a[108:111], v[228:231], v[152:155]\n"
    "s_waitcnt vmcnt(13)\n"
    "v_mfma_f32_16x16x32_bf16 v[124:127], a[112:115], v[232:235], v[124:127]\n"
    "v_mfma_f32_16x16x32_bf16 v[132:135], a[116:119], v[232:235], v[132:135]\n"
    "v_mfma_f32_16x16x32_bf16 v[140:143], a[120:123], v[232:235], v[140:143]\n"
    "v_mfma_f32_16x16x32_bf16 v[148:151], a[124:127], v[232:235], v[148:151]\n"
    "s_add_u32 s70, s70, 28672\n"
    "buffer_load_dwordx4 v[232:235], v113, s[12:15], s70 offen offset:512 sc1\n"
    "v_mfma_f32_16x16x32_bf16 v[128:131], a[112:115], v[108:111], v[128:131]\n"
    "v_mfma_f32_16x16x32_bf16 v[136:139], a[116:119], v[108:111], v[136:139]\n"
    "v_mfma_f32_16x16x32_bf16 v[144:147], a[120:123], v[108:111], v[144:147]\n"
    "s_waitcnt vmcnt(12)\n"
    "v_cndmask_b32 v228, 0, v188, s[4:5]\n"
    "v_cndmask_b32 v229, 0, v189, s[4:5]\n"
    "v_mfma_f32_16x16x32_bf16 v[152:155], a[124:127], v[108:111], v[152:155]\n"
    "v_cndmask_b32 v230, 0, v190, s[4:5]\n"
    "v_mfma_f32_16x16x32_bf16 v[124:127], a[128:131], v[236:239], v[124:127]\n"
    "s_add_u32 s70, s71, 0\n"
    "buffer_load_dwordx4 v[108:111], v113, s[12:15], s70 offen offset:0 sc1\n"
    "v_mfma_f32_16x16x32_bf16 v[132:135], a[132:135], v[236:239], v[132:135]\n"
    "v_cndmask_b32 v231, 0, v191, s[4:5]\n"
    "v_mfma_f32_16x16x32_bf16 v[140:143], a[136:139], v[236:239], v[140:143]\n"
    "buffer_load_dwordx4 v[96:99], v104, s[12:15], 0 offen sc1\n"
    "buffer_load_dwordx2 v[100:101], v105, s[12:15], 0 offen offset:64 sc1\n"
    "buffer_load_dwordx2 v[102:103], v106, s[12:15], 0 offen offset:96 sc1\n"
    "v_mfma_f32_16x16x32_bf16 v[148:151], a[140:143], v[236:239], v[148:151]\n"
    "buffer_load_dwordx4 v[236:239], v113, s[12:15], s70 offen offset:256 sc1\n"
    "v_mfma_f32_16x16x32_bf16 v[128:131], a[128:131], v[228:231], v[128:131]\n"
    "v_cndmask_b32 v188, v188, 0, s[4:5]\n"
    "v_cndmask_b32 v189, v189, 0, s[4:5]\n"
    "v_mfma_f32_16x16x32_bf16 v[136:139], a[132:135], v[228:231], v[136:139]\n"
    "v_cndmask_b32 v190, v190, 0, s[4:5]\n"
    "v_cndmask_b32 v191, v191, 0, s[4:5]\n"
    "v_mfma_f32_16x16x32_bf16 v[144:147], a[136:139], v[228:231], v[144:147]\n"
    "v_mfma_f32_16x16x32_bf16 v[152:155], a[140:143], v[228:231], v[152:155]\n"
    "s_waitcnt vmcnt(14)\n"
    "v_mfma_f32_16x16x32_bf16 v[124:127], a[144:147], v[196:199], v[124:127]\n"
    "v_mfma_f32_16x16x32_bf16 v[132:135], a[148:151], v[196:199], v[132:135]\n"
    "v_mfma_f32_16x16x32_bf16 v[140:143], a[152:155], v[196:199], v[140:143]\n"
    "v_mfma_f32_16x16x32_bf16 v[148:151], a[156:159], v[196:199], v[148:151]\n"
    "s_add_u32 s70, s70, 28672\n"
    "buffer_load_dwordx4 v[196:199], v113, s[12:15], s70 offen offset:0 sc1\n"
    "v_mfma_f32_16x16x32_bf16 v[128:131], a[144:147], v[188:191], v[128:131]\n"
    "v_mfma_f32_16x16x32_bf16 v[136:139], a[148:151], v[188:191], v[136:139]\n"
    "v_mfma_f32_16x16x32_bf16 v[144:147], a[152:155], v[188:191], v[144:147]\n"
    "s_waitcnt vmcnt(13)\n"
    "v_cndmask_b32 v228, 0, v200, s[4:5]\n"
    "v_cndmask_b32 v229, 0, v201, s[4:5]\n"
    "v_mfma_f32_16x16x32_bf16 v[152:155], a[156:159], v[188:191], v[152:155]\n"
    "buffer_load_dwordx4 v[188:191], v113, s[12:15], s70 offen offset:256 sc1\n"
    "v_mfma_f32_16x16x32_bf16 v[124:127], a[160:163], v[192:195], v[124:127]\n"
    "v_cndmask_b32 v230, 0, v202, s[4:5]\n"
    "v_cndmask_b32 v231, 0, v203, s[4:5]\n"
    "v_mfma_f32_16x16x32_bf16 v[132:135], a[164:167], v[192:195], v[132:135]\n"
    "v_mfma_f32_16x16x32_bf16 v[140:143], a[168:171], v[192:195], v[140:143]\n"
    "v_mfma_f32_16x16x32_bf16 v[148:151], a[172:175], v[192:195], v[148:151]\n"
    "v_mfma_f32_16x16x32_bf16 v[128:131], a[160:163], v[228:231], v[128:131]\n"
    "s_add_u32 s70, s70, 28672\n"
    "buffer_load_dwordx4 v[192:195], v113, s[12:15], s70 offen offset:0 sc1\n"
    "v_mfma_f32_16x16x32_bf16 v[136:139], a[164:167], v[228:231], v[136:139]\n"
    "v_cndmask_b32 v200, v200, 0, s[4:5]\n"
    "v_cndmask_b32 v201, v201, 0, s[4:5]\n"
    "v_mfma_f32_16x16x32_bf16 v[144:147], a[168:171], v[228:231], v[144:147]\n"
    "v_cndmask_b32 v202, v202, 0, s[4:5]\n"
    "v_cndmask_b32 v203, v203, 0, s[4:5]\n"
    "v_mfma_f32_16x16x32_bf16 v[152:155], a[172:175], v[228:231], v[152:155]\n"
    "s_waitcnt vmcnt(14)\n"
    "v_mfma_f32_16x16x32_bf16 v[124:127], a[176:179], v[208:211], v[124:127]\n"
    "v_mfma_f32_16x16x32_bf16 v[132:135], a[180:183], v[208:211], v[132:135]\n"
    "v_mfma_f32_16x16x32_bf16 v[140:143], a[184:187], v[208:211], v[140:143]\n"
    "v_lshlrev_b32 v123, 16, v123\n"
    "v_mfma_f32_16x16x32_bf16 v[148:151], a[188:191], v[208:211], v[148:151]\n"
    "buffer_load_dwordx4 v[208:211], v113, s[12:15], s70 offen offset:256 sc1\n"
    "v_mfma_f32_16x16x32_bf16 v[128:131], a[176:179], v[200:203], v[128:131]\n"
    "v_mfma_f32_16x16x32_bf16 v[136:139], a[180:183], v[200:203], v[136:139]\n"
    "v_mfma_f32_16x16x32_bf16 v[144:147], a[184:187], v[200:203], v[144:147]\n"
    "s_waitcnt vmcnt(13)\n"
    "v_cndmask_b32 v228, 0, v212, s[4:5]\n"
    "v_cndmask_b32 v229, 0, v213, s[4:5]\n"
    "v_mfma_f32_16x16x32_bf16 v[152:155], a[188:191], v[200:203], v[152:155]\n"
    "v_cndmask_b32 v230, 0, v214, s[4:5]\n"
    "v_mfma_f32_16x16x32_bf16 v[124:127], a[192:195], v[204:207], v[124:127]\n"
    "s_add_u32 s70, s70, 28672\n"
    "buffer_load_dwordx4 v[200:203], v113, s[12:15], s70 offen offset:0 sc1\n"
    "v_mfma_f32_16x16x32_bf16 v[132:135], a[196:199], v[204:207], v[132:135]\n"
    "v_cndmask_b32 v231, 0, v215, s[4:5]\n"
    "v_mfma_f32_16x16x32_bf16 v[140:143], a[200:203], v[204:207], v[140:143]\n"
    "v_mfma_f32_16x16x32_bf16 v[148:151], a[204:207], v[204:207], v[148:151]\n"
    "buffer_load_dwordx4 v[204:207], v113, s[12:15], s70 offen offset:256 sc1\n"
    "v_mfma_f32_16x16x32_bf16 v[128:131], a[192:195], v[228:231], v[128:131]\n"
    "v_cndmask_b32 v212, v212, 0, s[4:5]\n"
    "v_cndmask_b32 v213, v213, 0, s[4:5]\n"
    "v_mfma_f32_16x16x32_bf16 v[136:139], a[196:199], v[228:231], v[136:139]\n"
    "v_cndmask_b32 v214, v214, 0, s[4:5]\n"
    "v_cndmask_b32 v215, v215, 0, s[4:5]\n"
    "v_mfma_f32_16x16x32_bf16 v[144:147], a[200:203], v[228:231], v[144:147]\n"
    "v_mfma_f32_16x16x32_bf16 v[152:155], a[204:207], v[228:231], v[152:155]\n"
    "s_waitcnt vmcnt(14)\n"
    "v_mfma_f32_16x16x32_bf16 v[124:127], a[208:211], v[220:223], v[124:127]\n"
    "v_mfma_f32_16x16x32_bf16 v[132:135], a[212:215], v[220:223], v[132:135]\n"
    "v_mfma_f32_16x16x32_bf16 v[140:143], a[216:219], v[220:223], v[140:143]\n"
    "v_mfma_f32_16x16x32_bf16 v[148:151], a[220:223], v[220:223], v[148:151]\n"
    "s_add_u32 s70, s70, 28672\n"
    "buffer_load_dwordx4 v[220:223], v113, s[12:15], s70 offen offset:0 sc1\n"
    "v_mfma_f32_16x16x32_bf16 v[128:131], a[208:211], v[212:215], v[128:131]\n"
    "v_mfma_f32_16x16x32_bf16 v[136:139], a[212:215], v[212:215], v[136:139]\n"
    "v_mfma_f32_16x16x32_bf16 v[144:147], a[216:219], v[212:215], v[144:147]\n"
    "s_waitcnt vmcnt(13)\n"
    "v_cndmask_b32 v228, 0, v224, s[4:5]\n"
    "v_cndmask_b32 v229, 0, v225, s[4:5]\n"
    "v_mfma_f32_16x16x32_bf16 v[152:155], a[220:223], v[212:215], v[152:155]\n"
    "buffer_load_dwordx4 v[212:215], v113, s[12:15], s70 offen offset:256 sc1\n"
    "v_mfma_f32_16x16x32_bf16 v[124:127], a[224:227], v[216:219], v[124:127]\n"
    "v_cndmask_b32 v230, 0, v226, s[4:5]\n"
    "v_cndmask_b32 v231, 0, v227, s[4:5]\n"
    "v_mfma_f32_16x16x32_bf16 v[132:135], a[228:231], v[216:219], v[132:135]\n"
    "v_mfma_f32_16x16x32_bf16 v[140:143], a[232:235], v[216:219], v[140:143]\n"
    "v_mfma_f32_16x16x32_bf16 v[148:151], a[236:239], v[216:219], v[148:151]\n"
    "v_mfma_f32_16x16x32_bf16 v[128:131], a[224:227], v[228:231], v[128:131]\n"
    "s_add_u32 s70, s70, 28672\n"
    "buffer_load_dwordx4 v[216:219], v113, s[12:15], s70 offen offset:0 sc1\n"
    "v_mfma_f32_16x16x32_bf16 v[136:139], a[228:231], v[228:231], v[136:139]\n"
    "v_cndmask_b32 v224, v224, 0, s[4:5]\n"
    "v_cndmask_b32 v225, v225, 0, s[4:5]\n"
    "v_mfma_f32_16x16x32_bf16 v[144:147], a[232:235], v[228:231], v[144:147]\n"
    "v_cndmask_b32 v226, v226, 0, s[4:5]\n"
    "v_cndmask_b32 v227, v227, 0, s[4:5]\n"
    "v_mfma_f32_16x16x32_bf16 v[152:155], a[236:239], v[228:231], v[152:155]\n"
    "s_waitcnt vmcnt(14)\n"
    "v_mfma_f32_16x16x32_bf16 v[124:127], a[240:243], v[232:235], v[124:127]\n"
    "v_mfma_f32_16x16x32_bf16 v[132:135], a[244:247], v[232:235], v[132:135]\n"
    "v_mfma_f32_16x16x32_bf16 v[140:143], a[248:251], v[232:235], v[140:143]\n"
    "v_mfma_f32_16x16x32_bf16 v[148:151], a[252:255], v[232:235], v[148:151]\n"
    "buffer_load_dwordx4 v[232:235], v113, s[12:15], s70 offen offset:256 sc1\n"
    "v_mfma_f32_16x16x32_bf16 v[128:131], a[240:243], v[224:227], v[128:131]\n"
    "v_mfma_f32_16x16x32_bf16 v[136:139], a[244:247], v[224:227], v[136:139]\n"
    "v_mfma_f32_16x16x32_bf16 v[144:147], a[248:251], v[224:227], v[144:147]\n"
    "v_mfma_f32_16x16x32_bf16 v[152:155], a[252:255], v[224:227], v[152:155]\n"
    ".Lmark_p4k_66:\n"
    "s_waitcnt vmcnt(14)\n"
    "v_mfma_f32_16x16x32_bf16 v[156:159], a[0:3], v[108:111], 0\n"
    "s_add_u32 s70, s70, 28672\n"
    "buffer_load_dwordx4 v[224:227], v113, s[12:15], s70 offen offset:0 sc1\n"
    "v_mfma_f32_16x16x32_bf16 v[164:167], a[4:7], v[108:111], 0\n"
    "v_cmp_u_f32 vcc, v132, v136\n"
    "s_cbranch_vccnz .Lredo_71\n"
    ".Lredook_72:\n"
    ".Lback_68:\n"
    "ds_write_b128 v119, v[132:135] offset:0\n"
    "ds_write_b128 v119, v[136:139] offset:1024\n"
    "ds_write_b128 v119, v[140:143] offset:2048\n"
    "ds_write_b128 v119, v[144:147] offset:3072\n"
    "ds_write_b128 v119, v[148:151] offset:4096\n"
    "ds_write_b128 v119, v[152:155] offset:5120\n"
    "v_mfma_f32_16x16x32_bf16 v[172:175], a[8:11], v[108:111], 0\n"
    "s_waitcnt lgkmcnt(0)\n"
    "s_barrier\n"
    "ds_read_b128 v[132:135], v120 offset:0\n"
    "ds_read_b128 v[136:139], v120 offset:1024\n"
    "ds_read_b128 v[140:143], v121 offset:2048\n"
    "ds_read_b128 v[144:147], v121 offset:3072\n"
    "ds_read_b128 v[148:151], v122 offset:4096\n"
    "ds_read_b128 v[152:155], v122 offset:5120\n"
    "v_mfma_f32_16x16x32_bf16 v[180:183], a[12:15], v[108:111], 0\n"
    "buffer_load_dwordx4 v[108:111], v113, s[12:15], s70 offen offset:256 sc1\n"
    "s_waitcnt vmcnt(12)\n"
    "v_mfma_f32_16x16x32_bf16 v[160:163], a[0:3], v[236:239], 0\n"
    "v_mfma_f32_16x16x32_bf16 v[168:171], a[4:7], v[236:239], 0\n"
    "v_mfma_f32_16x16x32_bf16 v[176:179], a[8:11], v[236:239], 0\n"
    "v_mfma_f32_16x16x32_bf16 v[184:187], a[12:15], v[236:239], 0\n"
    "s_waitcnt vmcnt(11)\n"
    "v_mfma_f32_16x16x32_bf16 v[156:159], a[16:19], v[196:199], v[156:159]\n"
    "s_add_u32 s70, s70, 28672\n"
    "buffer_load_dwordx4 v[236:239], v113, s[12:15], s70 offen offset:0 sc1\n"
    "s_waitcnt lgkmcnt(3)\n"
    "v_pk_add_f32 v[132:133], v[132:133], v[140:141]\n"
    "v_pk_add_f32 v[134:135], v[134:135], v[142:143]\n"
    "s_waitcnt lgkmcnt(2)\n"
    "v_pk_add_f32 v[136:137], v[136:137], v[144:145]\n"
    "v_pk_add_f32 v[138:139], v[138:139], v[146:147]\n"
    "s_waitcnt lgkmcnt(1)\n"
    "v_pk_add_f32 v[124:125], v[124:125], v[148:149]\n"
    "v_pk_add_f32 v[126:127], v[126:127], v[150:151]\n"
    "s_waitcnt lgkmcnt(0)\n"
    "v_pk_add_f32 v[128:129], v[128:129], v[152:153]\n"
    "v_pk_add_f32 v[130:131], v[130:131], v[154:155]\n"
    "v_pk_add_f32 v[124:125], v[124:125], v[132:133]\n"
    "v_pk_add_f32 v[126:127], v[126:127], v[134:135]\n"
    "v_pk_add_f32 v[128:129], v[128:129], v[136:137]\n"
    "v_pk_add_f32 v[130:131], v[130:131], v[138:139]\n"
    "v_lshlrev_b32 v140, 16, v252\n"
    "v_and_b32 v141, 0xffff0000, v252\n"
    "v_lshlrev_b32 v142, 16, v253\n"
    "v_and_b32 v143, 0xffff0000, v253\n"
    "v_lshlrev_b32 v144, 16, v254\n"
    "v_and_b32 v145, 0xffff0000, v254\n"
    "v_lshlrev_b32 v146, 16, v255\n"
    "v_and_b32 v147, 0xffff0000, v255\n"
    "v_mov_b32 v152, v249\n"
    "v_add_f32_dpp v128, v128, v128 row_ror:8 row_mask:0xf bank_mask:0xf\n"
    "v_add_f32_dpp v129, v129, v129 row_ror:8 row_mask:0xf bank_mask:0xf\n"
    "v_add_f32_dpp v130, v130, v130 row_ror:8 row_mask:0xf bank_mask:0xf\n"
    "v_add_f32_dpp v131, v131, v131 row_ror:8 row_mask:0xf bank_mask:0xf\n"
    "v_permlane16_swap_b32 v124, v128\n"
    "v_permlane16_swap_b32 v125, v129\n"
    "v_permlane16_swap_b32 v126, v130\n"
    "v_permlane16_swap_b32 v127, v131\n"
    "v_pk_add_f32 v[124:125], v[124:125], v[140:141]\n"
    "v_pk_add_f32 v[126:127], v[126:127], v[142:143]\n"
    "v_pk_add_f32 v[128:129], v[128:129], v[144:145]\n"
    "v_pk_add_f32 v[130:131], v[130:131], v[146:147]\n"
    "v_pk_mul_f32 v[132:133], v[124:125], v[124:125]\n"
    "v_pk_mul_f32 v[134:135], v[126:127], v[126:127]\n"
    "v_pk_mul_f32 v[136:137], v[128:129], v[128:129]\n"
    "v_pk_mul_f32 v[138:139], v[130:131], v[130:131]\n"
    "v_pk_fma_f32 v[132:133], v[132:133], s[64:65], s[64:65] op_sel:[0,0,1] op_sel_hi:[1,0,1]\n"
    "v_pk_fma_f32 v[134:135], v[134:135], s[64:65], s[64:65] op_sel:[0,0,1] op_sel_hi:[1,0,1]\n"
    "v_pk_fma_f32 v[136:137], v[136:137], s[64:65], s[64:65] op_sel:[0,0,1] op_sel_hi:[1,0,1]\n"
    "v_pk_fma_f32 v[138:139], v[138:139], s[64:65], s[64:65] op_sel:[0,0,1] op_sel_hi:[1,0,1]\n"
    "v_pk_mul_f32 v[132:133], v[132:133], v[124:125]\n"
    "v_pk_mul_f32 v[134:135], v[134:135], v[126:127]\n"
    "v_pk_mul_f32 v[136:137], v[136:137], v[128:129]\n"
    "v_pk_mul_f32 v[138:139], v[138:139], v[130:131]\n"
    "v_exp_f32 v132, v132\n"
    "v_exp_f32 v133, v133\n"
    "v_exp_f32 v134, v134\n"
    "v_exp_f32 v135, v135\n"
    "v_exp_f32 v136, v136\n"
    "v_exp_f32 v137, v137\n"
    "v_exp_f32 v138, v138\n"
    "v_exp_f32 v139, v139\n"
    "v_pk_add_f32 v[132:133], v[132:133], 1.0 op_sel_hi:[1,0]\n"
    "v_pk_add_f32 v[134:135], v[134:135], 1.0 op_sel_hi:[1,0]\n"
    "v_pk_add_f32 v[136:137], v[136:137], 1.0 op_sel_hi:[1,0]\n"
    "v_pk_add_f32 v[138:139], v[138:139], 1.0 op_sel_hi:[1,0]\n"
    "v_rcp_f32 v132, v132\n"
    "v_rcp_f32 v133, v133\n"
    "v_rcp_f32 v134, v134\n"
    "v_rcp_f32 v135, v135\n"
    "v_rcp_f32 v136, v136\n"
    "v_rcp_f32 v137, v137\n"
    "v_rcp_f32 v138, v138\n"
    "v_rcp_f32 v139, v139\n"
    "v_pk_mul_f32 v[132:133], v[132:133], v[124:125]\n"
    "v_pk_mul_f32 v[134:135], v[134:135], v[126:127]\n"
    "v_pk_mul_f32 v[136:137], v[136:137], v[128:129]\n"
    "v_pk_mul_f32 v[138:139], v[138:139], v[130:131]\n"
    "v_cvt_pk_bf16_f32 v148, v132, v133\n"
    "v_cvt_pk_bf16_f32 v149, v134, v135\n"
    "v_cvt_pk_bf16_f32 v150, v136, v137\n"
    "v_cvt_pk_bf16_f32 v151, v138, v139\n"
    "buffer_store_dwordx4 v[148:151], v152, s[12:15], 0 offen offset:512 sc1\n"
    "v_max3_u32 v132, v96, v97, v98\n"
    "v_max3_u32 v132, v132, v99, v100\n"
    "v_max3_u32 v132, v132, v101, v102\n"
    "v_max_u32 v132, v132, v103\n"
    "v_cmp_eq_u32 vcc, -1, v132\n"
    "s_cbranch_vccnz .Lretry_73\n"
    ".Lx4_74:\n"
    "v_lshlrev_b32 v124, 16, v100\n"
    "v_mfma_f32_16x16x32_bf16 v[164:167], a[20:23], v[196:199], v[164:167]\n"
    "v_and_b32 v125, 0xffff0000, v100\n"
    "v_lshlrev_b32 v126, 16, v101\n"
    "v_and_b32 v127, 0xffff0000, v101\n"
    "v_lshlrev_b32 v128, 16, v102\n"
    "v_and_b32 v129, 0xffff0000, v102\n"
    "v_lshlrev_b32 v130, 16, v103\n"
    "v_and_b32 v131, 0xffff0000, v103\n"
    "v_add_f32 v124, v124, v123\n"
    "v_add_f32 v125, v125, v123\n"
    "v_add_f32 v126, v126, v123\n"
    "v_add_f32 v127, v127, v123\n"
    "v_fma_f32 v128, v123, 0.5, v128\n"
    "v_fma_f32 v129, v123, 0.5, v129\n"
    "v_fma_f32 v130, v123, 0.5, v130\n"
    "v_fma_f32 v131, v123, 0.5, v131\n"
    "s_waitcnt lgkmcnt(0)\n"
    "s_barrier\n"
    "v_mfma_f32_16x16x32_bf16 v[172:175], a[24:27], v[196:199], v[172:175]\n"
    "v_mfma_f32_16x16x32_bf16 v[180:183], a[28:31], v[196:199], v[180:183]\n"
    "buffer_load_dwordx4 v[196:199], v113, s[12:15], s70 offen offset:256 sc1\n"
    "s_waitcnt vmcnt(13)\n"
    "v_mfma_f32_16x16x32_bf16 v[160:163], a[16:19], v[188:191], v[160:163]\n"
    "v_mfma_f32_16x16x32_bf16 v[168:171], a[20:23], v[188:191], v[168:171]\n"
    "v_mfma_f32_16x16x32_bf16 v[176:179], a[24:27], v[188:191], v[176:179]\n"
    "v_mfma_f32_16x16x32_bf16 v[184:187], a[28:31], v[188:191], v[184:187]\n"
    "s_waitcnt vmcnt(12)\n"
    "v_mfma_f32_16x16x32_bf16 v[156:159], a[32:35], v[192:195], v[156:159]\n"
    "s_add_u32 s70, s70, 28672\n"
    "buffer_load_dwordx4 v[188:191], v113, s[12:15], s70 offen offset:0 sc1\n"
    "v_mfma_f32_16x16x32_bf16 v[164:167], a[36:39], v[192:195], v[164:167]\n"
    "v_mfma_f32_16x16x32_bf16 v[172:175], a[40:43], v[192:195], v[172:175]\n"
    "v_mfma_f32_16x16x32_bf16 v[180:183], a[44:47], v[192:195], v[180:183]\n"
    "buffer_load_dwordx4 v[192:195], v113, s[12:15], s70 offen offset:256 sc1\n"
    "s_waitcnt vmcnt(13)\n"
    "v_mfma_f32_16x16x32_bf16 v[160:163], a[32:35], v[208:211], v[160:163]\n"
    "v_mfma_f32_16x16x32_bf16 v[168:171], a[36:39], v[208:211], v[168:171]\n"
    "v_mfma_f32_16x16x32_bf16 v[176:179], a[40:43], v[208:211], v[176:179]\n"
    "v_mfma_f32_16x16x32_bf16 v[184:187], a[44:47], v[208:211], v[184:187]\n"
    "s_waitcnt vmcnt(12)\n"
    "v_mfma_f32_16x16x32_bf16 v[156:159], a[48:51], v[200:203], v[156:159]\n"
    "s_add_u32 s70, s70, 28672\n"
    "buffer_load_dwordx4 v[208:211], v113, s[12:15], s70 offen offset:0 sc1\n"
    "v_mfma_f32_16x16x32_bf16 v[164:167], a[52:55], v[200:203], v[164:167]\n"
    "v_mfma_f32_16x16x32_bf16 v[172:175], a[56:59], v[200:203], v[172:175]\n"
    "v_mfma_f32_16x16x32_bf16 v[180:183], a[60:63], v[200:203], v[180:183]\n"
    "buffer_load_dwordx4 v[200:203], v113, s[12:15], s70 offen offset:256 sc1\n"
    "s_waitcnt vmcnt(13)\n"
    "v_mfma_f32_16x16x32_bf16 v[160:163], a[48:51], v[204:207], v[160:163]\n"
    "v_mfma_f32_16x16x32_bf16 v[168:171], a[52:55], v[204:207], v[168:171]\n"
    "v_mfma_f32_16x16x32_bf16 v[176:179], a[56:59], v[204:207], v[176:179]\n"
    "v_mfma_f32_16x16x32_bf16 v[184:187], a[60:63], v[204:207], v[184:187]\n"
    "s_waitcnt vmcnt(12)\n"
    "v_mfma_f32_16x16x32_bf16 v[156:159], a[64:67], v[220:223], v[156:159]\n"
    "s_add_u32 s70, s70, 28672\n"
    "buffer_load_dwordx4 v[204:207], v113, s[12:15], s70 offen offset:0 sc1\n"
    "v_mfma_f32_16x16x32_bf16 v[164:167], a[68:71], v[220:223], v[164:167]\n"
    "v_mfma_f32_16x16x32_bf16 v[172:175], a[72:75], v[220:223], v[172:175]\n"
    "v_mfma_f32_16x16x32_bf16 v[180:183], a[76:79], v[220:223], v[180:183]\n"
    "buffer_load_dwordx4 v[220:223], v113, s[12:15], s70 offen offset:256 sc1\n"
    "s_waitcnt vmcnt(13)\n"
    "v_mfma_f32_16x16x32_bf16 v[160:163], a[64:67], v[212:215], v[160:163]\n"
    "v_mfma_f32_16x16x32_bf16 v[168:171], a[68:71], v[212:215], v[168:171]\n"
    "v_mfma_f32_16x16x32_bf16 v[176:179], a[72:75], v[212:215], v[176:179]\n"
    "v_mfma_f32_16x16x32_bf16 v[184:187], a[76:79], v[212:215], v[184:187]\n"
    "s_waitcnt vmcnt(12)\n"
    "v_mfma_f32_16x16x32_bf16 v[156:159], a[80:83], v[216:219], v[156:159]\n"
    "s_add_u32 s70, s70, 28672\n"
    "buffer_load_dwordx4 v[212:215], v113, s[12:15], s70 offen offset:0 sc1\n"
    "v_mfma_f32_16x16x32_bf16 v[164:167], a[84:87], v[216:219], v[164:167]\n"
    "v_mfma_f32_16x16x32_bf16 v[172:175], a[88:91], v[216:219], v[172:175]\n"
    "v_mfma_f32_16x16x32_bf16 v[180:183], a[92:95], v[216:219], v[180:183]\n"
    "buffer_load_dwordx4 v[216:219], v113, s[12:15], s70 offen offset:256 sc1\n"
    "s_waitcnt vmcnt(13)\n"
    "v_mfma_f32_16x16x32_bf16 v[160:163], a[80:83], v[232:235], v[160:163]\n"
    "v_mfma_f32_16x16x32_bf16 v[168:171], a[84:87], v[232:235], v[168:171]\n"
    "v_mfma_f32_16x16x32_bf16 v[176:179], a[88:91], v[232:235], v[176:179]\n"
    "v_mfma_f32_16x16x32_bf16 v[184:187], a[92:95], v[232:235], v[184:187]\n"
    "s_waitcnt vmcnt(12)\n"
    "v_mfma_f32_16x16x32_bf16 v[156:159], a[96:99], v[224:227], v[156:159]\n"
    "s_add_u32 s70, s70, 28672\n"
    "buffer_load_dwordx4 v[232:235], v113, s[12:15], s70 offen offset:0 sc1\n"
    "v_mfma_f32_16x16x32_bf16 v[164:167], a[100:103], v[224:227], v[164:167]\n"
    "v_mfma_f32_16x16x32_bf16 v[172:175], a[104:107], v[224:227], v[172:175]\n"
    "v_mfma_f32_16x16x32_bf16 v[180:183], a[108:111], v[224:227], v[180:183]\n"
    "buffer_load_dwordx4 v[224:227], v113, s[12:15], s70 offen offset:256 sc1\n"
    "s_waitcnt vmcnt(13)\n"
    "v_mfma_f32_16x16x32_bf16 v[160:163], a[96:99], v[108:111], v[160:163]\n"
    "v_mfma_f32_16x16x32_bf16 v[168:171], a[100:103], v[108:111], v[168:171]\n"
    "v_mfma_f32_16x16x32_bf16 v[176:179], a[104:107], v[108:111], v[176:179]\n"
    "v_mfma_f32_16x16x32_bf16 v[184:187], a[108:111], v[108:111], v[184:187]\n"
    "s_waitcnt vmcnt(12)\n"
    "v_mfma_f32_16x16x32_bf16 v[156:159], a[112:115], v[236:239], v[156:159]\n"
    "s_add_u32 s70, s70, 28672\n"
    "buffer_load_dwordx4 v[108:111], v113, s[12:15], s70 offen offset:0 sc1\n"
    "v_mfma_f32_16x16x32_bf16 v[164:167], a[116:119], v[236:239], v[164:167]\n"
    "v_mfma_f32_16x16x32_bf16 v[172:175], a[120:123], v[236:239], v[172:175]\n"
    "v_mfma_f32_16x16x32_bf16 v[180:183], a[124:127], v[236:239], v[180:183]\n"
    "buffer_load_dwordx4 v[236:239], v113, s[12:15], s70 offen offset:256 sc1\n"
    "s_waitcnt vmcnt(12)\n"
    "v_mfma_f32_16x16x32_bf16 v[160:163], a[112:115], v[196:199], v[160:163]\n"
    "v_mfma_f32_16x16x32_bf16 v[168:171], a[116:119], v[196:199], v[168:171]\n"
    "v_mfma_f32_16x16x32_bf16 v[176:179], a[120:123], v[196:199], v[176:179]\n"
    "v_mfma_f32_16x16x32_bf16 v[184:187], a[124:127], v[196:199], v[184:187]\n"
    "s_waitcnt vmcnt(11)\n"
    "v_mfma_f32_16x16x32_bf16 v[156:159], a[128:131], v[188:191], v[156:159]\n"
    "s_add_u32 s70, s70, 28672\n"
    "buffer_load_dwordx4 v[196:199], v113, s[12:15], s70 offen offset:0 sc1\n"
    "v_mfma_f32_16x16x32_bf16 v[164:167], a[132:135], v[188:191], v[164:167]\n"
    "v_mfma_f32_16x16x32_bf16 v[172:175], a[136:139], v[188:191], v[172:175]\n"
    "v_mfma_f32_16x16x32_bf16 v[180:183], a[140:143], v[188:191], v[180:183]\n"
    "buffer_load_dwordx4 v[188:191], v113, s[12:15], s70 offen offset:256 sc1\n"
    "s_waitcnt vmcnt(12)\n"
    "v_mfma_f32_16x16x32_bf16 v[160:163], a[128:131], v[192:195], v[160:163]\n"
    "v_mfma_f32_16x16x32_bf16 v[168:171], a[132:135], v[192:195], v[168:171]\n"
    "v_mfma_f32_16x16x32_bf16 v[176:179], a[136:139], v[192:195], v[176:179]\n"
    "v_mfma_f32_16x16x32_bf16 v[184:187], a[140:143], v[192:195], v[184:187]\n"
    "s_waitcnt vmcnt(11)\n"
    "v_mfma_f32_16x16x32_bf16 v[156:159], a[144:147], v[208:211], v[156:159]\n"
    "s_add_u32 s70, s70, 28672\n"
    "buffer_load_dwordx4 v[192:195], v113, s[12:15], s70 offen offset:0 sc1\n"
    "v_mfma_f32_16x16x32_bf16 v[164:167], a[148:151], v[208:211], v[164:167]\n"
    "v_mfma_f32_16x16x32_bf16 v[172:175], a[152:155], v[208:211], v[172:175]\n"
    "v_mfma_f32_16x16x32_bf16 v[180:183], a[156:159], v[208:211], v[180:183]\n"
    "buffer_load_dwordx4 v[208:211], v113, s[12:15], s70 offen offset:256 sc1\n"
    "s_waitcnt vmcnt(12)\n"
    "v_mfma_f32_16x16x32_bf16 v[160:163], a[144:147], v[200:203], v[160:163]\n"
    "v_mfma_f32_16x16x32_bf16 v[168:171], a[148:151], v[200:203], v[168:171]\n"
    "v_mfma_f32_16x16x32_bf16 v[176:179], a[152:155], v[200:203], v[176:179]\n"
    "v_mfma_f32_16x16x32_bf16 v[184:187], a[156:159], v[200:203], v[184:187]\n"
    "s_waitcnt vmcnt(11)\n"
    "v_mfma_f32_16x16x32_bf16 v[156:159], a[160:163], v[204:207], v[156:159]\n"
    "buffer_load_dword v251, v250, s[12:15], s82 offen sc1\n"
    "s_add_u32 s70, s82, 0\n"
    "buffer_load_dwordx4 v[200:203], v113, s[12:15], s70 offen offset:512 sc1\n"
    "v_mfma_f32_16x16x32_bf16 v[164:167], a[164:167], v[204:207], v[164:167]\n"
    "v_mfma_f32_16x16x32_bf16 v[172:175], a[168:171], v[204:207], v[172:175]\n"
    "v_mfma_f32_16x16x32_bf16 v[180:183], a[172:175], v[204:207], v[180:183]\n"
    "buffer_load_dwordx4 v[204:207], v114, s[12:15], s70 offen offset:768 sc1\n"
    "s_waitcnt vmcnt(13)\n"
    "v_mfma_f32_16x16x32_bf16 v[160:163], a[160:163], v[220:223], v[160:163]\n"
    "v_mfma_f32_16x16x32_bf16 v[168:171], a[164:167], v[220:223], v[168:171]\n"
    "v_mfma_f32_16x16x32_bf16 v[176:179], a[168:171], v[220:223], v[176:179]\n"
    "v_mfma_f32_16x16x32_bf16 v[184:187], a[172:175], v[220:223], v[184:187]\n"
    "s_waitcnt vmcnt(12)\n"
    "v_mfma_f32_16x16x32_bf16 v[156:159], a[176:179], v[212:215], v[156:159]\n"
    "s_add_u32 s70, s70, 28672\n"
    "buffer_load_dwordx4 v[220:223], v113, s[12:15], s70 offen offset:512 sc1\n"
    "v_mfma_f32_16x16x32_bf16 v[164:167], a[180:183], v[212:215], v[164:167]\n"
    "v_mfma_f32_16x16x32_bf16 v[172:175], a[184:187], v[212:215], v[172:175]\n"
    "v_mfma_f32_16x16x32_bf16 v[180:183], a[188:191], v[212:215], v[180:183]\n"
    "s_waitcnt vmcnt(12)\n"
    "v_mfma_f32_16x16x32_bf16 v[160:163], a[176:179], v[216:219], v[160:163]\n"
    "s_add_u32 s70, s70, 28672\n"
    "buffer_load_dwordx4 v[212:215], v113, s[12:15], s70 offen offset:512 sc1\n"
    "v_mfma_f32_16x16x32_bf16 v[168:171], a[180:183], v[216:219], v[168:171]\n"
    "v_mfma_f32_16x16x32_bf16 v[176:179], a[184:187], v[216:219], v[176:179]\n"
    "v_mfma_f32_16x16x32_bf16 v[184:187], a[188:191], v[216:219], v[184:187]\n"
    "buffer_load_dwordx4 v[216:219], v114, s[12:15], s70 offen offset:768 sc1\n"
    "s_waitcnt vmcnt(13)\n"
    "v_mfma_f32_16x16x32_bf16 v[156:159], a[192:195], v[232:235], v[156:159]\n"
    "v_mfma_f32_16x16x32_bf16 v[164:167], a[196:199], v[232:235], v[164:167]\n"
    "v_mfma_f32_16x16x32_bf16 v[172:175], a[200:203], v[232:235], v[172:175]\n"
    "v_mfma_f32_16x16x32_bf16 v[180:183], a[204:207], v[232:235], v[180:183]\n"
    "s_waitcnt vmcnt(12)\n"
    "v_mfma_f32_16x16x32_bf16 v[160:163], a[192:195], v[224:227], v[160:163]\n"
    "s_add_u32 s70, s70, 28672\n"
    "buffer_load_dwordx4 v[232:235], v113, s[12:15], s70 offen offset:512 sc1\n"
    "v_mfma_f32_16x16x32_bf16 v[168:171], a[196:199], v[224:227], v[168:171]\n"
    "v_mfma_f32_16x16x32_bf16 v[176:179], a[200:203], v[224:227], v[176:179]\n"
    "v_mfma_f32_16x16x32_bf16 v[184:187], a[204:207], v[224:227], v[184:187]\n"
    "s_waitcnt vmcnt(12)\n"
    "v_mfma_f32_16x16x32_bf16 v[156:159], a[208:211], v[108:111], v[156:159]\n"
    "s_add_u32 s70, s70, 28672\n"
    "buffer_load_dwordx4 v[224:227], v113, s[12:15], s70 offen offset:512 sc1\n"
    "v_mfma_f32_16x16x32_bf16 v[164:167], a[212:215], v[108:111], v[164:167]\n"
    "v_mfma_f32_16x16x32_bf16 v[172:175], a[216:219], v[108:111], v[172:175]\n"
    "v_mfma_f32_16x16x32_bf16 v[180:183], a[220:223], v[108:111], v[180:183]\n"
    "buffer_load_dwordx4 v[108:111], v114, s[12:15], s70 offen offset:768 sc1\n"
    "s_waitcnt vmcnt(13)\n"
    "v_mfma_f32_16x16x32_bf16 v[160:163], a[208:211], v[236:239], v[160:163]\n"
    "v_mfma_f32_16x16x32_bf16 v[168:171], a[212:215], v[236:239], v[168:171]\n"
    "v_mfma_f32_16x16x32_bf16 v[176:179], a[216:219], v[236:239], v[176:179]\n"
    "v_mfma_f32_16x16x32_bf16 v[184:187], a[220:223], v[236:239], v[184:187]\n"
    "s_waitcnt vmcnt(12)\n"
    "v_mfma_f32_16x16x32_bf16 v[156:159], a[224:227], v[196:199], v[156:159]\n"
    "v_mfma_f32_16x16x32_bf16 v[164:167], a[228:231], v[196:199], v[164:167]\n"
    "v_mfma_f32_16x16x32_bf16 v[172:175], a[232:235], v[196:199], v[172:175]\n"
    "v_mfma_f32_16x16x32_bf16 v[180:183], a[236:239], v[196:199], v[180:183]\n"
    "s_waitcnt vmcnt(11)\n"
    "v_mfma_f32_16x16x32_bf16 v[160:163], a[224:227], v[188:191], v[160:163]\n"
    "s_add_u32 s70, s70, 28672\n"
    "buffer_load_dwordx4 v[196:199], v113, s[12:15], s70 offen offset:512 sc1\n"
    "v_mfma_f32_16x16x32_bf16 v[168:171], a[228:231], v[188:191], v[168:171]\n"
    "v_mfma_f32_16x16x32_bf16 v[176:179], a[232:235], v[188:191], v[176:179]\n"
    "v_mfma_f32_16x16x32_bf16 v[184:187], a[236:239], v[188:191], v[184:187]\n"
    "s_waitcnt vmcnt(11)\n"
    "v_mfma_f32_16x16x32_bf16 v[156:159], a[240:243], v[192:195], v[156:159]\n"
    "s_add_u32 s70, s70, 28672\n"
    "buffer_load_dwordx4 v[188:191], v113, s[12:15], s70 offen offset:512 sc1\n"
    "v_mfma_f32_16x16x32_bf16 v[164:167], a[244:247], v[192:195], v[164:167]\n"
    "v_mfma_f32_16x16x32_bf16 v[172:175], a[248:251], v[192:195], v[172:175]\n"
    "v_mfma_f32_16x16x32_bf16 v[180:183], a[252:255], v[192:195], v[180:183]\n"
    "buffer_load_dwordx4 v[192:195], v114, s[12:15], s70 offen offset:768 sc1\n"
    "s_waitcnt vmcnt(12)\n"
    "v_mfma_f32_16x16x32_bf16 v[160:163], a[240:243], v[208:211], v[160:163]\n"
    "v_mfma_f32_16x16x32_bf16 v[168:171], a[244:247], v[208:211], v[168:171]\n"
    "s_waitcnt vmcnt(11)\n"
    "v_cmp_eq_u32 vcc, -1, v251\n"
    "s_cbranch_vccnz .Lgretry_75\n"
    ".Lgok_76:\n"
    "v_mfma_f32_16x16x32_bf16 v[176:179], a[248:251], v[208:211], v[176:179]\n"
    "s_waitcnt vmcnt(9)\n"
    "v_cndmask_b32 v228, 0, v204, s[4:5]\n"
    "v_cndmask_b32 v229, 0, v205, s[4:5]\n"
    "v_mfma_f32_16x16x32_bf16 v[184:187], a[252:255], v[208:211], v[184:187]\n"
    "v_cndmask_b32 v230, 0, v206, s[4:5]\n"
    ".Lmark_p4out_77:\n"
    "v_mfma_f32_16x16x32_bf16 v[124:127], v[200:203], v[0:3], v[124:127]\n"
    "v_cndmask_b32 v231, 0, v207, s[4:5]\n"
    "s_nop 1\n"
    "v_mfma_f32_16x16x32_bf16 v[128:131], v[228:231], v[0:3], v[128:131]\n"
    "s_add_u32 s70, s70, 28672\n"
    "buffer_load_dwordx4 v[208:211], v113, s[12:15], s70 offen offset:512 sc1\n"
    "v_cmp_u_f32 vcc, v164, v168\n"
    "s_cbranch_vccnz .Lredo_82\n"
    ".Lredook_83:\n"
    ".Lback_79:\n"
    "ds_write_b128 v119, v[164:167] offset:0\n"
    "ds_write_b128 v119, v[168:171] offset:1024\n"
    "ds_write_b128 v119, v[172:175] offset:2048\n"
    "ds_write_b128 v119, v[176:179] offset:3072\n"
    "ds_write_b128 v119, v[180:183] offset:4096\n"
    "ds_write_b128 v119, v[184:187] offset:5120\n"
    "v_mfma_f32_16x16x32_bf16 v[132:135], v[200:203], v[4:7], 0\n"
    "v_cndmask_b32 v204, v204, 0, s[4:5]\n"
    "v_cndmask_b32 v205, v205, 0, s[4:5]\n"
    "s_waitcnt lgkmcnt(0)\n"
    "s_barrier\n"
    "ds_read_b128 v[164:167], v120 offset:0\n"
    "ds_read_b128 v[168:171], v120 offset:1024\n"
    "ds_read_b128 v[172:175], v121 offset:2048\n"
    "ds_read_b128 v[176:179], v121 offset:3072\n"
    "ds_read_b128 v[180:183], v122 offset:4096\n"
    "ds_read_b128 v[184:187], v122 offset:5120\n"
    "v_mfma_f32_16x16x32_bf16 v[136:139], v[228:231], v[4:7], 0\n"
    "v_cndmask_b32 v206, v206, 0, s[4:5]\n"
    "v_cndmask_b32 v207, v207, 0, s[4:5]\n"
    "v_mfma_f32_16x16x32_bf16 v[140:143], v[200:203], v[8:11], 0\n"
    "v_mfma_f32_16x16x32_bf16 v[144:147], v[228:231], v[8:11], 0\n"
    "v_mfma_f32_16x16x32_bf16 v[148:151], v[200:203], v[12:15], 0\n"
    "s_add_u32 s70, s70, 28672\n"
    "buffer_load_dwordx4 v[200:203], v113, s[12:15], s70 offen offset:512 sc1\n"
    "v_mfma_f32_16x16x32_bf16 v[152:155], v[228:231], v[12:15], 0\n"
    "s_waitcnt vmcnt(10)\n"
    "v_mfma_f32_16x16x32_bf16 v[124:127], v[220:223], v[16:19], v[124:127]\n"
    "s_waitcnt lgkmcnt(3)\n"
    "v_pk_add_f32 v[164:165], v[164:165], v[172:173]\n"
    "v_pk_add_f32 v[166:167], v[166:167], v[174:175]\n"
    "s_waitcnt lgkmcnt(2)\n"
    "v_pk_add_f32 v[168:169], v[168:169], v[176:177]\n"
    "v_pk_add_f32 v[170:171], v[170:171], v[178:179]\n"
    "s_waitcnt lgkmcnt(1)\n"
    "v_pk_add_f32 v[156:157], v[156:157], v[180:181]\n"
    "v_pk_add_f32 v[158:159], v[158:159], v[182:183]\n"
    "s_waitcnt lgkmcnt(0)\n"
    "v_pk_add_f32 v[160:161], v[160:161], v[184:185]\n"
    "v_pk_add_f32 v[162:163], v[162:163], v[186:187]\n"
    "v_pk_add_f32 v[156:157], v[156:157], v[164:165]\n"
    "v_pk_add_f32 v[158:159], v[158:159], v[166:167]\n"
    "v_pk_add_f32 v[160:161], v[160:161], v[168:169]\n"
    "v_pk_add_f32 v[162:163], v[162:163], v[170:171]\n"
    "v_lshlrev_b32 v172, 16, v252\n"
    "v_and_b32 v173, 0xffff0000, v252\n"
    "v_lshlrev_b32 v174, 16, v253\n"
    "v_and_b32 v175, 0xffff0000, v253\n"
    "v_lshlrev_b32 v176, 16, v254\n"
    "v_and_b32 v177, 0xffff0000, v254\n"
    "v_lshlrev_b32 v178, 16, v255\n"
    "v_and_b32 v179, 0xffff0000, v255\n"
    "v_mov_b32 v184, v248\n"
    "v_add_u32 v185, -384, v250\n"
    "v_permlane16_swap_b32 v156, v160\n"
    "v_permlane16_swap_b32 v157, v161\n"
    "v_permlane16_swap_b32 v158, v162\n"
    "v_permlane16_swap_b32 v159, v163\n"
    "v_pk_add_f32 v[156:157], v[156:157], v[172:173]\n"
    "v_pk_add_f32 v[158:159], v[158:159], v[174:175]\n"
    "v_pk_add_f32 v[160:161], v[160:161], v[176:177]\n"
    "v_pk_add_f32 v[162:163], v[162:163], v[178:179]\n"
    "v_pk_mul_f32 v[164:165], v[156:157], v[156:157]\n"
    "v_pk_mul_f32 v[166:167], v[158:159], v[158:159]\n"
    "v_pk_mul_f32 v[168:169], v[160:161], v[160:161]\n"
    "v_pk_mul_f32 v[170:171], v[162:163], v[162:163]\n"
    "v_pk_fma_f32 v[164:165], v[164:165], s[64:65], s[64:65] op_sel:[0,0,1] op_sel_hi:[1,0,1]\n"
    "v_pk_fma_f32 v[166:167], v[166:167], s[64:65], s[64:65] op_sel:[0,0,1] op_sel_hi:[1,0,1]\n"
    "v_pk_fma_f32 v[168:169], v[168:169], s[64:65], s[64:65] op_sel:[0,0,1] op_sel_hi:[1,0,1]\n"
    "v_pk_fma_f32 v[170:171], v[170:171], s[64:65], s[64:65] op_sel:[0,0,1] op_sel_hi:[1,0,1]\n"
    "v_pk_mul_f32 v[164:165], v[164:165], v[156:157]\n"
    "v_pk_mul_f32 v[166:167], v[166:167], v[158:159]\n"
    "v_pk_mul_f32 v[168:169], v[168:169], v[160:161]\n"
    "v_pk_mul_f32 v[170:171], v[170:171], v[162:163]\n"
    "v_exp_f32 v164, v164\n"
    "v_exp_f32 v165, v165\n"
    "v_exp_f32 v166, v166\n"
    "v_exp_f32 v167, v167\n"
    "v_exp_f32 v168, v168\n"
    "v_exp_f32 v169, v169\n"
    "v_exp_f32 v170, v170\n"
    "v_exp_f32 v171, v171\n"
    "v_pk_add_f32 v[164:165], v[164:165], 1.0 op_sel_hi:[1,0]\n"
    "v_pk_add_f32 v[166:167], v[166:167], 1.0 op_sel_hi:[1,0]\n"
    "v_pk_add_f32 v[168:169], v[168:169], 1.0 op_sel_hi:[1,0]\n"
    "v_pk_add_f32 v[170:171], v[170:171], 1.0 op_sel_hi:[1,0]\n"
    "v_rcp_f32 v164, v164\n"
    "v_rcp_f32 v165, v165\n"
    "v_rcp_f32 v166, v166\n"
    "v_rcp_f32 v167, v167\n"
    "v_rcp_f32 v168, v168\n"
    "v_rcp_f32 v169, v169\n"
    "v_rcp_f32 v170, v170\n"
    "v_rcp_f32 v171, v171\n"
    "v_pk_mul_f32 v[164:165], v[164:165], v[156:157]\n"
    "v_pk_mul_f32 v[166:167], v[166:167], v[158:159]\n"
    "v_pk_mul_f32 v[168:169], v[168:169], v[160:161]\n"
    "v_pk_mul_f32 v[170:171], v[170:171], v[162:163]\n"
    "v_cvt_pk_bf16_f32 v180, v164, v165\n"
    "v_cvt_pk_bf16_f32 v181, v166, v167\n"
    "v_cvt_pk_bf16_f32 v182, v168, v169\n"
    "v_cvt_pk_bf16_f32 v183, v170, v171\n"
    "buffer_store_dwordx4 v[180:183], v184, s[12:15], 0 offen offset:0 sc1\n"
    "v_lshlrev_b32 v156, 16, v96\n"
    "v_and_b32 v157, 0xffff0000, v96\n"
    "v_lshlrev_b32 v158, 16, v97\n"
    "v_and_b32 v159, 0xffff0000, v97\n"
    "v_lshlrev_b32 v160, 16, v98\n"
    "v_and_b32 v161, 0xffff0000, v98\n"
    "v_lshlrev_b32 v162, 16, v99\n"
    "v_and_b32 v163, 0xffff0000, v99\n"
    "v_add_f32 v156, v156, v123\n"
    "v_mfma_f32_16x16x32_bf16 v[128:131], v[204:207], v[16:19], v[128:131]\n"
    "v_add_f32 v157, v157, v123\n"
    "v_add_f32 v158, v158, v123\n"
    "v_add_f32 v159, v159, v123\n"
    "v_add_f32 v160, v160, v123\n"
    "v_add_f32 v161, v161, v123\n"
    "v_add_f32 v162, v162, v123\n"
    "v_add_f32 v163, v163, v123\n"
    "s_waitcnt lgkmcnt(0)\n"
    "s_barrier\n"
    "v_mfma_f32_16x16x32_bf16 v[132:135], v[220:223], v[20:23], v[132:135]\n"
    "v_mfma_f32_16x16x32_bf16 v[136:139], v[204:207], v[20:23], v[136:139]\n"
    "v_mfma_f32_16x16x32_bf16 v[140:143], v[220:223], v[24:27], v[140:143]\n"
    "v_mfma_f32_16x16x32_bf16 v[144:147], v[204:207], v[24:27], v[144:147]\n"
    "v_mfma_f32_16x16x32_bf16 v[148:151], v[220:223], v[28:31], v[148:151]\n"
    "buffer_load_dwordx4 v[220:223], v114, s[12:15], s70 offen offset:768 sc1\n"
    "s_waitcnt vmcnt(10)\n"
    "v_cndmask_b32 v228, 0, v216, s[4:5]\n"
    "v_mfma_f32_16x16x32_bf16 v[152:155], v[204:207], v[28:31], v[152:155]\n"
    "v_cndmask_b32 v229, 0, v217, s[4:5]\n"
    "v_mfma_f32_16x16x32_bf16 v[124:127], v[212:215], v[32:35], v[124:127]\n"
    "v_cndmask_b32 v230, 0, v218, s[4:5]\n"
    "v_cndmask_b32 v231, 0, v219, s[4:5]\n"
    "s_nop 1\n"
    "v_mfma_f32_16x16x32_bf16 v[128:131], v[228:231], v[32:35], v[128:131]\n"
    "s_add_u32 s70, s70, 28672\n"
    "buffer_load_dwordx4 v[204:207], v113, s[12:15], s70 offen offset:512 sc1\n"
    "v_mfma_f32_16x16x32_bf16 v[132:135], v[212:215], v[36:39], v[132:135]\n"
    "v_cndmask_b32 v216, v216, 0, s[4:5]\n"
    "v_cndmask_b32 v217, v217, 0, s[4:5]\n"
    "v_mfma_f32_16x16x32_bf16 v[136:139], v[228:231], v[36:39], v[136:139]\n"
    "v_cndmask_b32 v218, v218, 0, s[4:5]\n"
    "v_cndmask_b32 v219, v219, 0, s[4:5]\n"
    "v_mfma_f32_16x16x32_bf16 v[140:143], v[212:215], v[40:43], v[140:143]\n"
    "v_mfma_f32_16x16x32_bf16 v[144:147], v[228:231], v[40:43], v[144:147]\n"
    "v_mfma_f32_16x16x32_bf16 v[148:151], v[212:215], v[44:47], v[148:151]\n"
    "s_add_u32 s70, s70, 28672\n"
    "buffer_load_dwordx4 v[212:215], v113, s[12:15], s70 offen offset:512 sc1\n"
    "v_mfma_f32_16x16x32_bf16 v[152:155], v[228:231], v[44:47], v[152:155]\n"
    "s_waitcnt vmcnt(11)\n"
    "v_mfma_f32_16x16x32_bf16 v[124:127], v[232:235], v[48:51], v[124:127]\n"
    "v_mfma_f32_16x16x32_bf16 v[128:131], v[216:219], v[48:51], v[128:131]\n"
    "v_mfma_f32_16x16x32_bf16 v[132:135], v[232:235], v[52:55], v[132:135]\n"
    "v_mfma_f32_16x16x32_bf16 v[136:139], v[216:219], v[52:55], v[136:139]\n"
    "v_mfma_f32_16x16x32_bf16 v[140:143], v[232:235], v[56:59], v[140:143]\n"
    "v_mfma_f32_16x16x32_bf16 v[144:147], v[216:219], v[56:59], v[144:147]\n"
    "v_mfma_f32_16x16x32_bf16 v[148:151], v[232:235], v[60:63], v[148:151]\n"
    "s_waitcnt vmcnt(9)\n"
    "v_cndmask_b32 v228, 0, v108, s[4:5]\n"
    "v_cndmask_b32 v229, 0, v109, s[4:5]\n"
    "v_mfma_f32_16x16x32_bf16 v[152:155], v[216:219], v[60:63], v[152:155]\n"
    "v_cndmask_b32 v230, 0, v110, s[4:5]\n"
    "v_mfma_f32_16x16x32_bf16 v[124:127], v[224:227], v[64:67], v[124:127]\n"
    "v_cndmask_b32 v231, 0, v111, s[4:5]\n"
    "s_nop 1\n"
    "v_mfma_f32_16x16x32_bf16 v[128:131], v[228:231], v[64:67], v[128:131]\n"
    "buffer_load_dwordx4 v[216:219], v114, s[12:15], s70 offen offset:768 sc1\n"
    "v_cndmask_b32 v108, v108, 0, s[4:5]\n"
    "v_mfma_f32_16x16x32_bf16 v[132:135], v[224:227], v[68:71], v[132:135]\n"
    "v_cndmask_b32 v109, v109, 0, s[4:5]\n"
    "v_cndmask_b32 v110, v110, 0, s[4:5]\n"
    "v_mfma_f32_16x16x32_bf16 v[136:139], v[228:231], v[68:71], v[136:139]\n"
    "v_cndmask_b32 v111, v111, 0, s[4:5]\n"
    "v_mfma_f32_16x16x32_bf16 v[140:143], v[224:227], v[72:75], v[140:143]\n"
    "v_mfma_f32_16x16x32_bf16 v[144:147], v[228:231], v[72:75], v[144:147]\n"
    "v_mfma_f32_16x16x32_bf16 v[148:151], v[224:227], v[76:79], v[148:151]\n"
    "ds_read_b64_tr_b16 v[236:237], v115 offset:0\n"
    "v_mfma_f32_16x16x32_bf16 v[152:155], v[228:231], v[76:79], v[152:155]\n"
    "ds_read_b64_tr_b16 v[238:239], v115 offset:512\n"
    "s_waitcnt vmcnt(9)\n"
    "v_mfma_f32_16x16x32_bf16 v[124:127], v[196:199], v[80:83], v[124:127]\n"
    "s_add_u32 s70, s70, 28672\n"
    "buffer_load_dwordx4 v[224:227], v113, s[12:15], s70 offen offset:512 sc1\n"
    "v_mfma_f32_16x16x32_bf16 v[128:131], v[108:111], v[80:83], v[128:131]\n"
    "ds_read_b64_tr_b16 v[240:241], v116 offset:0\n"
    "ds_read_b64_tr_b16 v[242:243], v116 offset:512\n"
    "v_mfma_f32_16x16x32_bf16 v[132:135], v[196:199], v[84:87], v[132:135]\n"
    "ds_read_b64_tr_b16 v[244:245], v117 offset:0\n"
    "v_mfma_f32_16x16x32_bf16 v[136:139], v[108:111], v[84:87], v[136:139]\n"
    "ds_read_b64_tr_b16 v[246:247], v117 offset:512\n"
    "v_mfma_f32_16x16x32_bf16 v[140:143], v[196:199], v[88:91], v[140:143]\n"
    "ds_read_b64_tr_b16 v[248:249], v118 offset:0\n"
    "v_mfma_f32_16x16x32_bf16 v[144:147], v[108:111], v[88:91], v[144:147]\n"
    "ds_read_b64_tr_b16 v[250:251], v118 offset:512\n"
    "v_mfma_f32_16x16x32_bf16 v[148:151], v[196:199], v[92:95], v[148:151]\n"
    "ds_read_b64_tr_b16 v[252:253], v115 offset:4096\n"
    "s_waitcnt vmcnt(8)\n"
    "v_cndmask_b32 v228, 0, v192, s[4:5]\n"
    "v_mfma_f32_16x16x32_bf16 v[152:155], v[108:111], v[92:95], v[152:155]\n"
    "s_waitcnt lgkmcnt(7)\n"
    "v_mfma_f32_16x16x32_bf16 v[124:127], v[188:191], v[236:239], v[124:127]\n"
    "s_add_u32 s70, s70, 28672\n"
    "buffer_load_dwordx4 v[196:199], v113, s[12:15], s70 offen offset:512 sc1\n"
    "v_cndmask_b32 v229, 0, v193, s[4:5]\n"
    "v_cndmask_b32 v230, 0, v194, s[4:5]\n"
    "v_cndmask_b32 v231, 0, v195, s[4:5]\n"
    "s_nop 1\n"
    "v_mfma_f32_16x16x32_bf16 v[128:131], v[228:231], v[236:239], v[128:131]\n"
    "ds_read_b64_tr_b16 v[254:255], v115 offset:4608\n"
    "buffer_load_dwordx4 v[108:111], v114, s[12:15], s70 offen offset:768 sc1\n"
    "s_waitcnt lgkmcnt(6)\n"
    "v_mfma_f32_16x16x32_bf16 v[132:135], v[188:191], v[240:243], v[132:135]\n"
    "ds_read_b64_tr_b16 v[232:233], v116 offset:4096\n"
    "ds_read_b64_tr_b16 v[234:235], v116 offset:4608\n"
    "v_mfma_f32_16x16x32_bf16 v[136:139], v[228:231], v[240:243], v[136:139]\n"
    "ds_read_b64_tr_b16 v[236:237], v117 offset:4096\n"
    "s_waitcnt lgkmcnt(7)\n"
    "v_mfma_f32_16x16x32_bf16 v[140:143], v[188:191], v[244:247], v[140:143]\n"
    "ds_read_b64_tr_b16 v[238:239], v117 offset:4608\n"
    "ds_read_b64_tr_b16 v[240:241], v118 offset:4096\n"
    "v_mfma_f32_16x16x32_bf16 v[144:147], v[228:231], v[244:247], v[144:147]\n"
    "v_cndmask_b32 v192, v192, 0, s[4:5]\n"
    "s_waitcnt lgkmcnt(7)\n"
    "v_mfma_f32_16x16x32_bf16 v[148:151], v[188:191], v[248:251], v[148:151]\n"
    "v_cndmask_b32 v193, v193, 0, s[4:5]\n"
    "v_cndmask_b32 v194, v194, 0, s[4:5]\n"
    "v_mfma_f32_16x16x32_bf16 v[152:155], v[228:231], v[248:251], v[152:155]\n"
    "v_cndmask_b32 v195, v195, 0, s[4:5]\n"
    "ds_read_b64_tr_b16 v[242:243], v118 offset:4608\n"
    "s_waitcnt vmcnt(9) lgkmcnt(6)\n"
    "v_mfma_f32_16x16x32_bf16 v[124:127], v[208:211], v[252:255], v[124:127]\n"
    "ds_read_b64_tr_b16 v[244:245], v115 offset:8192\n"
    "ds_read_b64_tr_b16 v[246:247], v115 offset:8704\n"
    "s_add_u32 s70, s70, 28672\n"
    "buffer_load_dwordx4 v[188:191], v113, s[12:15], s70 offen offset:512 sc1\n"
    "v_mfma_f32_16x16x32_bf16 v[128:131], v[192:195], v[252:255], v[128:131]\n"
    "ds_read_b64_tr_b16 v[248:249], v116 offset:8192\n"
    "s_waitcnt lgkmcnt(7)\n"
    "v_mfma_f32_16x16x32_bf16 v[132:135], v[208:211], v[232:235], v[132:135]\n"
    "ds_read_b64_tr_b16 v[250:251], v116 offset:8704\n"
    "ds_read_b64_tr_b16 v[252:253], v117 offset:8192\n"
    "v_mfma_f32_16x16x32_bf16 v[136:139], v[192:195], v[232:235], v[136:139]\n"
    "ds_read_b64_tr_b16 v[254:255], v117 offset:8704\n"
    "s_waitcnt lgkmcnt(8)\n"
    "v_mfma_f32_16x16x32_bf16 v[140:143], v[208:211], v[236:239], v[140:143]\n"
    "ds_read_b64_tr_b16 v[232:233], v118 offset:8192\n"
    "v_mfma_f32_16x16x32_bf16 v[144:147], v[192:195], v[236:239], v[144:147]\n"
    "ds_read_b64_tr_b16 v[234:235], v118 offset:8704\n"
    "s_waitcnt lgkmcnt(8)\n"
    "v_mfma_f32_16x16x32_bf16 v[148:151], v[208:211], v[240:243], v[148:151]\n"
    "ds_read_b64_tr_b16 v[236:237], v115 offset:12288\n"
    "s_waitcnt vmcnt(7)\n"
    "v_cndmask_b32 v228, 0, v220, s[4:5]\n"
    "v_mfma_f32_16x16x32_bf16 v[152:155], v[192:195], v[240:243], v[152:155]\n"
    "s_waitcnt lgkmcnt(7)\n"
    "v_mfma_f32_16x16x32_bf16 v[124:127], v[200:203], v[244:247], v[124:127]\n"
    "v_cndmask_b32 v229, 0, v221, s[4:5]\n"
    "v_cndmask_b32 v230, 0, v222, s[4:5]\n"
    "v_cndmask_b32 v231, 0, v223, s[4:5]\n"
    "s_nop 1\n"
    "v_mfma_f32_16x16x32_bf16 v[128:131], v[228:231], v[244:247], v[128:131]\n"
    "ds_read_b64_tr_b16 v[238:239], v115 offset:12800\n"
    "s_waitcnt lgkmcnt(6)\n"
    "v_mfma_f32_16x16x32_bf16 v[132:135], v[200:203], v[248:251], v[132:135]\n"
    "ds_read_b64_tr_b16 v[240:241], v116 offset:12288\n"
    "ds_read_b64_tr_b16 v[242:243], v116 offset:12800\n"
    "v_mfma_f32_16x16x32_bf16 v[136:139], v[228:231], v[248:251], v[136:139]\n"
    "ds_read_b64_tr_b16 v[244:245], v117 offset:12288\n"
    "s_waitcnt lgkmcnt(7)\n"
    "v_mfma_f32_16x16x32_bf16 v[140:143], v[200:203], v[252:255], v[140:143]\n"
    "ds_read_b64_tr_b16 v[246:247], v117 offset:12800\n"
    "ds_read_b64_tr_b16 v[248:249], v118 offset:12288\n"
    "v_mfma_f32_16x16x32_bf16 v[144:147], v[228:231], v[252:255], v[144:147]\n"
    "v_cndmask_b32 v220, v220, 0, s[4:5]\n"
    "s_waitcnt lgkmcnt(7)\n"
    "v_mfma_f32_16x16x32_bf16 v[148:151], v[200:203], v[232:235], v[148:151]\n"
    "v_cndmask_b32 v221, v221, 0, s[4:5]\n"
    "v_cndmask_b32 v222, v222, 0, s[4:5]\n"
    "v_mfma_f32_16x16x32_bf16 v[152:155], v[228:231], v[232:235], v[152:155]\n"
    "v_cndmask_b32 v223, v223, 0, s[4:5]\n"
    "ds_read_b64_tr_b16 v[250:251], v118 offset:12800\n"
    "s_waitcnt vmcnt(6) lgkmcnt(6)\n"
    "v_mfma_f32_16x16x32_bf16 v[124:127], v[204:207], v[236:239], v[124:127]\n"
    "ds_read_b64_tr_b16 v[252:253], v115 offset:16384\n"
    "ds_read_b64_tr_b16 v[254:255], v115 offset:16896\n"
    "v_mfma_f32_16x16x32_bf16 v[128:131], v[220:223], v[236:239], v[128:131]\n"
    "ds_read_b64_tr_b16 v[232:233], v116 offset:16384\n"
    "s_waitcnt lgkmcnt(7)\n"
    "v_mfma_f32_16x16x32_bf16 v[132:135], v[204:207], v[240:243], v[132:135]\n"
    "buffer_load_dword v186, v185, s[12:15], s82 offen sc1\n"
    "s_add_u32 s70, s82, 0\n"
    "buffer_load_dwordx4 v[208:211], v113, s[12:15], s70 offen offset:0 sc1\n"
    "buffer_load_dwordx4 v[192:195], v113, s[12:15], s70 offen offset:256 sc1\n"
    "s_add_u32 s70, s70, 28672\n"
    "buffer_load_dwordx4 v[200:203], v113, s[12:15], s70 offen offset:0 sc1\n"
    "v_mfma_f32_16x16x32_bf16 v[136:139], v[220:223], v[240:243], v[136:139]\n"
    "ds_read_b64_tr_b16 v[234:235], v116 offset:16896\n"
    "s_waitcnt lgkmcnt(6)\n"
    "v_mfma_f32_16x16x32_bf16 v[140:143], v[204:207], v[244:247], v[140:143]\n"
    "ds_read_b64_tr_b16 v[236:237], v117 offset:16384\n"
    "ds_read_b64_tr_b16 v[238:239], v117 offset:16896\n"
    "v_mfma_f32_16x16x32_bf16 v[144:147], v[220:223], v[244:247], v[144:147]\n"
    "ds_read_b64_tr_b16 v[240:241], v118 offset:16384\n"
    "s_waitcnt lgkmcnt(7)\n"
    "v_mfma_f32_16x16x32_bf16 v[148:151], v[204:207], v[248:251], v[148:151]\n"
    "ds_read_b64_tr_b16 v[242:243], v118 offset:16896\n"
    "ds_read_b64_tr_b16 v[244:245], v115 offset:20480\n"
    "v_mfma_f32_16x16x32_bf16 v[152:155], v[220:223], v[248:251], v[152:155]\n"
    "s_waitcnt vmcnt(9) lgkmcnt(7)\n"
    "v_mfma_f32_16x16x32_bf16 v[124:127], v[212:215], v[252:255], v[124:127]\n"
    "buffer_load_dwordx4 v[204:207], v113, s[12:15], s70 offen offset:256 sc1\n"
    "s_waitcnt vmcnt(9)\n"
    "v_cndmask_b32 v228, 0, v216, s[4:5]\n"
    "v_cndmask_b32 v229, 0, v217, s[4:5]\n"
    "v_cndmask_b32 v230, 0, v218, s[4:5]\n"
    "v_cndmask_b32 v231, 0, v219, s[4:5]\n"
    "s_nop 1\n"
    "v_mfma_f32_16x16x32_bf16 v[128:131], v[228:231], v[252:255], v[128:131]\n"
    "ds_read_b64_tr_b16 v[246:247], v115 offset:20992\n"
    "s_add_u32 s70, s70, 28672\n"
    "buffer_load_dwordx4 v[220:223], v113, s[12:15], s70 offen offset:0 sc1\n"
    "s_waitcnt lgkmcnt(6)\n"
    "v_mfma_f32_16x16x32_bf16 v[132:135], v[212:215], v[232:235], v[132:135]\n"
    "ds_read_b64_tr_b16 v[248:249], v116 offset:20480\n"
    "ds_read_b64_tr_b16 v[250:251], v116 offset:20992\n"
    "v_mfma_f32_16x16x32_bf16 v[136:139], v[228:231], v[232:235], v[136:139]\n"
    "ds_read_b64_tr_b16 v[252:253], v117 offset:20480\n"
    "s_waitcnt lgkmcnt(7)\n"
    "v_mfma_f32_16x16x32_bf16 v[140:143], v[212:215], v[236:239], v[140:143]\n"
    "ds_read_b64_tr_b16 v[254:255], v117 offset:20992\n"
    "ds_read_b64_tr_b16 v[232:233], v118 offset:20480\n"
    "v_mfma_f32_16x16x32_bf16 v[144:147], v[228:231], v[236:239], v[144:147]\n"
    "v_cndmask_b32 v216, v216, 0, s[4:5]\n"
    "s_waitcnt lgkmcnt(7)\n"
    "v_mfma_f32_16x16x32_bf16 v[148:151], v[212:215], v[240:243], v[148:151]\n"
    "v_cndmask_b32 v217, v217, 0, s[4:5]\n"
    "v_cndmask_b32 v218, v218, 0, s[4:5]\n"
    "v_mfma_f32_16x16x32_bf16 v[152:155], v[228:231], v[240:243], v[152:155]\n"
    "v_cndmask_b32 v219, v219, 0, s[4:5]\n"
    "ds_read_b64_tr_b16 v[234:235], v118 offset:20992\n"
    "s_waitcnt vmcnt(9) lgkmcnt(6)\n"
    "v_mfma_f32_16x16x32_bf16 v[124:127], v[224:227], v[244:247], v[124:127]\n"
    "ds_read_b64_tr_b16 v[236:237], v115 offset:24576\n"
    "buffer_load_dwordx4 v[212:215], v113, s[12:15], s70 offen offset:256 sc1\n"
    "v_mfma_f32_16x16x32_bf16 v[128:131], v[216:219], v[244:247], v[128:131]\n"
    "ds_read_b64_tr_b16 v[238:239], v115 offset:25088\n"
    "s_waitcnt lgkmcnt(6)\n"
    "v_mfma_f32_16x16x32_bf16 v[132:135], v[224:227], v[248:251], v[132:135]\n"
    "ds_read_b64_tr_b16 v[240:241], v116 offset:24576\n"
    "ds_read_b64_tr_b16 v[242:243], v116 offset:25088\n"
    "v_mfma_f32_16x16x32_bf16 v[136:139], v[216:219], v[248:251], v[136:139]\n"
    "ds_read_b64_tr_b16 v[244:245], v117 offset:24576\n"
    "s_waitcnt lgkmcnt(7)\n"
    "v_mfma_f32_16x16x32_bf16 v[140:143], v[224:227], v[252:255], v[140:143]\n"
    "ds_read_b64_tr_b16 v[246:247], v117 offset:25088\n"
    "ds_read_b64_tr_b16 v[248:249], v118 offset:24576\n"
    "v_mfma_f32_16x16x32_bf16 v[144:147], v[216:219], v[252:255], v[144:147]\n"
    "ds_read_b64_tr_b16 v[250:251], v118 offset:25088\n"
    "s_waitcnt lgkmcnt(8)\n"
    "v_mfma_f32_16x16x32_bf16 v[148:151], v[224:227], v[232:235], v[148:151]\n"
    "ds_read_b64_tr_b16 v[252:253], v115 offset:28672\n"
    "s_waitcnt vmcnt(8)\n"
    "v_cndmask_b32 v228, 0, v108, s[4:5]\n"
    "v_mfma_f32_16x16x32_bf16 v[152:155], v[216:219], v[232:235], v[152:155]\n"
    "s_waitcnt lgkmcnt(7)\n"
    "v_mfma_f32_16x16x32_bf16 v[124:127], v[196:199], v[236:239], v[124:127]\n"
    "s_add_u32 s70, s70, 28672\n"
    "buffer_load_dwordx4 v[224:227], v113, s[12:15], s70 offen offset:0 sc1\n"
    "v_cndmask_b32 v229, 0, v109, s[4:5]\n"
    "v_cndmask_b32 v230, 0, v110, s[4:5]\n"
    "v_cndmask_b32 v231, 0, v111, s[4:5]\n"
    "s_nop 1\n"
    "v_mfma_f32_16x16x32_bf16 v[128:131], v[228:231], v[236:239], v[128:131]\n"
    "ds_read_b64_tr_b16 v[254:255], v115 offset:29184\n"
    "buffer_load_dwordx4 v[216:219], v113, s[12:15], s70 offen offset:256 sc1\n"
    "s_waitcnt lgkmcnt(6)\n"
    "v_mfma_f32_16x16x32_bf16 v[132:135], v[196:199], v[240:243], v[132:135]\n"
    "ds_read_b64_tr_b16 v[232:233], v116 offset:28672\n"
    "ds_read_b64_tr_b16 v[234:235], v116 offset:29184\n"
    "v_mfma_f32_16x16x32_bf16 v[136:139], v[228:231], v[240:243], v[136:139]\n"
    "ds_read_b64_tr_b16 v[236:237], v117 offset:28672\n"
    "s_waitcnt lgkmcnt(7)\n"
    "v_mfma_f32_16x16x32_bf16 v[140:143], v[196:199], v[244:247], v[140:143]\n"
    "ds_read_b64_tr_b16 v[238:239], v117 offset:29184\n"
    "ds_read_b64_tr_b16 v[240:241], v118 offset:28672\n"
    "v_mfma_f32_16x16x32_bf16 v[144:147], v[228:231], v[244:247], v[144:147]\n"
    "v_cndmask_b32 v108, v108, 0, s[4:5]\n"
    "s_waitcnt lgkmcnt(7)\n"
    "v_mfma_f32_16x16x32_bf16 v[148:151], v[196:199], v[248:251], v[148:151]\n"
    "v_cndmask_b32 v109, v109, 0, s[4:5]\n"
    "v_cndmask_b32 v110, v110, 0, s[4:5]\n"
    "v_mfma_f32_16x16x32_bf16 v[152:155], v[228:231], v[248:251], v[152:155]\n"
    "v_cndmask_b32 v111, v111, 0, s[4:5]\n"
    "ds_read_b64_tr_b16 v[242:243], v118 offset:29184\n"
    "s_waitcnt vmcnt(9) lgkmcnt(6)\n"
    "v_mfma_f32_16x16x32_bf16 v[124:127], v[188:191], v[252:255], v[124:127]\n"
    "s_add_u32 s70, s70, 28672\n"
    "buffer_load_dwordx4 v[196:199], v113, s[12:15], s70 offen offset:0 sc1\n"
    "v_mfma_f32_16x16x32_bf16 v[128:131], v[108:111], v[252:255], v[128:131]\n"
    "s_waitcnt lgkmcnt(4)\n"
    "v_mfma_f32_16x16x32_bf16 v[132:135], v[188:191], v[232:235], v[132:135]\n"
    "v_mfma_f32_16x16x32_bf16 v[136:139], v[108:111], v[232:235], v[136:139]\n"
    "s_waitcnt lgkmcnt(2)\n"
    "v_mfma_f32_16x16x32_bf16 v[140:143], v[188:191], v[236:239], v[140:143]\n"
    "v_mfma_f32_16x16x32_bf16 v[144:147], v[108:111], v[236:239], v[144:147]\n"
    "s_waitcnt lgkmcnt(0)\n"
    "v_mfma_f32_16x16x32_bf16 v[148:151], v[188:191], v[240:243], v[148:151]\n"
    "buffer_load_dwordx4 v[188:191], v113, s[12:15], s70 offen offset:256 sc1\n"
    "v_mfma_f32_16x16x32_bf16 v[152:155], v[108:111], v[240:243], v[152:155]\n"
    ".Lmark_p5in_84:\n"
    "s_waitcnt vmcnt(10)\n"
    "v_cmp_eq_u32 vcc, -1, v186\n"
    "s_cbranch_vccnz .Lgretry_85\n"
    ".Lgok_86:\n"
    "s_waitcnt vmcnt(9)\n"
    "v_mfma_f32_16x16x32_bf16 v[156:159], v[208:211], v[0:3], v[156:159]\n"
    "s_waitcnt vmcnt(8)\n"
    "v_mfma_f32_16x16x32_bf16 v[160:163], v[192:195], v[0:3], v[160:163]\n"
    "s_add_u32 s70, s70, 28672\n"
    "buffer_load_dwordx4 v[108:111], v113, s[12:15], s70 offen offset:0 sc1\n"
    "v_pk_add_f32 v[240:241], v[132:133], v[134:135]\n"
    "v_pk_add_f32 v[242:243], v[136:137], v[138:139]\n"
    "v_pk_add_f32 v[240:241], v[240:241], v[242:243]\n"
    "v_cmp_u_f32 vcc, v240, v241\n"
    "s_cbranch_vccnz .Lredo_91\n"
    ".Lredook_92:\n"
    ".Lback_88:\n"
    "ds_write_b128 v119, v[132:135] offset:0\n"
    "ds_write_b128 v119, v[136:139] offset:1024\n"
    "ds_write_b128 v119, v[140:143] offset:2048\n"
    "ds_write_b128 v119, v[144:147] offset:3072\n"
    "ds_write_b128 v119, v[148:151] offset:4096\n"
    "ds_write_b128 v119, v[152:155] offset:5120\n"
    "v_mfma_f32_16x16x32_bf16 v[164:167], v[208:211], v[4:7], 0\n"
    "s_waitcnt lgkmcnt(0)\n"
    "s_barrier\n"
    "ds_read_b128 v[132:135], v120 offset:0\n"
    "ds_read_b128 v[136:139], v120 offset:1024\n"
    "ds_read_b128 v[140:143], v121 offset:2048\n"
    "ds_read_b128 v[144:147], v121 offset:3072\n"
    "ds_read_b128 v[148:151], v122 offset:4096\n"
    "ds_read_b128 v[152:155], v122 offset:5120\n"
    "v_mfma_f32_16x16x32_bf16 v[168:171], v[192:195], v[4:7], 0\n"
    "v_mfma_f32_16x16x32_bf16 v[172:175], v[208:211], v[8:11], 0\n"
    "v_mfma_f32_16x16x32_bf16 v[176:179], v[192:195], v[8:11], 0\n"
    "v_mfma_f32_16x16x32_bf16 v[180:183], v[208:211], v[12:15], 0\n"
    "buffer_load_dwordx4 v[208:211], v113, s[12:15], s70 offen offset:256 sc1\n"
    "v_mfma_f32_16x16x32_bf16 v[184:187], v[192:195], v[12:15], 0\n"
    "s_waitcnt vmcnt(9)\n"
    "v_mfma_f32_16x16x32_bf16 v[156:159], v[200:203], v[16:19], v[156:159]\n"
    "s_waitcnt lgkmcnt(3)\n"
    "v_pk_add_f32 v[132:133], v[132:133], v[140:141]\n"
    "v_pk_add_f32 v[134:135], v[134:135], v[142:143]\n"
    "s_waitcnt lgkmcnt(2)\n"
    "v_pk_add_f32 v[136:137], v[136:137], v[144:145]\n"
    "v_pk_add_f32 v[138:139], v[138:139], v[146:147]\n"
    "s_waitcnt lgkmcnt(1)\n"
    "v_pk_add_f32 v[124:125], v[124:125], v[148:149]\n"
    "v_pk_add_f32 v[126:127], v[126:127], v[150:151]\n"
    "s_waitcnt lgkmcnt(0)\n"
    "v_pk_add_f32 v[128:129], v[128:129], v[152:153]\n"
    "v_pk_add_f32 v[130:131], v[130:131], v[154:155]\n"
    "v_pk_add_f32 v[124:125], v[124:125], v[132:133]\n"
    "v_pk_add_f32 v[126:127], v[126:127], v[134:135]\n"
    "v_pk_add_f32 v[128:129], v[128:129], v[136:137]\n"
    "v_pk_add_f32 v[130:131], v[130:131], v[138:139]\n"
    "v_and_b32 v132, 15, v112\n"
    "v_bfe_u32 v133, v112, 4, 2\n"
    "v_bfrev_b32 v134, v133\n"
    "v_lshrrev_b32 v134, 26, v134\n"
    "v_mov_b32 v135, 1073741824\n"
    "v_subrev_u32 v140, 25165824, v104\n"
    "v_lshlrev_b32 v132, 4, v133\n"
    "v_sub_u32 v141, v140, v132\n"
    "v_add3_u32 v141, v141, v134, 64\n"
    "v_cmp_ne_u32 vcc, 3, v133\n"
    "s_nop 1\n"
    "v_cndmask_b32 v141, v135, v141, vcc\n"
    "v_mov_b32 v132, v128\n"
    "v_mov_b32 v133, v129\n"
    "v_mov_b32 v134, v130\n"
    "v_mov_b32 v135, v131\n"
    "v_permlane32_swap_b32 v128, v132\n"
    "v_permlane32_swap_b32 v129, v133\n"
    "v_permlane32_swap_b32 v130, v134\n"
    "v_permlane32_swap_b32 v131, v135\n"
    "v_pk_add_f32 v[128:129], v[128:129], v[132:133]\n"
    "v_pk_add_f32 v[130:131], v[130:131], v[134:135]\n"
    "s_nop 0\n"
    "v_permlane16_swap_b32 v124, v128\n"
    "v_permlane16_swap_b32 v125, v129\n"
    "v_permlane16_swap_b32 v126, v130\n"
    "v_permlane16_swap_b32 v127, v131\n"
    "v_cvt_pk_bf16_f32 v136, v124, v125\n"
    "v_cvt_pk_bf16_f32 v137, v126, v127\n"
    "v_cvt_pk_bf16_f32 v138, v128, v129\n"
    "v_cvt_pk_bf16_f32 v139, v130, v131\n"
    "buffer_store_dwordx4 v[136:139], v141, s[44:47], 0 offen\n"
    "s_waitcnt lgkmcnt(0)\n"
    "s_barrier\n"
    "s_waitcnt vmcnt(9)\n"
    "v_mfma_f32_16x16x32_bf16 v[160:163], v[204:207], v[16:19], v[160:163]\n"
    "s_add_u32 s70, s70, 28672\n"
    "buffer_load_dwordx4 v[192:195], v113, s[12:15], s70 offen offset:0 sc1\n"
    "v_mfma_f32_16x16x32_bf16 v[164:167], v[200:203], v[20:23], v[164:167]\n"
    "v_mfma_f32_16x16x32_bf16 v[168:171], v[204:207], v[20:23], v[168:171]\n"
    "v_mfma_f32_16x16x32_bf16 v[172:175], v[200:203], v[24:27], v[172:175]\n"
    "v_mfma_f32_16x16x32_bf16 v[176:179], v[204:207], v[24:27], v[176:179]\n"
    "v_mfma_f32_16x16x32_bf16 v[180:183], v[200:203], v[28:31], v[180:183]\n"
    "buffer_load_dwordx4 v[200:203], v113, s[12:15], s70 offen offset:256 sc1\n"
    "v_mfma_f32_16x16x32_bf16 v[184:187], v[204:207], v[28:31], v[184:187]\n"
    "s_waitcnt vmcnt(10)\n"
    "v_mfma_f32_16x16x32_bf16 v[156:159], v[220:223], v[32:35], v[156:159]\n"
    "s_waitcnt vmcnt(9)\n"
    "v_mfma_f32_16x16x32_bf16 v[160:163], v[212:215], v[32:35], v[160:163]\n"
    "s_add_u32 s70, s70, 28672\n"
    "buffer_load_dwordx4 v[204:207], v113, s[12:15], s70 offen offset:0 sc1\n"
    "v_mfma_f32_16x16x32_bf16 v[164:167], v[220:223], v[36:39], v[164:167]\n"
    "v_mfma_f32_16x16x32_bf16 v[168:171], v[212:215], v[36:39], v[168:171]\n"
    "v_mfma_f32_16x16x32_bf16 v[172:175], v[220:223], v[40:43], v[172:175]\n"
    "v_mfma_f32_16x16x32_bf16 v[176:179], v[212:215], v[40:43], v[176:179]\n"
    "v_mfma_f32_16x16x32_bf16 v[180:183], v[220:223], v[44:47], v[180:183]\n"
    "buffer_load_dwordx4 v[220:223], v113, s[12:15], s70 offen offset:256 sc1\n"
    "v_mfma_f32_16x16x32_bf16 v[184:187], v[212:215], v[44:47], v[184:187]\n"
    "s_waitcnt vmcnt(10)\n"
    "v_mfma_f32_16x16x32_bf16 v[156:159], v[224:227], v[48:51], v[156:159]\n"
    "s_waitcnt vmcnt(9)\n"
    "v_mfma_f32_16x16x32_bf16 v[160:163], v[216:219], v[48:51], v[160:163]\n"
    "s_add_u32 s70, s70, 28672\n"
    "buffer_load_dwordx4 v[212:215], v113, s[12:15], s70 offen offset:0 sc1\n"
    "v_mfma_f32_16x16x32_bf16 v[164:167], v[224:227], v[52:55], v[164:167]\n"
    "v_mfma_f32_16x16x32_bf16 v[168:171], v[216:219], v[52:55], v[168:171]\n"
    "v_mfma_f32_16x16x32_bf16 v[172:175], v[224:227], v[56:59], v[172:175]\n"
    "v_mfma_f32_16x16x32_bf16 v[176:179], v[216:219], v[56:59], v[176:179]\n"
    "v_mfma_f32_16x16x32_bf16 v[180:183], v[224:227], v[60:63], v[180:183]\n"
    "buffer_load_dwordx4 v[224:227], v113, s[12:15], s70 offen offset:256 sc1\n"
    "v_mfma_f32_16x16x32_bf16 v[184:187], v[216:219], v[60:63], v[184:187]\n"
    "s_waitcnt vmcnt(10)\n"
    "v_mfma_f32_16x16x32_bf16 v[156:159], v[196:199], v[64:67], v[156:159]\n"
    "s_waitcnt vmcnt(9)\n"
    "v_mfma_f32_16x16x32_bf16 v[160:163], v[188:191], v[64:67], v[160:163]\n"
    "s_add_u32 s70, s70, 28672\n"
    "buffer_load_dwordx4 v[216:219], v113, s[12:15], s70 offen offset:0 sc1\n"
    "v_mfma_f32_16x16x32_bf16 v[164:167], v[196:199], v[68:71], v[164:167]\n"
    "v_mfma_f32_16x16x32_bf16 v[168:171], v[188:191], v[68:71], v[168:171]\n"
    "v_mfma_f32_16x16x32_bf16 v[172:175], v[196:199], v[72:75], v[172:175]\n"
    "v_mfma_f32_16x16x32_bf16 v[176:179], v[188:191], v[72:75], v[176:179]\n"
    "v_mfma_f32_16x16x32_bf16 v[180:183], v[196:199], v[76:79], v[180:183]\n"
    "ds_read_b64_tr_b16 v[244:245], v115 offset:0\n"
    "buffer_load_dwordx4 v[196:199], v113, s[12:15], s70 offen offset:256 sc1\n"
    "v_mfma_f32_16x16x32_bf16 v[184:187], v[188:191], v[76:79], v[184:187]\n"
    "ds_read_b64_tr_b16 v[246:247], v115 offset:512\n"
    "s_waitcnt vmcnt(10)\n"
    "v_mfma_f32_16x16x32_bf16 v[156:159], v[108:111], v[80:83], v[156:159]\n"
    "ds_read_b64_tr_b16 v[248:249], v116 offset:0\n"
    "s_waitcnt vmcnt(9)\n"
    "v_mfma_f32_16x16x32_bf16 v[160:163], v[208:211], v[80:83], v[160:163]\n"
    "s_add_u32 s70, s70, 28672\n"
    "buffer_load_dwordx4 v[188:191], v113, s[12:15], s70 offen offset:0 sc1\n"
    "v_mfma_f32_16x16x32_bf16 v[164:167], v[108:111], v[84:87], v[164:167]\n"
    "ds_read_b64_tr_b16 v[250:251], v116 offset:512\n"
    "ds_read_b64_tr_b16 v[252:253], v117 offset:0\n"
    "v_mfma_f32_16x16x32_bf16 v[168:171], v[208:211], v[84:87], v[168:171]\n"
    "ds_read_b64_tr_b16 v[254:255], v117 offset:512\n"
    "v_mfma_f32_16x16x32_bf16 v[172:175], v[108:111], v[88:91], v[172:175]\n"
    "ds_read_b64_tr_b16 v[232:233], v118 offset:0\n"
    "v_mfma_f32_16x16x32_bf16 v[176:179], v[208:211], v[88:91], v[176:179]\n"
    "ds_read_b64_tr_b16 v[234:235], v118 offset:512\n"
    "v_mfma_f32_16x16x32_bf16 v[180:183], v[108:111], v[92:95], v[180:183]\n"
    "ds_read_b64_tr_b16 v[236:237], v115 offset:4096\n"
    "buffer_load_dwordx4 v[108:111], v113, s[12:15], s70 offen offset:256 sc1\n"
    "v_mfma_f32_16x16x32_bf16 v[184:187], v[208:211], v[92:95], v[184:187]\n"
    "s_waitcnt vmcnt(9) lgkmcnt(7)\n"
    "v_mfma_f32_16x16x32_bf16 v[156:159], v[192:195], v[244:247], v[156:159]\n"
    "ds_read_b64_tr_b16 v[238:239], v115 offset:4608\n"
    "s_waitcnt vmcnt(8)\n"
    "v_mfma_f32_16x16x32_bf16 v[160:163], v[200:203], v[244:247], v[160:163]\n"
    "s_add_u32 s70, s70, 28672\n"
    "buffer_load_dwordx4 v[208:211], v113, s[12:15], s70 offen offset:0 sc1\n"
    "s_waitcnt lgkmcnt(6)\n"
    "v_mfma_f32_16x16x32_bf16 v[164:167], v[192:195], v[248:251], v[164:167]\n"
    "ds_read_b64_tr_b16 v[240:241], v116 offset:4096\n"
    "ds_read_b64_tr_b16 v[242:243], v116 offset:4608\n"
    "v_mfma_f32_16x16x32_bf16 v[168:171], v[200:203], v[248:251], v[168:171]\n"
    "ds_read_b64_tr_b16 v[244:245], v117 offset:4096\n"
    "s_waitcnt lgkmcnt(7)\n"
    "v_mfma_f32_16x16x32_bf16 v[172:175], v[192:195], v[252:255], v[172:175]\n"
    "ds_read_b64_tr_b16 v[246:247], v117 offset:4608\n"
    "ds_read_b64_tr_b16 v[248:249], v118 offset:4096\n"
    "v_mfma_f32_16x16x32_bf16 v[176:179], v[200:203], v[252:255], v[176:179]\n"
    "ds_read_b64_tr_b16 v[250:251], v118 offset:4608\n"
    "s_waitcnt lgkmcnt(8)\n"
    "v_mfma_f32_16x16x32_bf16 v[180:183], v[192:195], v[232:235], v[180:183]\n"
    "ds_read_b64_tr_b16 v[252:253], v115 offset:8192\n"
    "buffer_load_dwordx4 v[192:195], v113, s[12:15], s70 offen offset:256 sc1\n"
    "v_mfma_f32_16x16x32_bf16 v[184:187], v[200:203], v[232:235], v[184:187]\n"
    "s_waitcnt vmcnt(9) lgkmcnt(7)\n"
    "v_mfma_f32_16x16x32_bf16 v[156:159], v[204:207], v[236:239], v[156:159]\n"
    "ds_read_b64_tr_b16 v[254:255], v115 offset:8704\n"
    "s_waitcnt vmcnt(8)\n"
    "v_mfma_f32_16x16x32_bf16 v[160:163], v[220:223], v[236:239], v[160:163]\n"
    "s_add_u32 s70, s70, 28672\n"
    "buffer_load_dwordx4 v[200:203], v113, s[12:15], s70 offen offset:0 sc1\n"
    "s_waitcnt lgkmcnt(6)\n"
    "v_mfma_f32_16x16x32_bf16 v[164:167], v[204:207], v[240:243], v[164:167]\n"
    "ds_read_b64_tr_b16 v[232:233], v116 offset:8192\n"
    "ds_read_b64_tr_b16 v[234:235], v116 offset:8704\n"
    "v_mfma_f32_16x16x32_bf16 v[168:171], v[220:223], v[240:243], v[168:171]\n"
    "ds_read_b64_tr_b16 v[236:237], v117 offset:8192\n"
    "s_waitcnt lgkmcnt(7)\n"
    "v_mfma_f32_16x16x32_bf16 v[172:175], v[204:207], v[244:247], v[172:175]\n"
    "ds_read_b64_tr_b16 v[238:239], v117 offset:8704\n"
    "ds_read_b64_tr_b16 v[240:241], v118 offset:8192\n"
    "v_mfma_f32_16x16x32_bf16 v[176:179], v[220:223], v[244:247], v[176:179]\n"
    "ds_read_b64_tr_b16 v[242:243], v118 offset:8704\n"
    "s_waitcnt lgkmcnt(8)\n"
    "v_mfma_f32_16x16x32_bf16 v[180:183], v[204:207], v[248:251], v[180:183]\n"
    "ds_read_b64_tr_b16 v[244:245], v115 offset:12288\n"
    "buffer_load_dwordx4 v[204:207], v113, s[12:15], s70 offen offset:256 sc1\n"
    "v_mfma_f32_16x16x32_bf16 v[184:187], v[220:223], v[248:251], v[184:187]\n"
    "s_waitcnt vmcnt(9) lgkmcnt(7)\n"
    "v_mfma_f32_16x16x32_bf16 v[156:159], v[212:215], v[252:255], v[156:159]\n"
    "ds_read_b64_tr_b16 v[246:247], v115 offset:12800\n"
    "s_waitcnt vmcnt(8)\n"
    "v_mfma_f32_16x16x32_bf16 v[160:163], v[224:227], v[252:255], v[160:163]\n"
    "s_add_u32 s70, s70, 28672\n"
    "buffer_load_dwordx4 v[220:223], v113, s[12:15], s70 offen offset:0 sc1\n"
    "s_waitcnt lgkmcnt(6)\n"
    "v_mfma_f32_16x16x32_bf16 v[164:167], v[212:215], v[232:235], v[164:167]\n"
    "ds_read_b64_tr_b16 v[248:249], v116 offset:12288\n"
    "ds_read_b64_tr_b16 v[250:251], v116 offset:12800\n"
    "v_mfma_f32_16x16x32_bf16 v[168:171], v[224:227], v[232:235], v[168:171]\n"
    "ds_read_b64_tr_b16 v[252:253], v117 offset:12288\n"
    "s_waitcnt lgkmcnt(7)\n"
    "v_mfma_f32_16x16x32_bf16 v[172:175], v[212:215], v[236:239], v[172:175]\n"
    "ds_read_b64_tr_b16 v[254:255], v117 offset:12800\n"
    "ds_read_b64_tr_b16 v[232:233], v118 offset:12288\n"
    "v_mfma_f32_16x16x32_bf16 v[176:179], v[224:227], v[236:239], v[176:179]\n"
    "ds_read_b64_tr_b16 v[234:235], v118 offset:12800\n"
    "s_waitcnt lgkmcnt(8)\n"
    "v_mfma_f32_16x16x32_bf16 v[180:183], v[212:215], v[240:243], v[180:183]\n"
    "ds_read_b64_tr_b16 v[236:237], v115 offset:16384\n"
    "buffer_load_dwordx4 v[212:215], v113, s[12:15], s70 offen offset:256 sc1\n"
    "v_mfma_f32_16x16x32_bf16 v[184:187], v[224:227], v[240:243], v[184:187]\n"
    "s_waitcnt vmcnt(9) lgkmcnt(7)\n"
    "v_mfma_f32_16x16x32_bf16 v[156:159], v[216:219], v[244:247], v[156:159]\n"
    "ds_read_b64_tr_b16 v[238:239], v115 offset:16896\n"
    "s_waitcnt vmcnt(8)\n"
    "v_mfma_f32_16x16x32_bf16 v[160:163], v[196:199], v[244:247], v[160:163]\n"
    "ds_read_b64_tr_b16 v[240:241], v116 offset:16384\n"
    "s_waitcnt lgkmcnt(7)\n"
    "v_mfma_f32_16x16x32_bf16 v[164:167], v[216:219], v[248:251], v[164:167]\n"
    "ds_read_b64_tr_b16 v[242:243], v116 offset:16896\n"
    "ds_read_b64_tr_b16 v[244:245], v117 offset:16384\n"
    "v_mfma_f32_16x16x32_bf16 v[168:171], v[196:199], v[248:251], v[168:171]\n"
    "ds_read_b64_tr_b16 v[246:247], v117 offset:16896\n"
    "s_waitcnt lgkmcnt(8)\n"
    "v_mfma_f32_16x16x32_bf16 v[172:175], v[216:219], v[252:255], v[172:175]\n"
    "ds_read_b64_tr_b16 v[248:249], v118 offset:16384\n"
    "v_mfma_f32_16x16x32_bf16 v[176:179], v[196:199], v[252:255], v[176:179]\n"
    "ds_read_b64_tr_b16 v[250:251], v118 offset:16896\n"
    "s_waitcnt lgkmcnt(8)\n"
    "v_mfma_f32_16x16x32_bf16 v[180:183], v[216:219], v[232:235], v[180:183]\n"
    "ds_read_b64_tr_b16 v[252:253], v115 offset:20480\n"
    "v_mfma_f32_16x16x32_bf16 v[184:187], v[196:199], v[232:235], v[184:187]\n"
    "s_waitcnt vmcnt(7) lgkmcnt(7)\n"
    "v_mfma_f32_16x16x32_bf16 v[156:159], v[188:191], v[236:239], v[156:159]\n"
    "ds_read_b64_tr_b16 v[254:255], v115 offset:20992\n"
    "s_waitcnt vmcnt(6)\n"
    "v_mfma_f32_16x16x32_bf16 v[160:163], v[108:111], v[236:239], v[160:163]\n"
    "ds_read_b64_tr_b16 v[232:233], v116 offset:20480\n"
    "s_waitcnt lgkmcnt(7)\n"
    "v_mfma_f32_16x16x32_bf16 v[164:167], v[188:191], v[240:243], v[164:167]\n"
    "ds_read_b64_tr_b16 v[234:235], v116 offset:20992\n"
    "ds_read_b64_tr_b16 v[236:237], v117 offset:20480\n"
    "v_mfma_f32_16x16x32_bf16 v[168:171], v[108:111], v[240:243], v[168:171]\n"
    "ds_read_b64_tr_b16 v[238:239], v117 offset:20992\n"
    "s_waitcnt lgkmcnt(8)\n"
    "v_mfma_f32_16x16x32_bf16 v[172:175], v[188:191], v[244:247], v[172:175]\n"
    "ds_read_b64_tr_b16 v[240:241], v118 offset:20480\n"
    "v_mfma_f32_16x16x32_bf16 v[176:179], v[108:111], v[244:247], v[176:179]\n"
    "ds_read_b64_tr_b16 v[242:243], v118 offset:20992\n"
    "s_waitcnt lgkmcnt(8)\n"
    "v_mfma_f32_16x16x32_bf16 v[180:183], v[188:191], v[248:251], v[180:183]\n"
    "ds_read_b64_tr_b16 v[244:245], v116 offset:24576\n"
    "v_mfma_f32_16x16x32_bf16 v[184:187], v[108:111], v[248:251], v[184:187]\n"
    "s_waitcnt vmcnt(5) lgkmcnt(7)\n"
    "v_mfma_f32_16x16x32_bf16 v[156:159], v[208:211], v[252:255], v[156:159]\n"
    "ds_read_b64_tr_b16 v[246:247], v116 offset:25088\n"
    "s_waitcnt vmcnt(4)\n"
    "v_mfma_f32_16x16x32_bf16 v[160:163], v[192:195], v[252:255], v[160:163]\n"
    "ds_read_b64_tr_b16 v[248:249], v117 offset:24576\n"
    "s_waitcnt lgkmcnt(7)\n"
    "v_mfma_f32_16x16x32_bf16 v[164:167], v[208:211], v[232:235], v[164:167]\n"
    "ds_read_b64_tr_b16 v[250:251], v117 offset:25088\n"
    "ds_read_b64_tr_b16 v[252:253], v118 offset:24576\n"
    "v_mfma_f32_16x16x32_bf16 v[168:171], v[192:195], v[232:235], v[168:171]\n"
    "ds_read_b64_tr_b16 v[254:255], v118 offset:25088\n"
    "s_waitcnt lgkmcnt(8)\n"
    "v_mfma_f32_16x16x32_bf16 v[172:175], v[208:211], v[236:239], v[172:175]\n"
    "ds_read_b64_tr_b16 v[232:233], v116 offset:28672\n"
    "v_mfma_f32_16x16x32_bf16 v[176:179], v[192:195], v[236:239], v[176:179]\n"
    "ds_read_b64_tr_b16 v[234:235], v116 offset:29184\n"
    "s_waitcnt lgkmcnt(8)\n"
    "v_mfma_f32_16x16x32_bf16 v[180:183], v[208:211], v[240:243], v[180:183]\n"
    "ds_read_b64_tr_b16 v[236:237], v117 offset:28672\n"
    "v_mfma_f32_16x16x32_bf16 v[184:187], v[192:195], v[240:243], v[184:187]\n"
    "s_waitcnt vmcnt(3) lgkmcnt(7)\n"
    "v_mfma_f32_16x16x32_bf16 v[164:167], v[200:203], v[244:247], v[164:167]\n"
    "ds_read_b64_tr_b16 v[238:239], v117 offset:29184\n"
    "s_waitcnt vmcnt(2)\n"
    "v_mfma_f32_16x16x32_bf16 v[168:171], v[204:207], v[244:247], v[168:171]\n"
    "ds_read_b64_tr_b16 v[240:241], v118 offset:28672\n"
    "s_waitcnt lgkmcnt(7)\n"
    "v_mfma_f32_16x16x32_bf16 v[172:175], v[200:203], v[248:251], v[172:175]\n"
    "ds_read_b64_tr_b16 v[242:243], v118 offset:29184\n"
    "ds_read_b64_tr_b16 v[244:245], v115 offset:24576\n"
    "v_mfma_f32_16x16x32_bf16 v[176:179], v[204:207], v[248:251], v[176:179]\n"
    "ds_read_b64_tr_b16 v[246:247], v115 offset:25088\n"
    "s_waitcnt lgkmcnt(8)\n"
    "v_mfma_f32_16x16x32_bf16 v[180:183], v[200:203], v[252:255], v[180:183]\n"
    "ds_read_b64_tr_b16 v[248:249], v115 offset:28672\n"
    "v_mfma_f32_16x16x32_bf16 v[184:187], v[204:207], v[252:255], v[184:187]\n"
    "s_waitcnt vmcnt(1) lgkmcnt(7)\n"
    "v_mfma_f32_16x16x32_bf16 v[164:167], v[220:223], v[232:235], v[164:167]\n"
    "ds_read_b64_tr_b16 v[250:251], v115 offset:29184\n"
    "s_waitcnt vmcnt(0)\n"
    "v_mfma_f32_16x16x32_bf16 v[168:171], v[212:215], v[232:235], v[168:171]\n"
    "s_waitcnt lgkmcnt(6)\n"
    "v_mfma_f32_16x16x32_bf16 v[172:175], v[220:223], v[236:239], v[172:175]\n"
    "v_mfma_f32_16x16x32_bf16 v[176:179], v[212:215], v[236:239], v[176:179]\n"
    "v_pk_add_f32 v[148:149], v[164:165], v[166:167]\n"
    "v_pk_add_f32 v[150:151], v[168:169], v[170:171]\n"
    "v_pk_add_f32 v[148:149], v[148:149], v[150:151]\n"
    "v_cmp_u_f32 vcc, v148, v149\n"
    "s_waitcnt lgkmcnt(4)\n"
    "v_mfma_f32_16x16x32_bf16 v[180:183], v[220:223], v[240:243], v[180:183]\n"
    "ds_write_b128 v119, v[164:167] offset:0\n"
    "v_mfma_f32_16x16x32_bf16 v[184:187], v[212:215], v[240:243], v[184:187]\n"
    "s_waitcnt lgkmcnt(3)\n"
    "v_mfma_f32_16x16x32_bf16 v[156:159], v[200:203], v[244:247], v[156:159]\n"
    "ds_write_b128 v119, v[168:171] offset:1024\n"
    "v_mfma_f32_16x16x32_bf16 v[160:163], v[204:207], v[244:247], v[160:163]\n"
    "s_waitcnt lgkmcnt(2)\n"
    "v_mfma_f32_16x16x32_bf16 v[156:159], v[220:223], v[248:251], v[156:159]\n"
    "ds_write_b128 v119, v[172:175] offset:2048\n"
    "v_mfma_f32_16x16x32_bf16 v[160:163], v[212:215], v[248:251], v[160:163]\n"
    "ds_write_b128 v119, v[176:179] offset:3072\n"
    ".Lmark_p5k_93:\n"
    "ds_write_b128 v119, v[180:183] offset:4096\n"
    "ds_write_b128 v119, v[184:187] offset:5120\n"
    "s_cbranch_vccnz .Lredo_98\n"
    ".Lredook_99:\n"
    ".Lback_95:\n"
    "s_waitcnt lgkmcnt(0)\n"
    "s_barrier\n"
    "ds_read_b128 v[164:167], v120 offset:0\n"
    "ds_read_b128 v[168:171], v120 offset:1024\n"
    "ds_read_b128 v[172:175], v121 offset:2048\n"
    "ds_read_b128 v[176:179], v121 offset:3072\n"
    "ds_read_b128 v[180:183], v122 offset:4096\n"
    "ds_read_b128 v[184:187], v122 offset:5120\n"
    "s_waitcnt lgkmcnt(3)\n"
    "v_pk_add_f32 v[164:165], v[164:165], v[172:173]\n"
    "v_pk_add_f32 v[166:167], v[166:167], v[174:175]\n"
    "s_waitcnt lgkmcnt(2)\n"
    "v_pk_add_f32 v[168:169], v[168:169], v[176:177]\n"
    "v_pk_add_f32 v[170:171], v[170:171], v[178:179]\n"
    "s_waitcnt lgkmcnt(1)\n"
    "v_pk_add_f32 v[156:157], v[156:157], v[180:181]\n"
    "v_pk_add_f32 v[158:159], v[158:159], v[182:183]\n"
    "s_waitcnt lgkmcnt(0)\n"
    "v_pk_add_f32 v[160:161], v[160:161], v[184:185]\n"
    "v_pk_add_f32 v[162:163], v[162:163], v[186:187]\n"
    "v_pk_add_f32 v[156:157], v[156:157], v[164:165]\n"
    "v_pk_add_f32 v[158:159], v[158:159], v[166:167]\n"
    "v_pk_add_f32 v[160:161], v[160:161], v[168:169]\n"
    "v_pk_add_f32 v[162:163], v[162:163], v[170:171]\n"
    "v_cvt_pk_bf16_f32 v168, v156, v157\n"
    "v_cvt_pk_bf16_f32 v169, v158, v159\n"
    "v_cvt_pk_bf16_f32 v170, v160, v161\n"
    "v_cvt_pk_bf16_f32 v171, v162, v163\n"
    "buffer_store_dwordx4 v[168:171], v140, s[44:47], 0 offen\n"
    ".Lmark_end_100:\n"
    ".Lend:\n"
    "s_waitcnt vmcnt(0) lgkmcnt(0)\n"
    "s_endpgm\n"
    ".Lretry_4:\n"
    "s_sub_u32 s60, s60, 1\n"
    "s_cbranch_scc1 .Lretry_4_giveup\n"
    "buffer_load_dwordx4 v[220:223], v114, s[12:15], s78 offen sc1\n"
    "buffer_load_dwordx4 v[224:227], v114, s[12:15], s79 offen sc1\n"
    "buffer_load_dwordx4 v[228:231], v114, s[12:15], s80 offen sc1\n"
    "buffer_load_dwordx4 v[232:235], v114, s[12:15], s81 offen sc1\n"
    "buffer_load_dword v32, v254, s[12:15], s82 offen sc1\n"
    "s_waitcnt vmcnt(0)\n"
    "v_add3_u32 v236, v220, v221, v222\n"
    "v_add3_u32 v237, v224, v225, v226\n"
    "v_add3_u32 v238, v228, v229, v230\n"
    "v_add3_u32 v239, v232, v233, v234\n"
    "v_add_u32 v236, v236, v223\n"
    "v_add_u32 v237, v237, v227\n"
    "v_add_u32 v238, v238, v231\n"
    "v_add_u32 v239, v239, v235\n"
    "v_add_u32_dpp v236, v236, v236 quad_perm:[1,0,3,2] row_mask:0xf bank_mask:0xf\n"
    "v_add_u32_dpp v237, v237, v237 quad_perm:[1,0,3,2] row_mask:0xf bank_mask:0xf\n"
    "v_add_u32_dpp v238, v238, v238 quad_perm:[1,0,3,2] row_mask:0xf bank_mask:0xf\n"
    "v_add_u32_dpp v239, v239, v239 quad_perm:[1,0,3,2] row_mask:0xf bank_mask:0xf\n"
    "v_add_u32_dpp v236, v236, v236 quad_perm:[2,3,0,1] row_mask:0xf bank_mask:0xf\n"
    "v_add_u32_dpp v237, v237, v237 quad_perm:[2,3,0,1] row_mask:0xf bank_mask:0xf\n"
    "v_add_u32_dpp v238, v238, v238 quad_perm:[2,3,0,1] row_mask:0xf bank_mask:0xf\n"
    "v_add_u32_dpp v239, v239, v239 quad_perm:[2,3,0,1] row_mask:0xf bank_mask:0xf\n"
    "v_cndmask_b32 v236, v236, v237, s[16:17]\n"
    "v_cndmask_b32 v236, v236, v238, s[18:19]\n"
    "v_cndmask_b32 v236, v236, v239, s[26:27]\n"
    "v_add_u32 v236, v236, v32\n"
    "v_cmp_ne_u32 vcc, s58, v236\n"
    "s_cbranch_vccnz .Lretry_4\n"
    "s_branch .Lx0_5\n"
    ".Lretry_4_giveup:\n"
    "s_mov_b32 s60, 0\n"
    "s_waitcnt vmcnt(0)\n"
    "s_branch .Lx0_5\n"
    ".Lretry_8:\n"
    "s_sub_u32 s60, s60, 1\n"
    "s_cbranch_scc1 .Lretry_8_giveup\n"
    "buffer_load_dwordx4 v[188:191], v115, s[12:15], s70 offen sc1\n"
    "buffer_load_dwordx4 v[192:195], v123, s[12:15], s71 offen sc1\n"
    "s_waitcnt vmcnt(0)\n"
    "v_max3_u32 v244, v188, v189, v190\n"
    "v_max3_u32 v244, v244, v191, v192\n"
    "v_max3_u32 v244, v244, v193, v194\n"
    "v_max_u32 v244, v244, v195\n"
    "v_cmp_eq_u32 vcc, -1, v244\n"
    "s_cbranch_vccnz .Lretry_8\n"
    "s_branch .Lz_9\n"
    ".Lretry_8_giveup:\n"
    "s_mov_b32 s60, 0\n"
    "s_waitcnt vmcnt(0)\n"
    "s_branch .Lz_9\n"
    ".Lg4retry_13:\n"
    "s_sub_u32 s60, s60, 1\n"
    "s_cbranch_scc1 .Lg4retry_13_giveup\n"
    "buffer_load_dword v124, v125, s[12:15], s71 offen sc1\n"
    "s_waitcnt vmcnt(0)\n"
    "v_cmp_eq_u32 vcc, -1, v124\n"
    "s_cbranch_vccnz .Lg4retry_13\n"
    "s_branch .Lg4ok_14\n"
    ".Lg4retry_13_giveup:\n"
    "s_mov_b32 s60, 0\n"
    "s_waitcnt vmcnt(0)\n"
    "s_branch .Lg4ok_14\n"
    ".Lclean_17:\n"
    "v_mov_b32 v239, 0\n"
    "s_add_u32 s81, s71, 0\n"
    "buffer_load_dwordx4 v[240:243], v113, s[12:15], s81 offen offset:512 sc1\n"
    "buffer_load_dwordx4 v[244:247], v114, s[12:15], s81 offen offset:768 sc1\n"
    "s_waitcnt vmcnt(0)\n"
    "v_max3_u32 v239, v239, v244, v245\n"
    "v_max3_u32 v239, v239, v246, v247\n"
    "v_max3_u32 v239, v239, v240, v241\n"
    "v_max3_u32 v239, v239, v242, v243\n"
    "v_cndmask_b32 v228, 0, v244, s[4:5]\n"
    "v_cndmask_b32 v229, 0, v245, s[4:5]\n"
    "v_cndmask_b32 v230, 0, v246, s[4:5]\n"
    "v_cndmask_b32 v231, 0, v247, s[4:5]\n"
    "v_mfma_f32_16x16x32_bf16 v[124:127], a[0:3], v[240:243], 0\n"
    "s_nop 0\n"
    "v_mfma_f32_16x16x32_bf16 v[128:131], a[0:3], v[228:231], 0\n"
    "v_mfma_f32_16x16x32_bf16 v[132:135], a[4:7], v[240:243], 0\n"
    "v_mfma_f32_16x16x32_bf16 v[136:139], a[4:7], v[228:231], 0\n"
    "v_mfma_f32_16x16x32_bf16 v[140:143], a[8:11], v[240:243], 0\n"
    "v_mfma_f32_16x16x32_bf16 v[144:147], a[8:11], v[228:231], 0\n"
    "v_mfma_f32_16x16x32_bf16 v[148:151], a[12:15], v[240:243], 0\n"
    "v_mfma_f32_16x16x32_bf16 v[152:155], a[12:15], v[228:231], 0\n"
    "s_add_u32 s81, s71, 28672\n"
    "buffer_load_dwordx4 v[240:243], v113, s[12:15], s81 offen offset:512 sc1\n"
    "s_waitcnt vmcnt(0)\n"
    "v_max3_u32 v239, v239, v240, v241\n"
    "v_max3_u32 v239, v239, v242, v243\n"
    "v_cndmask_b32 v228, v244, 0, s[4:5]\n"
    "v_cndmask_b32 v229, v245, 0, s[4:5]\n"
    "v_cndmask_b32 v230, v246, 0, s[4:5]\n"
    "v_cndmask_b32 v231, v247, 0, s[4:5]\n"
    "v_mfma_f32_16x16x32_bf16 v[124:127], a[16:19], v[240:243], v[124:127]\n"
    "s_nop 0\n"
    "v_mfma_f32_16x16x32_bf16 v[128:131], a[16:19], v[228:231], v[128:131]\n"
    "v_mfma_f32_16x16x32_bf16 v[132:135], a[20:23], v[240:243], v[132:135]\n"
    "v_mfma_f32_16x16x32_bf16 v[136:139], a[20:23], v[228:231], v[136:139]\n"
    "v_mfma_f32_16x16x32_bf16 v[140:143], a[24:27], v[240:243], v[140:143]\n"
    "v_mfma_f32_16x16x32_bf16 v[144:147], a[24:27], v[228:231], v[144:147]\n"
    "v_mfma_f32_16x16x32_bf16 v[148:151], a[28:31], v[240:243], v[148:151]\n"
    "v_mfma_f32_16x16x32_bf16 v[152:155], a[28:31], v[228:231], v[152:155]\n"
    "s_add_u32 s81, s71, 57344\n"
    "buffer_load_dwordx4 v[240:243], v113, s[12:15], s81 offen offset:512 sc1\n"
    "buffer_load_dwordx4 v[244:247], v114, s[12:15], s81 offen offset:768 sc1\n"
    "s_waitcnt vmcnt(0)\n"
    "v_max3_u32 v239, v239, v244, v245\n"
    "v_max3_u32 v239, v239, v246, v247\n"
    "v_max3_u32 v239, v239, v240, v241\n"
    "v_max3_u32 v239, v239, v242, v243\n"
    "v_cndmask_b32 v228, 0, v244, s[4:5]\n"
    "v_cndmask_b32 v229, 0, v245, s[4:5]\n"
    "v_cndmask_b32 v230, 0, v246, s[4:5]\n"
    "v_cndmask_b32 v231, 0, v247, s[4:5]\n"
    "v_mfma_f32_16x16x32_bf16 v[124:127], a[32:35], v[240:243], v[124:127]\n"
    "s_nop 0\n"
    "v_mfma_f32_16x16x32_bf16 v[128:131], a[32:35], v[228:231], v[128:131]\n"
    "v_mfma_f32_16x16x32_bf16 v[132:135], a[36:39], v[240:243], v[132:135]\n"
    "v_mfma_f32_16x16x32_bf16 v[136:139], a[36:39], v[228:231], v[136:139]\n"
    "v_mfma_f32_16x16x32_bf16 v[140:143], a[40:43], v[240:243], v[140:143]\n"
    "v_mfma_f32_16x16x32_bf16 v[144:147], a[40:43], v[228:231], v[144:147]\n"
    "v_mfma_f32_16x16x32_bf16 v[148:151], a[44:47], v[240:243], v[148:151]\n"
    "v_mfma_f32_16x16x32_bf16 v[152:155], a[44:47], v[228:231], v[152:155]\n"
    "s_add_u32 s81, s71, 86016\n"
    "buffer_load_dwordx4 v[240:243], v113, s[12:15], s81 offen offset:512 sc1\n"
    "s_waitcnt vmcnt(0)\n"
    "v_max3_u32 v239, v239, v240, v241\n"
    "v_max3_u32 v239, v239, v242, v243\n"
    "v_cndmask_b32 v228, v244, 0, s[4:5]\n"
    "v_cndmask_b32 v229, v245, 0, s[4:5]\n"
    "v_cndmask_b32 v230, v246, 0, s[4:5]\n"
    "v_cndmask_b32 v231, v247, 0, s[4:5]\n"
    "v_mfma_f32_16x16x32_bf16 v[124:127], a[48:51], v[240:243], v[124:127]\n"
    "s_nop 0\n"
    "v_mfma_f32_16x16x32_bf16 v[128:131], a[48:51], v[228:231], v[128:131]\n"
    "v_mfma_f32_16x16x32_bf16 v[132:135], a[52:55], v[240:243], v[132:135]\n"
    "v_mfma_f32_16x16x32_bf16 v[136:139], a[52:55], v[228:231], v[136:139]\n"
    "v_mfma_f32_16x16x32_bf16 v[140:143], a[56:59], v[240:243], v[140:143]\n"
    "v_mfma_f32_16x16x32_bf16 v[144:147], a[56:59], v[228:231], v[144:147]\n"
    "v_mfma_f32_16x16x32_bf16 v[148:151], a[60:63], v[240:243], v[148:151]\n"
    "v_mfma_f32_16x16x32_bf16 v[152:155], a[60:63], v[228:231], v[152:155]\n"
    "s_add_u32 s81, s71, 114688\n"
    "buffer_load_dwordx4 v[240:243], v113, s[12:15], s81 offen offset:512 sc1\n"
    "buffer_load_dwordx4 v[244:247], v114, s[12:15], s81 offen offset:768 sc1\n"
    "s_waitcnt vmcnt(0)\n"
    "v_max3_u32 v239, v239, v244, v245\n"
    "v_max3_u32 v239, v239, v246, v247\n"
    "v_max3_u32 v239, v239, v240, v241\n"
    "v_max3_u32 v239, v239, v242, v243\n"
    "v_cndmask_b32 v228, 0, v244, s[4:5]\n"
    "v_cndmask_b32 v229, 0, v245, s[4:5]\n"
    "v_cndmask_b32 v230, 0, v246, s[4:5]\n"
    "v_cndmask_b32 v231, 0, v247, s[4:5]\n"
    "v_mfma_f32_16x16x32_bf16 v[124:127], a[64:67], v[240:243], v[124:127]\n"
    "s_nop 0\n"
    "v_mfma_f32_16x16x32_bf16 v[128:131], a[64:67], v[228:231], v[128:131]\n"
    "v_mfma_f32_16x16x32_bf16 v[132:135], a[68:71], v[240:243], v[132:135]\n"
    "v_mfma_f32_16x16x32_bf16 v[136:139], a[68:71], v[228:231], v[136:139]\n"
    "v_mfma_f32_16x16x32_bf16 v[140:143], a[72:75], v[240:243], v[140:143]\n"
    "v_mfma_f32_16x16x32_bf16 v[144:147], a[72:75], v[228:231], v[144:147]\n"
    "v_mfma_f32_16x16x32_bf16 v[148:151], a[76:79], v[240:243], v[148:151]\n"
    "v_mfma_f32_16x16x32_bf16 v[152:155], a[76:79], v[228:231], v[152:155]\n"
    "s_add_u32 s81, s71, 143360\n"
    "buffer_load_dwordx4 v[240:243], v113, s[12:15], s81 offen offset:512 sc1\n"
    "s_waitcnt vmcnt(0)\n"
    "v_max3_u32 v239, v239, v240, v241\n"
    "v_max3_u32 v239, v239, v242, v243\n"
    "v_cndmask_b32 v228, v244, 0, s[4:5]\n"
    "v_cndmask_b32 v229, v245, 0, s[4:5]\n"
    "v_cndmask_b32 v230, v246, 0, s[4:5]\n"
    "v_cndmask_b32 v231, v247, 0, s[4:5]\n"
    "v_mfma_f32_16x16x32_bf16 v[124:127], a[80:83], v[240:243], v[124:127]\n"
    "s_nop 0\n"
    "v_mfma_f32_16x16x32_bf16 v[128:131], a[80:83], v[228:231], v[128:131]\n"
    "v_mfma_f32_16x16x32_bf16 v[132:135], a[84:87], v[240:243], v[132:135]\n"
    "v_mfma_f32_16x16x32_bf16 v[136:139], a[84:87], v[228:231], v[136:139]\n"
    "v_mfma_f32_16x16x32_bf16 v[140:143], a[88:91], v[240:243], v[140:143]\n"
    "v_mfma_f32_16x16x32_bf16 v[144:147], a[88:91], v[228:231], v[144:147]\n"
    "v_mfma_f32_16x16x32_bf16 v[148:151], a[92:95], v[240:243], v[148:151]\n"
    "v_mfma_f32_16x16x32_bf16 v[152:155], a[92:95], v[228:231], v[152:155]\n"
    "s_add_u32 s81, s71, 172032\n"
    "buffer_load_dwordx4 v[240:243], v113, s[12:15], s81 offen offset:512 sc1\n"
    "buffer_load_dwordx4 v[244:247], v114, s[12:15], s81 offen offset:768 sc1\n"
    "s_waitcnt vmcnt(0)\n"
    "v_max3_u32 v239, v239, v244, v245\n"
    "v_max3_u32 v239, v239, v246, v247\n"
    "v_max3_u32 v239, v239, v240, v241\n"
    "v_max3_u32 v239, v239, v242, v243\n"
    "v_cndmask_b32 v228, 0, v244, s[4:5]\n"
    "v_cndmask_b32 v229, 0, v245, s[4:5]\n"
    "v_cndmask_b32 v230, 0, v246, s[4:5]\n"
    "v_cndmask_b32 v231, 0, v247, s[4:5]\n"
    "v_mfma_f32_16x16x32_bf16 v[124:127], a[96:99], v[240:243], v[124:127]\n"
    "s_nop 0\n"
    "v_mfma_f32_16x16x32_bf16 v[128:131], a[96:99], v[228:231], v[128:131]\n"
    "v_mfma_f32_16x16x32_bf16 v[132:135], a[100:103], v[240:243], v[132:135]\n"
    "v_mfma_f32_16x16x32_bf16 v[136:139], a[100:103], v[228:231], v[136:139]\n"
    "v_mfma_f32_16x16x32_bf16 v[140:143], a[104:107], v[240:243], v[140:143]\n"
    "v_mfma_f32_16x16x32_bf16 v[144:147], a[104:107], v[228:231], v[144:147]\n"
    "v_mfma_f32_16x16x32_bf16 v[148:151], a[108:111], v[240:243], v[148:151]\n"
    "v_mfma_f32_16x16x32_bf16 v[152:155], a[108:111], v[228:231], v[152:155]\n"
    "s_add_u32 s81, s71, 200704\n"
    "buffer_load_dwordx4 v[240:243], v113, s[12:15], s81 offen offset:512 sc1\n"
    "s_waitcnt vmcnt(0)\n"
    "v_max3_u32 v239, v239, v240, v241\n"
    "v_max3_u32 v239, v239, v242, v243\n"
    "v_cndmask_b32 v228, v244, 0, s[4:5]\n"
    "v_cndmask_b32 v229, v245, 0, s[4:5]\n"
    "v_cndmask_b32 v230, v246, 0, s[4:5]\n"
    "v_cndmask_b32 v231, v247, 0, s[4:5]\n"
    "v_mfma_f32_16x16x32_bf16 v[124:127], a[112:115], v[240:243], v[124:127]\n"
    "s_nop 0\n"
    "v_mfma_f32_16x16x32_bf16 v[128:131], a[112:115], v[228:231], v[128:131]\n"
    "v_mfma_f32_16x16x32_bf16 v[132:135], a[116:119], v[240:243], v[132:135]\n"
    "v_mfma_f32_16x16x32_bf16 v[136:139], a[116:119], v[228:231], v[136:139]\n"
    "v_mfma_f32_16x16x32_bf16 v[140:143], a[120:123], v[240:243], v[140:143]\n"
    "v_mfma_f32_16x16x32_bf16 v[144:147], a[120:123], v[228:231], v[144:147]\n"
    "v_mfma_f32_16x16x32_bf16 v[148:151], a[124:127], v[240:243], v[148:151]\n"
    "v_mfma_f32_16x16x32_bf16 v[152:155], a[124:127], v[228:231], v[152:155]\n"
    "s_add_u32 s81, s71, 229376\n"
    "buffer_load_dwordx4 v[240:243], v113, s[12:15], s81 offen offset:512 sc1\n"
    "buffer_load_dwordx4 v[244:247], v114, s[12:15], s81 offen offset:768 sc1\n"
    "s_waitcnt vmcnt(0)\n"
    "v_max3_u32 v239, v239, v244, v245\n"
    "v_max3_u32 v239, v239, v246, v247\n"
    "v_max3_u32 v239, v239, v240, v241\n"
    "v_max3_u32 v239, v239, v242, v243\n"
    "v_cndmask_b32 v228, 0, v244, s[4:5]\n"
    "v_cndmask_b32 v229, 0, v245, s[4:5]\n"
    "v_cndmask_b32 v230, 0, v246, s[4:5]\n"
    "v_cndmask_b32 v231, 0, v247, s[4:5]\n"
    "v_mfma_f32_16x16x32_bf16 v[124:127], a[128:131], v[240:243], v[124:127]\n"
    "s_nop 0\n"
    "v_mfma_f32_16x16x32_bf16 v[128:131], a[128:131], v[228:231], v[128:131]\n"
    "v_mfma_f32_16x16x32_bf16 v[132:135], a[132:135], v[240:243], v[132:135]\n"
    "v_mfma_f32_16x16x32_bf16 v[136:139], a[132:135], v[228:231], v[136:139]\n"
    "v_mfma_f32_16x16x32_bf16 v[140:143], a[136:139], v[240:243], v[140:143]\n"
    "v_mfma_f32_16x16x32_bf16 v[144:147], a[136:139], v[228:231], v[144:147]\n"
    "v_mfma_f32_16x16x32_bf16 v[148:151], a[140:143], v[240:243], v[148:151]\n"
    "v_mfma_f32_16x16x32_bf16 v[152:155], a[140:143], v[228:231], v[152:155]\n"
    "s_add_u32 s81, s71, 258048\n"
    "buffer_load_dwordx4 v[240:243], v113, s[12:15], s81 offen offset:512 sc1\n"
    "s_waitcnt vmcnt(0)\n"
    "v_max3_u32 v239, v239, v240, v241\n"
    "v_max3_u32 v239, v239, v242, v243\n"
    "v_cndmask_b32 v228, v244, 0, s[4:5]\n"
    "v_cndmask_b32 v229, v245, 0, s[4:5]\n"
    "v_cndmask_b32 v230, v246, 0, s[4:5]\n"
    "v_cndmask_b32 v231, v247, 0, s[4:5]\n"
    "v_mfma_f32_16x16x32_bf16 v[124:127], a[144:147], v[240:243], v[124:127]\n"
    "s_nop 0\n"
    "v_mfma_f32_16x16x32_bf16 v[128:131], a[144:147], v[228:231], v[128:131]\n"
    "v_mfma_f32_16x16x32_bf16 v[132:135], a[148:151], v[240:243], v[132:135]\n"
    "v_mfma_f32_16x16x32_bf16 v[136:139], a[148:151], v[228:231], v[136:139]\n"
    "v_mfma_f32_16x16x32_bf16 v[140:143], a[152:155], v[240:243], v[140:143]\n"
    "v_mfma_f32_16x16x32_bf16 v[144:147], a[152:155], v[228:231], v[144:147]\n"
    "v_mfma_f32_16x16x32_bf16 v[148:151], a[156:159], v[240:243], v[148:151]\n"
    "v_mfma_f32_16x16x32_bf16 v[152:155], a[156:159], v[228:231], v[152:155]\n"
    "s_add_u32 s81, s71, 286720\n"
    "buffer_load_dwordx4 v[240:243], v113, s[12:15], s81 offen offset:512 sc1\n"
    "buffer_load_dwordx4 v[244:247], v114, s[12:15], s81 offen offset:768 sc1\n"
    "s_waitcnt vmcnt(0)\n"
    "v_max3_u32 v239, v239, v244, v245\n"
    "v_max3_u32 v239, v239, v246, v247\n"
    "v_max3_u32 v239, v239, v240, v241\n"
    "v_max3_u32 v239, v239, v242, v243\n"
    "v_cndmask_b32 v228, 0, v244, s[4:5]\n"
    "v_cndmask_b32 v229, 0, v245, s[4:5]\n"
    "v_cndmask_b32 v230, 0, v246, s[4:5]\n"
    "v_cndmask_b32 v231, 0, v247, s[4:5]\n"
    "v_mfma_f32_16x16x32_bf16 v[124:127], a[160:163], v[240:243], v[124:127]\n"
    "s_nop 0\n"
    "v_mfma_f32_16x16x32_bf16 v[128:131], a[160:163], v[228:231], v[128:131]\n"
    "v_mfma_f32_16x16x32_bf16 v[132:135], a[164:167], v[240:243], v[132:135]\n"
    "v_mfma_f32_16x16x32_bf16 v[136:139], a[164:167], v[228:231], v[136:139]\n"
    "v_mfma_f32_16x16x32_bf16 v[140:143], a[168:171], v[240:243], v[140:143]\n"
    "v_mfma_f32_16x16x32_bf16 v[144:147], a[168:171], v[228:231], v[144:147]\n"
    "v_mfma_f32_16x16x32_bf16 v[148:151], a[172:175], v[240:243], v[148:151]\n"
    "v_mfma_f32_16x16x32_bf16 v[152:155], a[172:175], v[228:231], v[152:155]\n"
    "s_add_u32 s81, s71, 315392\n"
    "buffer_load_dwordx4 v[240:243], v113, s[12:15], s81 offen offset:512 sc1\n"
    "s_waitcnt vmcnt(0)\n"
    "v_max3_u32 v239, v239, v240, v241\n"
    "v_max3_u32 v239, v239, v242, v243\n"
    "v_cndmask_b32 v228, v244, 0, s[4:5]\n"
    "v_cndmask_b32 v229, v245, 0, s[4:5]\n"
    "v_cndmask_b32 v230, v246, 0, s[4:5]\n"
    "v_cndmask_b32 v231, v247, 0, s[4:5]\n"
    "v_mfma_f32_16x16x32_bf16 v[124:127], a[176:179], v[240:243], v[124:127]\n"
    "s_nop 0\n"
    "v_mfma_f32_16x16x32_bf16 v[128:131], a[176:179], v[228:231], v[128:131]\n"
    "v_mfma_f32_16x16x32_bf16 v[132:135], a[180:183], v[240:243], v[132:135]\n"
    "v_mfma_f32_16x16x32_bf16 v[136:139], a[180:183], v[228:231], v[136:139]\n"
    "v_mfma_f32_16x16x32_bf16 v[140:143], a[184:187], v[240:243], v[140:143]\n"
    "v_mfma_f32_16x16x32_bf16 v[144:147], a[184:187], v[228:231], v[144:147]\n"
    "v_mfma_f32_16x16x32_bf16 v[148:151], a[188:191], v[240:243], v[148:151]\n"
    "v_mfma_f32_16x16x32_bf16 v[152:155], a[188:191], v[228:231], v[152:155]\n"
    "s_add_u32 s81, s71, 344064\n"
    "buffer_load_dwordx4 v[240:243], v113, s[12:15], s81 offen offset:512 sc1\n"
    "buffer_load_dwordx4 v[244:247], v114, s[12:15], s81 offen offset:768 sc1\n"
    "s_waitcnt vmcnt(0)\n"
    "v_max3_u32 v239, v239, v244, v245\n"
    "v_max3_u32 v239, v239, v246, v247\n"
    "v_max3_u32 v239, v239, v240, v241\n"
    "v_max3_u32 v239, v239, v242, v243\n"
    "v_cndmask_b32 v228, 0, v244, s[4:5]\n"
    "v_cndmask_b32 v229, 0, v245, s[4:5]\n"
    "v_cndmask_b32 v230, 0, v246, s[4:5]\n"
    "v_cndmask_b32 v231, 0, v247, s[4:5]\n"
    "v_mfma_f32_16x16x32_bf16 v[124:127], a[192:195], v[240:243], v[124:127]\n"
    "s_nop 0\n"
    "v_mfma_f32_16x16x32_bf16 v[128:131], a[192:195], v[228:231], v[128:131]\n"
    "v_mfma_f32_16x16x32_bf16 v[132:135], a[196:199], v[240:243], v[132:135]\n"
    "v_mfma_f32_16x16x32_bf16 v[136:139], a[196:199], v[228:231], v[136:139]\n"
    "v_mfma_f32_16x16x32_bf16 v[140:143], a[200:203], v[240:243], v[140:143]\n"
    "v_mfma_f32_16x16x32_bf16 v[144:147], a[200:203], v[228:231], v[144:147]\n"
    "v_mfma_f32_16x16x32_bf16 v[148:151], a[204:207], v[240:243], v[148:151]\n"
    "v_mfma_f32_16x16x32_bf16 v[152:155], a[204:207], v[228:231], v[152:155]\n"
    "s_add_u32 s81, s71, 372736\n"
    "buffer_load_dwordx4 v[240:243], v113, s[12:15], s81 offen offset:512 sc1\n"
    "s_waitcnt vmcnt(0)\n"
    "v_max3_u32 v239, v239, v240, v241\n"
    "v_max3_u32 v239, v239, v242, v243\n"
    "v_cndmask_b32 v228, v244, 0, s[4:5]\n"
    "v_cndmask_b32 v229, v245, 0, s[4:5]\n"
    "v_cndmask_b32 v230, v246, 0, s[4:5]\n"
    "v_cndmask_b32 v231, v247, 0, s[4:5]\n"
    "v_mfma_f32_16x16x32_bf16 v[124:127], a[208:211], v[240:243], v[124:127]\n"
    "s_nop 0\n"
    "v_mfma_f32_16x16x32_bf16 v[128:131], a[208:211], v[228:231], v[128:131]\n"
    "v_mfma_f32_16x16x32_bf16 v[132:135], a[212:215], v[240:243], v[132:135]\n"
    "v_mfma_f32_16x16x32_bf16 v[136:139], a[212:215], v[228:231], v[136:139]\n"
    "v_mfma_f32_16x16x32_bf16 v[140:143], a[216:219], v[240:243], v[140:143]\n"
    "v_mfma_f32_16x16x32_bf16 v[144:147], a[216:219], v[228:231], v[144:147]\n"
    "v_mfma_f32_16x16x32_bf16 v[148:151], a[220:223], v[240:243], v[148:151]\n"
    "v_mfma_f32_16x16x32_bf16 v[152:155], a[220:223], v[228:231], v[152:155]\n"
    "s_add_u32 s81, s71, 401408\n"
    "buffer_load_dwordx4 v[240:243], v113, s[12:15], s81 offen offset:512 sc1\n"
    "buffer_load_dwordx4 v[244:247], v114, s[12:15], s81 offen offset:768 sc1\n"
    "s_waitcnt vmcnt(0)\n"
    "v_max3_u32 v239, v239, v244, v245\n"
    "v_max3_u32 v239, v239, v246, v247\n"
    "v_max3_u32 v239, v239, v240, v241\n"
    "v_max3_u32 v239, v239, v242, v243\n"
    "v_cndmask_b32 v228, 0, v244, s[4:5]\n"
    "v_cndmask_b32 v229, 0, v245, s[4:5]\n"
    "v_cndmask_b32 v230, 0, v246, s[4:5]\n"
    "v_cndmask_b32 v231, 0, v247, s[4:5]\n"
    "v_mfma_f32_16x16x32_bf16 v[124:127], a[224:227], v[240:243], v[124:127]\n"
    "s_nop 0\n"
    "v_mfma_f32_16x16x32_bf16 v[128:131], a[224:227], v[228:231], v[128:131]\n"
    "v_mfma_f32_16x16x32_bf16 v[132:135], a[228:231], v[240:243], v[132:135]\n"
    "v_mfma_f32_16x16x32_bf16 v[136:139], a[228:231], v[228:231], v[136:139]\n"
    "v_mfma_f32_16x16x32_bf16 v[140:143], a[232:235], v[240:243], v[140:143]\n"
    "v_mfma_f32_16x16x32_bf16 v[144:147], a[232:235], v[228:231], v[144:147]\n"
    "v_mfma_f32_16x16x32_bf16 v[148:151], a[236:239], v[240:243], v[148:151]\n"
    "v_mfma_f32_16x16x32_bf16 v[152:155], a[236:239], v[228:231], v[152:155]\n"
    "s_add_u32 s81, s71, 430080\n"
    "buffer_load_dwordx4 v[240:243], v113, s[12:15], s81 offen offset:512 sc1\n"
    "s_waitcnt vmcnt(0)\n"
    "v_max3_u32 v239, v239, v240, v241\n"
    "v_max3_u32 v239, v239, v242, v243\n"
    "v_cndmask_b32 v228, v244, 0, s[4:5]\n"
    "v_cndmask_b32 v229, v245, 0, s[4:5]\n"
    "v_cndmask_b32 v230, v246, 0, s[4:5]\n"
    "v_cndmask_b32 v231, v247, 0, s[4:5]\n"
    "v_mfma_f32_16x16x32_bf16 v[124:127], a[240:243], v[240:243], v[124:127]\n"
    "s_nop 0\n"
    "v_mfma_f32_16x16x32_bf16 v[128:131], a[240:243], v[228:231], v[128:131]\n"
    "v_mfma_f32_16x16x32_bf16 v[132:135], a[244:247], v[240:243], v[132:135]\n"
    "v_mfma_f32_16x16x32_bf16 v[136:139], a[244:247], v[228:231], v[136:139]\n"
    "v_mfma_f32_16x16x32_bf16 v[140:143], a[248:251], v[240:243], v[140:143]\n"
    "v_mfma_f32_16x16x32_bf16 v[144:147], a[248:251], v[228:231], v[144:147]\n"
    "v_mfma_f32_16x16x32_bf16 v[148:151], a[252:255], v[240:243], v[148:151]\n"
    "v_mfma_f32_16x16x32_bf16 v[152:155], a[252:255], v[228:231], v[152:155]\n"
    "v_cmp_eq_u32 vcc, -1, v239\n"
    "s_cbranch_vccnz .Lredo_19\n"
    ".Lredook_20:\n"
    "s_waitcnt vmcnt(0) lgkmcnt(0)\n"
    "s_branch .Lback_18\n"
    ".Lredo_19:\n"
    "s_waitcnt vmcnt(0) lgkmcnt(0)\n"
    "s_sub_u32 s60, s60, 1\n"
    "s_cbranch_scc0 .Lclean_17\n"
    "s_mov_b32 s60, 0\n"
    "s_branch .Lredook_20\n"
    ".Lredo_21:\n"
    "s_waitcnt vmcnt(0) lgkmcnt(0)\n"
    "s_branch .Lclean_17\n"
    ".Lgretry_23:\n"
    "s_sub_u32 s60, s60, 1\n"
    "s_cbranch_scc1 .Lgretry_23_giveup\n"
    "buffer_load_dword v251, v250, s[12:15], s82 offen sc1\n"
    "s_waitcnt vmcnt(0)\n"
    "v_cmp_eq_u32 vcc, -1, v251\n"
    "s_cbranch_vccnz .Lgretry_23\n"
    "s_add_u32 s81, s82, 0\n"
    "buffer_load_dwordx4 v[220:223], v113, s[12:15], s81 offen offset:512 sc1\n"
    "s_add_u32 s81, s82, 0\n"
    "buffer_load_dwordx4 v[212:215], v114, s[12:15], s81 offen offset:768 sc1\n"
    "s_add_u32 s81, s82, 28672\n"
    "buffer_load_dwordx4 v[216:219], v113, s[12:15], s81 offen offset:512 sc1\n"
    "s_add_u32 s81, s82, 57344\n"
    "buffer_load_dwordx4 v[196:199], v113, s[12:15], s81 offen offset:512 sc1\n"
    "s_add_u32 s81, s82, 57344\n"
    "buffer_load_dwordx4 v[224:227], v114, s[12:15], s81 offen offset:768 sc1\n"
    "s_add_u32 s81, s82, 86016\n"
    "buffer_load_dwordx4 v[188:191], v113, s[12:15], s81 offen offset:512 sc1\n"
    "s_add_u32 s81, s82, 114688\n"
    "buffer_load_dwordx4 v[208:211], v113, s[12:15], s81 offen offset:512 sc1\n"
    "s_add_u32 s81, s82, 114688\n"
    "buffer_load_dwordx4 v[192:195], v114, s[12:15], s81 offen offset:768 sc1\n"
    "s_add_u32 s81, s82, 143360\n"
    "buffer_load_dwordx4 v[200:203], v113, s[12:15], s81 offen offset:512 sc1\n"
    "s_waitcnt vmcnt(0)\n"
    "s_branch .Lgok_24\n"
    ".Lgretry_23_giveup:\n"
    "s_mov_b32 s60, 0\n"
    "s_waitcnt vmcnt(0)\n"
    "s_branch .Lgok_24\n"
    ".Lclean_26:\n"
    "v_mov_b32 v239, 0\n"
    "s_add_u32 s81, s71, 0\n"
    "buffer_load_dwordx4 v[240:243], v113, s[12:15], s81 offen offset:0 sc1\n"
    "buffer_load_dwordx4 v[244:247], v113, s[12:15], s81 offen offset:256 sc1\n"
    "s_waitcnt vmcnt(0)\n"
    "v_max3_u32 v239, v239, v244, v245\n"
    "v_max3_u32 v239, v239, v246, v247\n"
    "v_max3_u32 v239, v239, v240, v241\n"
    "v_max3_u32 v239, v239, v242, v243\n"
    "v_mfma_f32_16x16x32_bf16 v[156:159], a[0:3], v[240:243], 0\n"
    "v_mfma_f32_16x16x32_bf16 v[160:163], a[0:3], v[244:247], 0\n"
    "v_mfma_f32_16x16x32_bf16 v[164:167], a[4:7], v[240:243], 0\n"
    "v_mfma_f32_16x16x32_bf16 v[168:171], a[4:7], v[244:247], 0\n"
    "v_mfma_f32_16x16x32_bf16 v[172:175], a[8:11], v[240:243], 0\n"
    "v_mfma_f32_16x16x32_bf16 v[176:179], a[8:11], v[244:247], 0\n"
    "v_mfma_f32_16x16x32_bf16 v[180:183], a[12:15], v[240:243], 0\n"
    "v_mfma_f32_16x16x32_bf16 v[184:187], a[12:15], v[244:247], 0\n"
    "s_add_u32 s81, s71, 28672\n"
    "buffer_load_dwordx4 v[240:243], v113, s[12:15], s81 offen offset:0 sc1\n"
    "buffer_load_dwordx4 v[244:247], v113, s[12:15], s81 offen offset:256 sc1\n"
    "s_waitcnt vmcnt(0)\n"
    "v_max3_u32 v239, v239, v244, v245\n"
    "v_max3_u32 v239, v239, v246, v247\n"
    "v_max3_u32 v239, v239, v240, v241\n"
    "v_max3_u32 v239, v239, v242, v243\n"
    "v_mfma_f32_16x16x32_bf16 v[156:159], a[16:19], v[240:243], v[156:159]\n"
    "v_mfma_f32_16x16x32_bf16 v[160:163], a[16:19], v[244:247], v[160:163]\n"
    "v_mfma_f32_16x16x32_bf16 v[164:167], a[20:23], v[240:243], v[164:167]\n"
    "v_mfma_f32_16x16x32_bf16 v[168:171], a[20:23], v[244:247], v[168:171]\n"
    "v_mfma_f32_16x16x32_bf16 v[172:175], a[24:27], v[240:243], v[172:175]\n"
    "v_mfma_f32_16x16x32_bf16 v[176:179], a[24:27], v[244:247], v[176:179]\n"
    "v_mfma_f32_16x16x32_bf16 v[180:183], a[28:31], v[240:243], v[180:183]\n"
    "v_mfma_f32_16x16x32_bf16 v[184:187], a[28:31], v[244:247], v[184:187]\n"
    "s_add_u32 s81, s71, 57344\n"
    "buffer_load_dwordx4 v[240:243], v113, s[12:15], s81 offen offset:0 sc1\n"
    "buffer_load_dwordx4 v[244:247], v113, s[12:15], s81 offen offset:256 sc1\n"
    "s_waitcnt vmcnt(0)\n"
    "v_max3_u32 v239, v239, v244, v245\n"
    "v_max3_u32 v239, v239, v246, v247\n"
    "v_max3_u32 v239, v239, v240, v241\n"
    "v_max3_u32 v239, v239, v242, v243\n"
    "v_mfma_f32_16x16x32_bf16 v[156:159], a[32:35], v[240:243], v[156:159]\n"
    "v_mfma_f32_16x16x32_bf16 v[160:163], a[32:35], v[244:247], v[160:163]\n"
    "v_mfma_f32_16x16x32_bf16 v[164:167], a[36:39], v[240:243], v[164:167]\n"
    "v_mfma_f32_16x16x32_bf16 v[168:171], a[36:39], v[244:247], v[168:171]\n"
    "v_mfma_f32_16x16x32_bf16 v[172:175], a[40:43], v[240:243], v[172:175]\n"
    "v_mfma_f32_16x16x32_bf16 v[176:179], a[40:43], v[244:247], v[176:179]\n"
    "v_mfma_f32_16x16x32_bf16 v[180:183], a[44:47], v[240:243], v[180:183]\n"
    "v_mfma_f32_16x16x32_bf16 v[184:187], a[44:47], v[244:247], v[184:187]\n"
    "s_add_u32 s81, s71, 86016\n"
    "buffer_load_dwordx4 v[240:243], v113, s[12:15], s81 offen offset:0 sc1\n"
    "buffer_load_dwordx4 v[244:247], v113, s[12:15], s81 offen offset:256 sc1\n"
    "s_waitcnt vmcnt(0)\n"
    "v_max3_u32 v239, v239, v244, v245\n"
    "v_max3_u32 v239, v239, v246, v247\n"
    "v_max3_u32 v239, v239, v240, v241\n"
    "v_max3_u32 v239, v239, v242, v243\n"
    "v_mfma_f32_16x16x32_bf16 v[156:159], a[48:51], v[240:243], v[156:159]\n"
    "v_mfma_f32_16x16x32_bf16 v[160:163], a[48:51], v[244:247], v[160:163]\n"
    "v_mfma_f32_16x16x32_bf16 v[164:167], a[52:55], v[240:243], v[164:167]\n"
    "v_mfma_f32_16x16x32_bf16 v[168:171], a[52:55], v[244:247], v[168:171]\n"
    "v_mfma_f32_16x16x32_bf16 v[172:175], a[56:59], v[240:243], v[172:175]\n"
    "v_mfma_f32_16x16x32_bf16 v[176:179], a[56:59], v[244:247], v[176:179]\n"
    "v_mfma_f32_16x16x32_bf16 v[180:183], a[60:63], v[240:243], v[180:183]\n"
    "v_mfma_f32_16x16x32_bf16 v[184:187], a[60:63], v[244:247], v[184:187]\n"
    "s_add_u32 s81, s71, 114688\n"
    "buffer_load_dwordx4 v[240:243], v113, s[12:15], s81 offen offset:0 sc1\n"
    "buffer_load_dwordx4 v[244:247], v113, s[12:15], s81 offen offset:256 sc1\n"
    "s_waitcnt vmcnt(0)\n"
    "v_max3_u32 v239, v239, v244, v245\n"
    "v_max3_u32 v239, v239, v246, v247\n"
    "v_max3_u32 v239, v239, v240, v241\n"
    "v_max3_u32 v239, v239, v242, v243\n"
    "v_mfma_f32_16x16x32_bf16 v[156:159], a[64:67], v[240:243], v[156:159]\n"
    "v_mfma_f32_16x16x32_bf16 v[160:163], a[64:67], v[244:247], v[160:163]\n"
    "v_mfma_f32_16x16x32_bf16 v[164:167], a[68:71], v[240:243], v[164:167]\n"
    "v_mfma_f32_16x16x32_bf16 v[168:171], a[68:71], v[244:247], v[168:171]\n"
    "v_mfma_f32_16x16x32_bf16 v[172:175], a[72:75], v[240:243], v[172:175]\n"
    "v_mfma_f32_16x16x32_bf16 v[176:179], a[72:75], v[244:247], v[176:179]\n"
    "v_mfma_f32_16x16x32_bf16 v[180:183], a[76:79], v[240:243], v[180:183]\n"
    "v_mfma_f32_16x16x32_bf16 v[184:187], a[76:79], v[244:247], v[184:187]\n"
    "s_add_u32 s81, s71, 143360\n"
    "buffer_load_dwordx4 v[240:243], v113, s[12:15], s81 offen offset:0 sc1\n"
    "buffer_load_dwordx4 v[244:247], v113, s[12:15], s81 offen offset:256 sc1\n"
    "s_waitcnt vmcnt(0)\n"
    "v_max3_u32 v239, v239, v244, v245\n"
    "v_max3_u32 v239, v239, v246, v247\n"
    "v_max3_u32 v239, v239, v240, v241\n"
    "v_max3_u32 v239, v239, v242, v243\n"
    "v_mfma_f32_16x16x32_bf16 v[156:159], a[80:83], v[240:243], v[156:159]\n"
    "v_mfma_f32_16x16x32_bf16 v[160:163], a[80:83], v[244:247], v[160:163]\n"
    "v_mfma_f32_16x16x32_bf16 v[164:167], a[84:87], v[240:243], v[164:167]\n"
    "v_mfma_f32_16x16x32_bf16 v[168:171], a[84:87], v[244:247], v[168:171]\n"
    "v_mfma_f32_16x16x32_bf16 v[172:175], a[88:91], v[240:243], v[172:175]\n"
    "v_mfma_f32_16x16x32_bf16 v[176:179], a[88:91], v[244:247], v[176:179]\n"
    "v_mfma_f32_16x16x32_bf16 v[180:183], a[92:95], v[240:243], v[180:183]\n"
    "v_mfma_f32_16x16x32_bf16 v[184:187], a[92:95], v[244:247], v[184:187]\n"
    "s_add_u32 s81, s71, 172032\n"
    "buffer_load_dwordx4 v[240:243], v113, s[12:15], s81 offen offset:0 sc1\n"
    "buffer_load_dwordx4 v[244:247], v113, s[12:15], s81 offen offset:256 sc1\n"
    "s_waitcnt vmcnt(0)\n"
    "v_max3_u32 v239, v239, v244, v245\n"
    "v_max3_u32 v239, v239, v246, v247\n"
    "v_max3_u32 v239, v239, v240, v241\n"
    "v_max3_u32 v239, v239, v242, v243\n"
    "v_mfma_f32_16x16x32_bf16 v[156:159], a[96:99], v[240:243], v[156:159]\n"
    "v_mfma_f32_16x16x32_bf16 v[160:163], a[96:99], v[244:247], v[160:163]\n"
    "v_mfma_f32_16x16x32_bf16 v[164:167], a[100:103], v[240:243], v[164:167]\n"
    "v_mfma_f32_16x16x32_bf16 v[168:171], a[100:103], v[244:247], v[168:171]\n"
    "v_mfma_f32_16x16x32_bf16 v[172:175], a[104:107], v[240:243], v[172:175]\n"
    "v_mfma_f32_16x16x32_bf16 v[176:179], a[104:107], v[244:247], v[176:179]\n"
    "v_mfma_f32_16x16x32_bf16 v[180:183], a[108:111], v[240:243], v[180:183]\n"
    "v_mfma_f32_16x16x32_bf16 v[184:187], a[108:111], v[244:247], v[184:187]\n"
    "s_add_u32 s81, s71, 200704\n"
    "buffer_load_dwordx4 v[240:243], v113, s[12:15], s81 offen offset:0 sc1\n"
    "buffer_load_dwordx4 v[244:247], v113, s[12:15], s81 offen offset:256 sc1\n"
    "s_waitcnt vmcnt(0)\n"
    "v_max3_u32 v239, v239, v244, v245\n"
    "v_max3_u32 v239, v239, v246, v247\n"
    "v_max3_u32 v239, v239, v240, v241\n"
    "v_max3_u32 v239, v239, v242, v243\n"
    "v_mfma_f32_16x16x32_bf16 v[156:159], a[112:115], v[240:243], v[156:159]\n"
    "v_mfma_f32_16x16x32_bf16 v[160:163], a[112:115], v[244:247], v[160:163]\n"
    "v_mfma_f32_16x16x32_bf16 v[164:167], a[116:119], v[240:243], v[164:167]\n"
    "v_mfma_f32_16x16x32_bf16 v[168:171], a[116:119], v[244:247], v[168:171]\n"
    "v_mfma_f32_16x16x32_bf16 v[172:175], a[120:123], v[240:243], v[172:175]\n"
    "v_mfma_f32_16x16x32_bf16 v[176:179], a[120:123], v[244:247], v[176:179]\n"
    "v_mfma_f32_16x16x32_bf16 v[180:183], a[124:127], v[240:243], v[180:183]\n"
    "v_mfma_f32_16x16x32_bf16 v[184:187], a[124:127], v[244:247], v[184:187]\n"
    "s_add_u32 s81, s71, 229376\n"
    "buffer_load_dwordx4 v[240:243], v113, s[12:15], s81 offen offset:0 sc1\n"
    "buffer_load_dwordx4 v[244:247], v113, s[12:15], s81 offen offset:256 sc1\n"
    "s_waitcnt vmcnt(0)\n"
    "v_max3_u32 v239, v239, v244, v245\n"
    "v_max3_u32 v239, v239, v246, v247\n"
    "v_max3_u32 v239, v239, v240, v241\n"
    "v_max3_u32 v239, v239, v242, v243\n"
    "v_mfma_f32_16x16x32_bf16 v[156:159], a[128:131], v[240:243], v[156:159]\n"
    "v_mfma_f32_16x16x32_bf16 v[160:163], a[128:131], v[244:247], v[160:163]\n"
    "v_mfma_f32_16x16x32_bf16 v[164:167], a[132:135], v[240:243], v[164:167]\n"
    "v_mfma_f32_16x16x32_bf16 v[168:171], a[132:135], v[244:247], v[168:171]\n"
    "v_mfma_f32_16x16x32_bf16 v[172:175], a[136:139], v[240:243], v[172:175]\n"
    "v_mfma_f32_16x16x32_bf16 v[176:179], a[136:139], v[244:247], v[176:179]\n"
    "v_mfma_f32_16x16x32_bf16 v[180:183], a[140:143], v[240:243], v[180:183]\n"
    "v_mfma_f32_16x16x32_bf16 v[184:187], a[140:143], v[244:247], v[184:187]\n"
    "s_add_u32 s81, s71, 258048\n"
    "buffer_load_dwordx4 v[240:243], v113, s[12:15], s81 offen offset:0 sc1\n"
    "buffer_load_dwordx4 v[244:247], v113, s[12:15], s81 offen offset:256 sc1\n"
    "s_waitcnt vmcnt(0)\n"
    "v_max3_u32 v239, v239, v244, v245\n"
    "v_max3_u32 v239, v239, v246, v247\n"
    "v_max3_u32 v239, v239, v240, v241\n"
    "v_max3_u32 v239, v239, v242, v243\n"
    "v_mfma_f32_16x16x32_bf16 v[156:159], a[144:147], v[240:243], v[156:159]\n"
    "v_mfma_f32_16x16x32_bf16 v[160:163], a[144:147], v[244:247], v[160:163]\n"
    "v_mfma_f32_16x16x32_bf16 v[164:167], a[148:151], v[240:243], v[164:167]\n"
    "v_mfma_f32_16x16x32_bf16 v[168:171], a[148:151], v[244:247], v[168:171]\n"
    "v_mfma_f32_16x16x32_bf16 v[172:175], a[152:155], v[240:243], v[172:175]\n"
    "v_mfma_f32_16x16x32_bf16 v[176:179], a[152:155], v[244:247], v[176:179]\n"
    "v_mfma_f32_16x16x32_bf16 v[180:183], a[156:159], v[240:243], v[180:183]\n"
    "v_mfma_f32_16x16x32_bf16 v[184:187], a[156:159], v[244:247], v[184:187]\n"
    "s_add_u32 s81, s71, 286720\n"
    "buffer_load_dwordx4 v[240:243], v113, s[12:15], s81 offen offset:0 sc1\n"
    "buffer_load_dwordx4 v[244:247], v113, s[12:15], s81 offen offset:256 sc1\n"
    "s_waitcnt vmcnt(0)\n"
    "v_max3_u32 v239, v239, v244, v245\n"
    "v_max3_u32 v239, v239, v246, v247\n"
    "v_max3_u32 v239, v239, v240, v241\n"
    "v_max3_u32 v239, v239, v242, v243\n"
    "v_mfma_f32_16x16x32_bf16 v[156:159], a[160:163], v[240:243], v[156:159]\n"
    "v_mfma_f32_16x16x32_bf16 v[160:163], a[160:163], v[244:247], v[160:163]\n"
    "v_mfma_f32_16x16x32_bf16 v[164:167], a[164:167], v[240:243], v[164:167]\n"
    "v_mfma_f32_16x16x32_bf16 v[168:171], a[164:167], v[244:247], v[168:171]\n"
    "v_mfma_f32_16x16x32_bf16 v[172:175], a[168:171], v[240:243], v[172:175]\n"
    "v_mfma_f32_16x16x32_bf16 v[176:179], a[168:171], v[244:247], v[176:179]\n"
    "v_mfma_f32_16x16x32_bf16 v[180:183], a[172:175], v[240:243], v[180:183]\n"
    "v_mfma_f32_16x16x32_bf16 v[184:187], a[172:175], v[244:247], v[184:187]\n"
    "s_add_u32 s81, s71, 315392\n"
    "buffer_load_dwordx4 v[240:243], v113, s[12:15], s81 offen offset:0 sc1\n"
    "buffer_load_dwordx4 v[244:247], v113, s[12:15], s81 offen offset:256 sc1\n"
    "s_waitcnt vmcnt(0)\n"
    "v_max3_u32 v239, v239, v244, v245\n"
    "v_max3_u32 v239, v239, v246, v247\n"
    "v_max3_u32 v239, v239, v240, v241\n"
    "v_max3_u32 v239, v239, v242, v243\n"
    "v_mfma_f32_16x16x32_bf16 v[156:159], a[176:179], v[240:243], v[156:159]\n"
    "v_mfma_f32_16x16x32_bf16 v[160:163], a[176:179], v[244:247], v[160:163]\n"
    "v_mfma_f32_16x16x32_bf16 v[164:167], a[180:183], v[240:243], v[164:167]\n"
    "v_mfma_f32_16x16x32_bf16 v[168:171], a[180:183], v[244:247], v[168:171]\n"
    "v_mfma_f32_16x16x32_bf16 v[172:175], a[184:187], v[240:243], v[172:175]\n"
    "v_mfma_f32_16x16x32_bf16 v[176:179], a[184:187], v[244:247], v[176:179]\n"
    "v_mfma_f32_16x16x32_bf16 v[180:183], a[188:191], v[240:243], v[180:183]\n"
    "v_mfma_f32_16x16x32_bf16 v[184:187], a[188:191], v[244:247], v[184:187]\n"
    "s_add_u32 s81, s71, 344064\n"
    "buffer_load_dwordx4 v[240:243], v113, s[12:15], s81 offen offset:0 sc1\n"
    "buffer_load_dwordx4 v[244:247], v113, s[12:15], s81 offen offset:256 sc1\n"
    "s_waitcnt vmcnt(0)\n"
    "v_max3_u32 v239, v239, v244, v245\n"
    "v_max3_u32 v239, v239, v246, v247\n"
    "v_max3_u32 v239, v239, v240, v241\n"
    "v_max3_u32 v239, v239, v242, v243\n"
    "v_mfma_f32_16x16x32_bf16 v[156:159], a[192:195], v[240:243], v[156:159]\n"
    "v_mfma_f32_16x16x32_bf16 v[160:163], a[192:195], v[244:247], v[160:163]\n"
    "v_mfma_f32_16x16x32_bf16 v[164:167], a[196:199], v[240:243], v[164:167]\n"
    "v_mfma_f32_16x16x32_bf16 v[168:171], a[196:199], v[244:247], v[168:171]\n"
    "v_mfma_f32_16x16x32_bf16 v[172:175], a[200:203], v[240:243], v[172:175]\n"
    "v_mfma_f32_16x16x32_bf16 v[176:179], a[200:203], v[244:247], v[176:179]\n"
    "v_mfma_f32_16x16x32_bf16 v[180:183], a[204:207], v[240:243], v[180:183]\n"
    "v_mfma_f32_16x16x32_bf16 v[184:187], a[204:207], v[244:247], v[184:187]\n"
    "s_add_u32 s81, s71, 372736\n"
    "buffer_load_dwordx4 v[240:243], v113, s[12:15], s81 offen offset:0 sc1\n"
    "buffer_load_dwordx4 v[244:247], v113, s[12:15], s81 offen offset:256 sc1\n"
    "s_waitcnt vmcnt(0)\n"
    "v_max3_u32 v239, v239, v244, v245\n"
    "v_max3_u32 v239, v239, v246, v247\n"
    "v_max3_u32 v239, v239, v240, v241\n"
    "v_max3_u32 v239, v239, v242, v243\n"
    "v_mfma_f32_16x16x32_bf16 v[156:159], a[208:211], v[240:243], v[156:159]\n"
    "v_mfma_f32_16x16x32_bf16 v[160:163], a[208:211], v[244:247], v[160:163]\n"
    "v_mfma_f32_16x16x32_bf16 v[164:167], a[212:215], v[240:243], v[164:167]\n"
    "v_mfma_f32_16x16x32_bf16 v[168:171], a[212:215], v[244:247], v[168:171]\n"
    "v_mfma_f32_16x16x32_bf16 v[172:175], a[216:219], v[240:243], v[172:175]\n"
    "v_mfma_f32_16x16x32_bf16 v[176:179], a[216:219], v[244:247], v[176:179]\n"
    "v_mfma_f32_16x16x32_bf16 v[180:183], a[220:223], v[240:243], v[180:183]\n"
    "v_mfma_f32_16x16x32_bf16 v[184:187], a[220:223], v[244:247], v[184:187]\n"
    "s_add_u32 s81, s71, 401408\n"
    "buffer_load_dwordx4 v[240:243], v113, s[12:15], s81 offen offset:0 sc1\n"
    "buffer_load_dwordx4 v[244:247], v113, s[12:15], s81 offen offset:256 sc1\n"
    "s_waitcnt vmcnt(0)\n"
    "v_max3_u32 v239, v239, v244, v245\n"
    "v_max3_u32 v239, v239, v246, v247\n"
    "v_max3_u32 v239, v239, v240, v241\n"
    "v_max3_u32 v239, v239, v242, v243\n"
    "v_mfma_f32_16x16x32_bf16 v[156:159], a[224:227], v[240:243], v[156:159]\n"
    "v_mfma_f32_16x16x32_bf16 v[160:163], a[224:227], v[244:247], v[160:163]\n"
    "v_mfma_f32_16x16x32_bf16 v[164:167], a[228:231], v[240:243], v[164:167]\n"
    "v_mfma_f32_16x16x32_bf16 v[168:171], a[228:231], v[244:247], v[168:171]\n"
    "v_mfma_f32_16x16x32_bf16 v[172:175], a[232:235], v[240:243], v[172:175]\n"
    "v_mfma_f32_16x16x32_bf16 v[176:179], a[232:235], v[244:247], v[176:179]\n"
    "v_mfma_f32_16x16x32_bf16 v[180:183], a[236:239], v[240:243], v[180:183]\n"
    "v_mfma_f32_16x16x32_bf16 v[184:187], a[236:239], v[244:247], v[184:187]\n"
    "s_add_u32 s81, s71, 430080\n"
    "buffer_load_dwordx4 v[240:243], v113, s[12:15], s81 offen offset:0 sc1\n"
    "buffer_load_dwordx4 v[244:247], v113, s[12:15], s81 offen offset:256 sc1\n"
    "s_waitcnt vmcnt(0)\n"
    "v_max3_u32 v239, v239, v244, v245\n"
    "v_max3_u32 v239, v239, v246, v247\n"
    "v_max3_u32 v239, v239, v240, v241\n"
    "v_max3_u32 v239, v239, v242, v243\n"
    "v_mfma_f32_16x16x32_bf16 v[156:159], a[240:243], v[240:243], v[156:159]\n"
    "v_mfma_f32_16x16x32_bf16 v[160:163], a[240:243], v[244:247], v[160:163]\n"
    "v_mfma_f32_16x16x32_bf16 v[164:167], a[244:247], v[240:243], v[164:167]\n"
    "v_mfma_f32_16x16x32_bf16 v[168:171], a[244:247], v[244:247], v[168:171]\n"
    "v_mfma_f32_16x16x32_bf16 v[172:175], a[248:251], v[240:243], v[172:175]\n"
    "v_mfma_f32_16x16x32_bf16 v[176:179], a[248:251], v[244:247], v[176:179]\n"
    "v_mfma_f32_16x16x32_bf16 v[180:183], a[252:255], v[240:243], v[180:183]\n"
    "v_mfma_f32_16x16x32_bf16 v[184:187], a[252:255], v[244:247], v[184:187]\n"
    "v_cmp_eq_u32 vcc, -1, v239\n"
    "s_cbranch_vccnz .Lredo_28\n"
    ".Lredook_29:\n"
    "s_waitcnt vmcnt(0) lgkmcnt(0)\n"
    "s_branch .Lback_27\n"
    ".Lredo_28:\n"
    "s_waitcnt vmcnt(0) lgkmcnt(0)\n"
    "s_sub_u32 s60, s60, 1\n"
    "s_cbranch_scc0 .Lclean_26\n"
    "s_mov_b32 s60, 0\n"
    "s_branch .Lredook_29\n"
    ".Lredo_30:\n"
    "s_waitcnt vmcnt(0) lgkmcnt(0)\n"
    "s_branch .Lclean_26\n"
    ".Lgretry_33:\n"
    "s_sub_u32 s60, s60, 1\n"
    "s_cbranch_scc1 .Lgretry_33_giveup\n"
    "buffer_load_dword v186, v185, s[12:15], s82 offen sc1\n"
    "s_waitcnt vmcnt(0)\n"
    "v_cmp_eq_u32 vcc, -1, v186\n"
    "s_cbranch_vccnz .Lgretry_33\n"
    "s_add_u32 s81, s82, 0\n"
    "buffer_load_dwordx4 v[220:223], v113, s[12:15], s81 offen offset:0 sc1\n"
    "s_add_u32 s81, s82, 0\n"
    "buffer_load_dwordx4 v[212:215], v113, s[12:15], s81 offen offset:256 sc1\n"
    "s_add_u32 s81, s82, 28672\n"
    "buffer_load_dwordx4 v[188:191], v113, s[12:15], s81 offen offset:0 sc1\n"
    "s_add_u32 s81, s82, 28672\n"
    "buffer_load_dwordx4 v[196:199], v113, s[12:15], s81 offen offset:256 sc1\n"
    "s_add_u32 s81, s82, 57344\n"
    "buffer_load_dwordx4 v[224:227], v113, s[12:15], s81 offen offset:0 sc1\n"
    "s_add_u32 s81, s82, 57344\n"
    "buffer_load_dwordx4 v[200:203], v113, s[12:15], s81 offen offset:256 sc1\n"
    "s_add_u32 s81, s82, 86016\n"
    "buffer_load_dwordx4 v[208:211], v113, s[12:15], s81 offen offset:0 sc1\n"
    "s_add_u32 s81, s82, 86016\n"
    "buffer_load_dwordx4 v[192:195], v113, s[12:15], s81 offen offset:256 sc1\n"
    "s_add_u32 s81, s82, 114688\n"
    "buffer_load_dwordx4 v[216:219], v113, s[12:15], s81 offen offset:0 sc1\n"
    "s_add_u32 s81, s82, 114688\n"
    "buffer_load_dwordx4 v[204:207], v113, s[12:15], s81 offen offset:256 sc1\n"
    "s_waitcnt vmcnt(0)\n"
    "s_branch .Lgok_34\n"
    ".Lgretry_33_giveup:\n"
    "s_mov_b32 s60, 0\n"
    "s_waitcnt vmcnt(0)\n"
    "s_branch .Lgok_34\n"
    ".Lclean_35:\n"
    "v_mov_b32 v239, 0\n"
    "v_mov_b32 v124, v123\n"
    "v_mov_b32 v125, v123\n"
    "v_mov_b32 v126, v123\n"
    "v_mov_b32 v127, v123\n"
    "v_mul_f32 v128, 0.5, v123\n"
    "v_mul_f32 v129, 0.5, v123\n"
    "v_mul_f32 v130, 0.5, v123\n"
    "v_mul_f32 v131, 0.5, v123\n"
    "s_add_u32 s81, s82, 0\n"
    "buffer_load_dwordx4 v[240:243], v113, s[12:15], s81 offen offset:512 sc1\n"
    "buffer_load_dwordx4 v[244:247], v114, s[12:15], s81 offen offset:768 sc1\n"
    "s_waitcnt vmcnt(0)\n"
    "v_max3_u32 v239, v239, v244, v245\n"
    "v_max3_u32 v239, v239, v246, v247\n"
    "v_max3_u32 v239, v239, v240, v241\n"
    "v_max3_u32 v239, v239, v242, v243\n"
    "v_cndmask_b32 v228, 0, v244, s[4:5]\n"
    "v_cndmask_b32 v229, 0, v245, s[4:5]\n"
    "v_cndmask_b32 v230, 0, v246, s[4:5]\n"
    "v_cndmask_b32 v231, 0, v247, s[4:5]\n"
    "v_mfma_f32_16x16x32_bf16 v[124:127], v[240:243], v[0:3], v[124:127]\n"
    "s_nop 0\n"
    "v_mfma_f32_16x16x32_bf16 v[128:131], v[228:231], v[0:3], v[128:131]\n"
    "v_mfma_f32_16x16x32_bf16 v[132:135], v[240:243], v[4:7], 0\n"
    "v_mfma_f32_16x16x32_bf16 v[136:139], v[228:231], v[4:7], 0\n"
    "v_mfma_f32_16x16x32_bf16 v[140:143], v[240:243], v[8:11], 0\n"
    "v_mfma_f32_16x16x32_bf16 v[144:147], v[228:231], v[8:11], 0\n"
    "v_mfma_f32_16x16x32_bf16 v[148:151], v[240:243], v[12:15], 0\n"
    "v_mfma_f32_16x16x32_bf16 v[152:155], v[228:231], v[12:15], 0\n"
    "s_add_u32 s81, s82, 28672\n"
    "buffer_load_dwordx4 v[240:243], v113, s[12:15], s81 offen offset:512 sc1\n"
    "s_waitcnt vmcnt(0)\n"
    "v_max3_u32 v239, v239, v240, v241\n"
    "v_max3_u32 v239, v239, v242, v243\n"
    "v_cndmask_b32 v228, v244, 0, s[4:5]\n"
    "v_cndmask_b32 v229, v245, 0, s[4:5]\n"
    "v_cndmask_b32 v230, v246, 0, s[4:5]\n"
    "v_cndmask_b32 v231, v247, 0, s[4:5]\n"
    "v_mfma_f32_16x16x32_bf16 v[124:127], v[240:243], v[16:19], v[124:127]\n"
    "s_nop 0\n"
    "v_mfma_f32_16x16x32_bf16 v[128:131], v[228:231], v[16:19], v[128:131]\n"
    "v_mfma_f32_16x16x32_bf16 v[132:135], v[240:243], v[20:23], v[132:135]\n"
    "v_mfma_f32_16x16x32_bf16 v[136:139], v[228:231], v[20:23], v[136:139]\n"
    "v_mfma_f32_16x16x32_bf16 v[140:143], v[240:243], v[24:27], v[140:143]\n"
    "v_mfma_f32_16x16x32_bf16 v[144:147], v[228:231], v[24:27], v[144:147]\n"
    "v_mfma_f32_16x16x32_bf16 v[148:151], v[240:243], v[28:31], v[148:151]\n"
    "v_mfma_f32_16x16x32_bf16 v[152:155], v[228:231], v[28:31], v[152:155]\n"
    "s_add_u32 s81, s82, 57344\n"
    "buffer_load_dwordx4 v[240:243], v113, s[12:15], s81 offen offset:512 sc1\n"
    "buffer_load_dwordx4 v[244:247], v114, s[12:15], s81 offen offset:768 sc1\n"
    "s_waitcnt vmcnt(0)\n"
    "v_max3_u32 v239, v239, v244, v245\n"
    "v_max3_u32 v239, v239, v246, v247\n"
    "v_max3_u32 v239, v239, v240, v241\n"
    "v_max3_u32 v239, v239, v242, v243\n"
    "v_cndmask_b32 v228, 0, v244, s[4:5]\n"
    "v_cndmask_b32 v229, 0, v245, s[4:5]\n"
    "v_cndmask_b32 v230, 0, v246, s[4:5]\n"
    "v_cndmask_b32 v231, 0, v247, s[4:5]\n"
    "v_mfma_f32_16x16x32_bf16 v[124:127], v[240:243], v[32:35], v[124:127]\n"
    "s_nop 0\n"
    "v_mfma_f32_16x16x32_bf16 v[128:131], v[228:231], v[32:35], v[128:131]\n"
    "v_mfma_f32_16x16x32_bf16 v[132:135], v[240:243], v[36:39], v[132:135]\n"
    "v_mfma_f32_16x16x32_bf16 v[136:139], v[228:231], v[36:39], v[136:139]\n"
    "v_mfma_f32_16x16x32_bf16 v[140:143], v[240:243], v[40:43], v[140:143]\n"
    "v_mfma_f32_16x16x32_bf16 v[144:147], v[228:231], v[40:43], v[144:147]\n"
    "v_mfma_f32_16x16x32_bf16 v[148:151], v[240:243], v[44:47], v[148:151]\n"
    "v_mfma_f32_16x16x32_bf16 v[152:155], v[228:231], v[44:47], v[152:155]\n"
    "s_add_u32 s81, s82, 86016\n"
    "buffer_load_dwordx4 v[240:243], v113, s[12:15], s81 offen offset:512 sc1\n"
    "s_waitcnt vmcnt(0)\n"
    "v_max3_u32 v239, v239, v240, v241\n"
    "v_max3_u32 v239, v239, v242, v243\n"
    "v_cndmask_b32 v228, v244, 0, s[4:5]\n"
    "v_cndmask_b32 v229, v245, 0, s[4:5]\n"
    "v_cndmask_b32 v230, v246, 0, s[4:5]\n"
    "v_cndmask_b32 v231, v247, 0, s[4:5]\n"
    "v_mfma_f32_16x16x32_bf16 v[124:127], v[240:243], v[48:51], v[124:127]\n"
    "s_nop 0\n"
    "v_mfma_f32_16x16x32_bf16 v[128:131], v[228:231], v[48:51], v[128:131]\n"
    "v_mfma_f32_16x16x32_bf16 v[132:135], v[240:243], v[52:55], v[132:135]\n"
    "v_mfma_f32_16x16x32_bf16 v[136:139], v[228:231], v[52:55], v[136:139]\n"
    "v_mfma_f32_16x16x32_bf16 v[140:143], v[240:243], v[56:59], v[140:143]\n"
    "v_mfma_f32_16x16x32_bf16 v[144:147], v[228:231], v[56:59], v[144:147]\n"
    "v_mfma_f32_16x16x32_bf16 v[148:151], v[240:243], v[60:63], v[148:151]\n"
    "v_mfma_f32_16x16x32_bf16 v[152:155], v[228:231], v[60:63], v[152:155]\n"
    "s_add_u32 s81, s82, 114688\n"
    "buffer_load_dwordx4 v[240:243], v113, s[12:15], s81 offen offset:512 sc1\n"
    "buffer_load_dwordx4 v[244:247], v114, s[12:15], s81 offen offset:768 sc1\n"
    "s_waitcnt vmcnt(0)\n"
    "v_max3_u32 v239, v239, v244, v245\n"
    "v_max3_u32 v239, v239, v246, v247\n"
    "v_max3_u32 v239, v239, v240, v241\n"
    "v_max3_u32 v239, v239, v242, v243\n"
    "v_cndmask_b32 v228, 0, v244, s[4:5]\n"
    "v_cndmask_b32 v229, 0, v245, s[4:5]\n"
    "v_cndmask_b32 v230, 0, v246, s[4:5]\n"
    "v_cndmask_b32 v231, 0, v247, s[4:5]\n"
    "v_mfma_f32_16x16x32_bf16 v[124:127], v[240:243], v[64:67], v[124:127]\n"
    "s_nop 0\n"
    "v_mfma_f32_16x16x32_bf16 v[128:131], v[228:231], v[64:67], v[128:131]\n"
    "v_mfma_f32_16x16x32_bf16 v[132:135], v[240:243], v[68:71], v[132:135]\n"
    "v_mfma_f32_16x16x32_bf16 v[136:139], v[228:231], v[68:71], v[136:139]\n"
    "v_mfma_f32_16x16x32_bf16 v[140:143], v[240:243], v[72:75], v[140:143]\n"
    "v_mfma_f32_16x16x32_bf16 v[144:147], v[228:231], v[72:75], v[144:147]\n"
    "v_mfma_f32_16x16x32_bf16 v[148:151], v[240:243], v[76:79], v[148:151]\n"
    "v_mfma_f32_16x16x32_bf16 v[152:155], v[228:231], v[76:79], v[152:155]\n"
    "s_add_u32 s81, s82, 143360\n"
    "buffer_load_dwordx4 v[240:243], v113, s[12:15], s81 offen offset:512 sc1\n"
    "s_waitcnt vmcnt(0)\n"
    "v_max3_u32 v239, v239, v240, v241\n"
    "v_max3_u32 v239, v239, v242, v243\n"
    "v_cndmask_b32 v228, v244, 0, s[4:5]\n"
    "v_cndmask_b32 v229, v245, 0, s[4:5]\n"
    "v_cndmask_b32 v230, v246, 0, s[4:5]\n"
    "v_cndmask_b32 v231, v247, 0, s[4:5]\n"
    "v_mfma_f32_16x16x32_bf16 v[124:127], v[240:243], v[80:83], v[124:127]\n"
    "s_nop 0\n"
    "v_mfma_f32_16x16x32_bf16 v[128:131], v[228:231], v[80:83], v[128:131]\n"
    "v_mfma_f32_16x16x32_bf16 v[132:135], v[240:243], v[84:87], v[132:135]\n"
    "v_mfma_f32_16x16x32_bf16 v[136:139], v[228:231], v[84:87], v[136:139]\n"
    "v_mfma_f32_16x16x32_bf16 v[140:143], v[240:243], v[88:91], v[140:143]\n"
    "v_mfma_f32_16x16x32_bf16 v[144:147], v[228:231], v[88:91], v[144:147]\n"
    "v_mfma_f32_16x16x32_bf16 v[148:151], v[240:243], v[92:95], v[148:151]\n"
    "v_mfma_f32_16x16x32_bf16 v[152:155], v[228:231], v[92:95], v[152:155]\n"
    "s_add_u32 s81, s82, 172032\n"
    "buffer_load_dwordx4 v[240:243], v113, s[12:15], s81 offen offset:512 sc1\n"
    "buffer_load_dwordx4 v[244:247], v114, s[12:15], s81 offen offset:768 sc1\n"
    "s_waitcnt vmcnt(0)\n"
    "v_max3_u32 v239, v239, v244, v245\n"
    "v_max3_u32 v239, v239, v246, v247\n"
    "v_max3_u32 v239, v239, v240, v241\n"
    "v_max3_u32 v239, v239, v242, v243\n"
    "v_cndmask_b32 v228, 0, v244, s[4:5]\n"
    "v_cndmask_b32 v229, 0, v245, s[4:5]\n"
    "v_cndmask_b32 v230, 0, v246, s[4:5]\n"
    "v_cndmask_b32 v231, 0, v247, s[4:5]\n"
    "ds_read_b64_tr_b16 v[248:249], v115 offset:0\n"
    "ds_read_b64_tr_b16 v[250:251], v115 offset:512\n"
    "s_waitcnt lgkmcnt(0)\n"
    "v_mfma_f32_16x16x32_bf16 v[124:127], v[240:243], v[248:251], v[124:127]\n"
    "v_mfma_f32_16x16x32_bf16 v[128:131], v[228:231], v[248:251], v[128:131]\n"
    "ds_read_b64_tr_b16 v[248:249], v116 offset:0\n"
    "ds_read_b64_tr_b16 v[250:251], v116 offset:512\n"
    "s_waitcnt lgkmcnt(0)\n"
    "v_mfma_f32_16x16x32_bf16 v[132:135], v[240:243], v[248:251], v[132:135]\n"
    "v_mfma_f32_16x16x32_bf16 v[136:139], v[228:231], v[248:251], v[136:139]\n"
    "ds_read_b64_tr_b16 v[248:249], v117 offset:0\n"
    "ds_read_b64_tr_b16 v[250:251], v117 offset:512\n"
    "s_waitcnt lgkmcnt(0)\n"
    "v_mfma_f32_16x16x32_bf16 v[140:143], v[240:243], v[248:251], v[140:143]\n"
    "v_mfma_f32_16x16x32_bf16 v[144:147], v[228:231], v[248:251], v[144:147]\n"
    "ds_read_b64_tr_b16 v[248:249], v118 offset:0\n"
    "ds_read_b64_tr_b16 v[250:251], v118 offset:512\n"
    "s_waitcnt lgkmcnt(0)\n"
    "v_mfma_f32_16x16x32_bf16 v[148:151], v[240:243], v[248:251], v[148:151]\n"
    "v_mfma_f32_16x16x32_bf16 v[152:155], v[228:231], v[248:251], v[152:155]\n"
    "s_add_u32 s81, s82, 200704\n"
    "buffer_load_dwordx4 v[240:243], v113, s[12:15], s81 offen offset:512 sc1\n"
    "s_waitcnt vmcnt(0)\n"
    "v_max3_u32 v239, v239, v240, v241\n"
    "v_max3_u32 v239, v239, v242, v243\n"
    "v_cndmask_b32 v228, v244, 0, s[4:5]\n"
    "v_cndmask_b32 v229, v245, 0, s[4:5]\n"
    "v_cndmask_b32 v230, v246, 0, s[4:5]\n"
    "v_cndmask_b32 v231, v247, 0, s[4:5]\n"
    "ds_read_b64_tr_b16 v[248:249], v115 offset:4096\n"
    "ds_read_b64_tr_b16 v[250:251], v115 offset:4608\n"
    "s_waitcnt lgkmcnt(0)\n"
    "v_mfma_f32_16x16x32_bf16 v[124:127], v[240:243], v[248:251], v[124:127]\n"
    "v_mfma_f32_16x16x32_bf16 v[128:131], v[228:231], v[248:251], v[128:131]\n"
    "ds_read_b64_tr_b16 v[248:249], v116 offset:4096\n"
    "ds_read_b64_tr_b16 v[250:251], v116 offset:4608\n"
    "s_waitcnt lgkmcnt(0)\n"
    "v_mfma_f32_16x16x32_bf16 v[132:135], v[240:243], v[248:251], v[132:135]\n"
    "v_mfma_f32_16x16x32_bf16 v[136:139], v[228:231], v[248:251], v[136:139]\n"
    "ds_read_b64_tr_b16 v[248:249], v117 offset:4096\n"
    "ds_read_b64_tr_b16 v[250:251], v117 offset:4608\n"
    "s_waitcnt lgkmcnt(0)\n"
    "v_mfma_f32_16x16x32_bf16 v[140:143], v[240:243], v[248:251], v[140:143]\n"
    "v_mfma_f32_16x16x32_bf16 v[144:147], v[228:231], v[248:251], v[144:147]\n"
    "ds_read_b64_tr_b16 v[248:249], v118 offset:4096\n"
    "ds_read_b64_tr_b16 v[250:251], v118 offset:4608\n"
    "s_waitcnt lgkmcnt(0)\n"
    "v_mfma_f32_16x16x32_bf16 v[148:151], v[240:243], v[248:251], v[148:151]\n"
    "v_mfma_f32_16x16x32_bf16 v[152:155], v[228:231], v[248:251], v[152:155]\n"
    "s_add_u32 s81, s82, 229376\n"
    "buffer_load_dwordx4 v[240:243], v113, s[12:15], s81 offen offset:512 sc1\n"
    "buffer_load_dwordx4 v[244:247], v114, s[12:15], s81 offen offset:768 sc1\n"
    "s_waitcnt vmcnt(0)\n"
    "v_max3_u32 v239, v239, v244, v245\n"
    "v_max3_u32 v239, v239, v246, v247\n"
    "v_max3_u32 v239, v239, v240, v241\n"
    "v_max3_u32 v239, v239, v242, v243\n"
    "v_cndmask_b32 v228, 0, v244, s[4:5]\n"
    "v_cndmask_b32 v229, 0, v245, s[4:5]\n"
    "v_cndmask_b32 v230, 0, v246, s[4:5]\n"
    "v_cndmask_b32 v231, 0, v247, s[4:5]\n"
    "ds_read_b64_tr_b16 v[248:249], v115 offset:8192\n"
    "ds_read_b64_tr_b16 v[250:251], v115 offset:8704\n"
    "s_waitcnt lgkmcnt(0)\n"
    "v_mfma_f32_16x16x32_bf16 v[124:127], v[240:243], v[248:251], v[124:127]\n"
    "v_mfma_f32_16x16x32_bf16 v[128:131], v[228:231], v[248:251], v[128:131]\n"
    "ds_read_b64_tr_b16 v[248:249], v116 offset:8192\n"
    "ds_read_b64_tr_b16 v[250:251], v116 offset:8704\n"
    "s_waitcnt lgkmcnt(0)\n"
    "v_mfma_f32_16x16x32_bf16 v[132:135], v[240:243], v[248:251], v[132:135]\n"
    "v_mfma_f32_16x16x32_bf16 v[136:139], v[228:231], v[248:251], v[136:139]\n"
    "ds_read_b64_tr_b16 v[248:249], v117 offset:8192\n"
    "ds_read_b64_tr_b16 v[250:251], v117 offset:8704\n"
    "s_waitcnt lgkmcnt(0)\n"
    "v_mfma_f32_16x16x32_bf16 v[140:143], v[240:243], v[248:251], v[140:143]\n"
    "v_mfma_f32_16x16x32_bf16 v[144:147], v[228:231], v[248:251], v[144:147]\n"
    "ds_read_b64_tr_b16 v[248:249], v118 offset:8192\n"
    "ds_read_b64_tr_b16 v[250:251], v118 offset:8704\n"
    "s_waitcnt lgkmcnt(0)\n"
    "v_mfma_f32_16x16x32_bf16 v[148:151], v[240:243], v[248:251], v[148:151]\n"
    "v_mfma_f32_16x16x32_bf16 v[152:155], v[228:231], v[248:251], v[152:155]\n"
    "s_add_u32 s81, s82, 258048\n"
    "buffer_load_dwordx4 v[240:243], v113, s[12:15], s81 offen offset:512 sc1\n"
    "s_waitcnt vmcnt(0)\n"
    "v_max3_u32 v239, v239, v240, v241\n"
    "v_max3_u32 v239, v239, v242, v243\n"
    "v_cndmask_b32 v228, v244, 0, s[4:5]\n"
    "v_cndmask_b32 v229, v245, 0, s[4:5]\n"
    "v_cndmask_b32 v230, v246, 0, s[4:5]\n"
    "v_cndmask_b32 v231, v247, 0, s[4:5]\n"
    "ds_read_b64_tr_b16 v[248:249], v115 offset:12288\n"
    "ds_read_b64_tr_b16 v[250:251], v115 offset:12800\n"
    "s_waitcnt lgkmcnt(0)\n"
    "v_mfma_f32_16x16x32_bf16 v[124:127], v[240:243], v[248:251], v[124:127]\n"
    "v_mfma_f32_16x16x32_bf16 v[128:131], v[228:231], v[248:251], v[128:131]\n"
    "ds_read_b64_tr_b16 v[248:249], v116 offset:12288\n"
    "ds_read_b64_tr_b16 v[250:251], v116 offset:12800\n"
    "s_waitcnt lgkmcnt(0)\n"
    "v_mfma_f32_16x16x32_bf16 v[132:135], v[240:243], v[248:251], v[132:135]\n"
    "v_mfma_f32_16x16x32_bf16 v[136:139], v[228:231], v[248:251], v[136:139]\n"
    "ds_read_b64_tr_b16 v[248:249], v117 offset:12288\n"
    "ds_read_b64_tr_b16 v[250:251], v117 offset:12800\n"
    "s_waitcnt lgkmcnt(0)\n"
    "v_mfma_f32_16x16x32_bf16 v[140:143], v[240:243], v[248:251], v[140:143]\n"
    "v_mfma_f32_16x16x32_bf16 v[144:147], v[228:231], v[248:251], v[144:147]\n"
    "ds_read_b64_tr_b16 v[248:249], v118 offset:12288\n"
    "ds_read_b64_tr_b16 v[250:251], v118 offset:12800\n"
    "s_waitcnt lgkmcnt(0)\n"
    "v_mfma_f32_16x16x32_bf16 v[148:151], v[240:243], v[248:251], v[148:151]\n"
    "v_mfma_f32_16x16x32_bf16 v[152:155], v[228:231], v[248:251], v[152:155]\n"
    "s_add_u32 s81, s82, 286720\n"
    "buffer_load_dwordx4 v[240:243], v113, s[12:15], s81 offen offset:512 sc1\n"
    "buffer_load_dwordx4 v[244:247], v114, s[12:15], s81 offen offset:768 sc1\n"
    "s_waitcnt vmcnt(0)\n"
    "v_max3_u32 v239, v239, v244, v245\n"
    "v_max3_u32 v239, v239, v246, v247\n"
    "v_max3_u32 v239, v239, v240, v241\n"
    "v_max3_u32 v239, v239, v242, v243\n"
    "v_cndmask_b32 v228, 0, v244, s[4:5]\n"
    "v_cndmask_b32 v229, 0, v245, s[4:5]\n"
    "v_cndmask_b32 v230, 0, v246, s[4:5]\n"
    "v_cndmask_b32 v231, 0, v247, s[4:5]\n"
    "ds_read_b64_tr_b16 v[248:249], v115 offset:16384\n"
    "ds_read_b64_tr_b16 v[250:251], v115 offset:16896\n"
    "s_waitcnt lgkmcnt(0)\n"
    "v_mfma_f32_16x16x32_bf16 v[124:127], v[240:243], v[248:251], v[124:127]\n"
    "v_mfma_f32_16x16x32_bf16 v[128:131], v[228:231], v[248:251], v[128:131]\n"
    "ds_read_b64_tr_b16 v[248:249], v116 offset:16384\n"
    "ds_read_b64_tr_b16 v[250:251], v116 offset:16896\n"
    "s_waitcnt lgkmcnt(0)\n"
    "v_mfma_f32_16x16x32_bf16 v[132:135], v[240:243], v[248:251], v[132:135]\n"
    "v_mfma_f32_16x16x32_bf16 v[136:139], v[228:231], v[248:251], v[136:139]\n"
    "ds_read_b64_tr_b16 v[248:249], v117 offset:16384\n"
    "ds_read_b64_tr_b16 v[250:251], v117 offset:16896\n"
    "s_waitcnt lgkmcnt(0)\n"
    "v_mfma_f32_16x16x32_bf16 v[140:143], v[240:243], v[248:251], v[140:143]\n"
    "v_mfma_f32_16x16x32_bf16 v[144:147], v[228:231], v[248:251], v[144:147]\n"
    "ds_read_b64_tr_b16 v[248:249], v118 offset:16384\n"
    "ds_read_b64_tr_b16 v[250:251], v118 offset:16896\n"
    "s_waitcnt lgkmcnt(0)\n"
    "v_mfma_f32_16x16x32_bf16 v[148:151], v[240:243], v[248:251], v[148:151]\n"
    "v_mfma_f32_16x16x32_bf16 v[152:155], v[228:231], v[248:251], v[152:155]\n"
    "s_add_u32 s81, s82, 315392\n"
    "buffer_load_dwordx4 v[240:243], v113, s[12:15], s81 offen offset:512 sc1\n"
    "s_waitcnt vmcnt(0)\n"
    "v_max3_u32 v239, v239, v240, v241\n"
    "v_max3_u32 v239, v239, v242, v243\n"
    "v_cndmask_b32 v228, v244, 0, s[4:5]\n"
    "v_cndmask_b32 v229, v245, 0, s[4:5]\n"
    "v_cndmask_b32 v230, v246, 0, s[4:5]\n"
    "v_cndmask_b32 v231, v247, 0, s[4:5]\n"
    "ds_read_b64_tr_b16 v[248:249], v115 offset:20480\n"
    "ds_read_b64_tr_b16 v[250:251], v115 offset:20992\n"
    "s_waitcnt lgkmcnt(0)\n"
    "v_mfma_f32_16x16x32_bf16 v[124:127], v[240:243], v[248:251], v[124:127]\n"
    "v_mfma_f32_16x16x32_bf16 v[128:131], v[228:231], v[248:251], v[128:131]\n"
    "ds_read_b64_tr_b16 v[248:249], v116 offset:20480\n"
    "ds_read_b64_tr_b16 v[250:251], v116 offset:20992\n"
    "s_waitcnt lgkmcnt(0)\n"
    "v_mfma_f32_16x16x32_bf16 v[132:135], v[240:243], v[248:251], v[132:135]\n"
    "v_mfma_f32_16x16x32_bf16 v[136:139], v[228:231], v[248:251], v[136:139]\n"
    "ds_read_b64_tr_b16 v[248:249], v117 offset:20480\n"
    "ds_read_b64_tr_b16 v[250:251], v117 offset:20992\n"
    "s_waitcnt lgkmcnt(0)\n"
    "v_mfma_f32_16x16x32_bf16 v[140:143], v[240:243], v[248:251], v[140:143]\n"
    "v_mfma_f32_16x16x32_bf16 v[144:147], v[228:231], v[248:251], v[144:147]\n"
    "ds_read_b64_tr_b16 v[248:249], v118 offset:20480\n"
    "ds_read_b64_tr_b16 v[250:251], v118 offset:20992\n"
    "s_waitcnt lgkmcnt(0)\n"
    "v_mfma_f32_16x16x32_bf16 v[148:151], v[240:243], v[248:251], v[148:151]\n"
    "v_mfma_f32_16x16x32_bf16 v[152:155], v[228:231], v[248:251], v[152:155]\n"
    "s_add_u32 s81, s82, 344064\n"
    "buffer_load_dwordx4 v[240:243], v113, s[12:15], s81 offen offset:512 sc1\n"
    "buffer_load_dwordx4 v[244:247], v114, s[12:15], s81 offen offset:768 sc1\n"
    "s_waitcnt vmcnt(0)\n"
    "v_max3_u32 v239, v239, v244, v245\n"
    "v_max3_u32 v239, v239, v246, v247\n"
    "v_max3_u32 v239, v239, v240, v241\n"
    "v_max3_u32 v239, v239, v242, v243\n"
    "v_cndmask_b32 v228, 0, v244, s[4:5]\n"
    "v_cndmask_b32 v229, 0, v245, s[4:5]\n"
    "v_cndmask_b32 v230, 0, v246, s[4:5]\n"
    "v_cndmask_b32 v231, 0, v247, s[4:5]\n"
    "ds_read_b64_tr_b16 v[248:249], v115 offset:24576\n"
    "ds_read_b64_tr_b16 v[250:251], v115 offset:25088\n"
    "s_waitcnt lgkmcnt(0)\n"
    "v_mfma_f32_16x16x32_bf16 v[124:127], v[240:243], v[248:251], v[124:127]\n"
    "v_mfma_f32_16x16x32_bf16 v[128:131], v[228:231], v[248:251], v[128:131]\n"
    "ds_read_b64_tr_b16 v[248:249], v116 offset:24576\n"
    "ds_read_b64_tr_b16 v[250:251], v116 offset:25088\n"
    "s_waitcnt lgkmcnt(0)\n"
    "v_mfma_f32_16x16x32_bf16 v[132:135], v[240:243], v[248:251], v[132:135]\n"
    "v_mfma_f32_16x16x32_bf16 v[136:139], v[228:231], v[248:251], v[136:139]\n"
    "ds_read_b64_tr_b16 v[248:249], v117 offset:24576\n"
    "ds_read_b64_tr_b16 v[250:251], v117 offset:25088\n"
    "s_waitcnt lgkmcnt(0)\n"
    "v_mfma_f32_16x16x32_bf16 v[140:143], v[240:243], v[248:251], v[140:143]\n"
    "v_mfma_f32_16x16x32_bf16 v[144:147], v[228:231], v[248:251], v[144:147]\n"
    "ds_read_b64_tr_b16 v[248:249], v118 offset:24576\n"
    "ds_read_b64_tr_b16 v[250:251], v118 offset:25088\n"
    "s_waitcnt lgkmcnt(0)\n"
    "v_mfma_f32_16x16x32_bf16 v[148:151], v[240:243], v[248:251], v[148:151]\n"
    "v_mfma_f32_16x16x32_bf16 v[152:155], v[228:231], v[248:251], v[152:155]\n"
    "s_add_u32 s81, s82, 372736\n"
    "buffer_load_dwordx4 v[240:243], v113, s[12:15], s81 offen offset:512 sc1\n"
    "s_waitcnt vmcnt(0)\n"
    "v_max3_u32 v239, v239, v240, v241\n"
    "v_max3_u32 v239, v239, v242, v243\n"
    "v_cndmask_b32 v228, v244, 0, s[4:5]\n"
    "v_cndmask_b32 v229, v245, 0, s[4:5]\n"
    "v_cndmask_b32 v230, v246, 0, s[4:5]\n"
    "v_cndmask_b32 v231, v247, 0, s[4:5]\n"
    "ds_read_b64_tr_b16 v[248:249], v115 offset:28672\n"
    "ds_read_b64_tr_b16 v[250:251], v115 offset:29184\n"
    "s_waitcnt lgkmcnt(0)\n"
    "v_mfma_f32_16x16x32_bf16 v[124:127], v[240:243], v[248:251], v[124:127]\n"
    "v_mfma_f32_16x16x32_bf16 v[128:131], v[228:231], v[248:251], v[128:131]\n"
    "ds_read_b64_tr_b16 v[248:249], v116 offset:28672\n"
    "ds_read_b64_tr_b16 v[250:251], v116 offset:29184\n"
    "s_waitcnt lgkmcnt(0)\n"
    "v_mfma_f32_16x16x32_bf16 v[132:135], v[240:243], v[248:251], v[132:135]\n"
    "v_mfma_f32_16x16x32_bf16 v[136:139], v[228:231], v[248:251], v[136:139]\n"
    "ds_read_b64_tr_b16 v[248:249], v117 offset:28672\n"
    "ds_read_b64_tr_b16 v[250:251], v117 offset:29184\n"
    "s_waitcnt lgkmcnt(0)\n"
    "v_mfma_f32_16x16x32_bf16 v[140:143], v[240:243], v[248:251], v[140:143]\n"
    "v_mfma_f32_16x16x32_bf16 v[144:147], v[228:231], v[248:251], v[144:147]\n"
    "ds_read_b64_tr_b16 v[248:249], v118 offset:28672\n"
    "ds_read_b64_tr_b16 v[250:251], v118 offset:29184\n"
    "s_waitcnt lgkmcnt(0)\n"
    "v_mfma_f32_16x16x32_bf16 v[148:151], v[240:243], v[248:251], v[148:151]\n"
    "v_mfma_f32_16x16x32_bf16 v[152:155], v[228:231], v[248:251], v[152:155]\n"
    "v_cmp_eq_u32 vcc, -1, v239\n"
    "s_cbranch_vccnz .Lredo_37\n"
    ".Lredook_38:\n"
    "s_waitcnt vmcnt(0) lgkmcnt(0)\n"
    "s_branch .Lback_36\n"
    ".Lredo_37:\n"
    "s_waitcnt vmcnt(0) lgkmcnt(0)\n"
    "s_sub_u32 s60, s60, 1\n"
    "s_cbranch_scc0 .Lclean_35\n"
    "s_mov_b32 s60, 0\n"
    "s_branch .Lredook_38\n"
    ".Lredo_39:\n"
    "s_waitcnt vmcnt(0) lgkmcnt(0)\n"
    "s_branch .Lclean_35\n"
    ".Lclean_43:\n"
    "v_mov_b32 v239, 0\n"
    "v_mov_b32 v156, v123\n"
    "v_mov_b32 v157, v123\n"
    "v_mov_b32 v158, v123\n"
    "v_mov_b32 v159, v123\n"
    "v_mov_b32 v160, v123\n"
    "v_mov_b32 v161, v123\n"
    "v_mov_b32 v162, v123\n"
    "v_mov_b32 v163, v123\n"
    "s_add_u32 s81, s82, 0\n"
    "buffer_load_dwordx4 v[240:243], v113, s[12:15], s81 offen offset:0 sc1\n"
    "buffer_load_dwordx4 v[244:247], v113, s[12:15], s81 offen offset:256 sc1\n"
    "s_waitcnt vmcnt(0)\n"
    "v_max3_u32 v239, v239, v244, v245\n"
    "v_max3_u32 v239, v239, v246, v247\n"
    "v_max3_u32 v239, v239, v240, v241\n"
    "v_max3_u32 v239, v239, v242, v243\n"
    "v_mfma_f32_16x16x32_bf16 v[156:159], v[240:243], v[0:3], v[156:159]\n"
    "v_mfma_f32_16x16x32_bf16 v[160:163], v[244:247], v[0:3], v[160:163]\n"
    "v_mfma_f32_16x16x32_bf16 v[164:167], v[240:243], v[4:7], 0\n"
    "v_mfma_f32_16x16x32_bf16 v[168:171], v[244:247], v[4:7], 0\n"
    "v_mfma_f32_16x16x32_bf16 v[172:175], v[240:243], v[8:11], 0\n"
    "v_mfma_f32_16x16x32_bf16 v[176:179], v[244:247], v[8:11], 0\n"
    "v_mfma_f32_16x16x32_bf16 v[180:183], v[240:243], v[12:15], 0\n"
    "v_mfma_f32_16x16x32_bf16 v[184:187], v[244:247], v[12:15], 0\n"
    "s_add_u32 s81, s82, 28672\n"
    "buffer_load_dwordx4 v[240:243], v113, s[12:15], s81 offen offset:0 sc1\n"
    "buffer_load_dwordx4 v[244:247], v113, s[12:15], s81 offen offset:256 sc1\n"
    "s_waitcnt vmcnt(0)\n"
    "v_max3_u32 v239, v239, v244, v245\n"
    "v_max3_u32 v239, v239, v246, v247\n"
    "v_max3_u32 v239, v239, v240, v241\n"
    "v_max3_u32 v239, v239, v242, v243\n"
    "v_mfma_f32_16x16x32_bf16 v[156:159], v[240:243], v[16:19], v[156:159]\n"
    "v_mfma_f32_16x16x32_bf16 v[160:163], v[244:247], v[16:19], v[160:163]\n"
    "v_mfma_f32_16x16x32_bf16 v[164:167], v[240:243], v[20:23], v[164:167]\n"
    "v_mfma_f32_16x16x32_bf16 v[168:171], v[244:247], v[20:23], v[168:171]\n"
    "v_mfma_f32_16x16x32_bf16 v[172:175], v[240:243], v[24:27], v[172:175]\n"
    "v_mfma_f32_16x16x32_bf16 v[176:179], v[244:247], v[24:27], v[176:179]\n"
    "v_mfma_f32_16x16x32_bf16 v[180:183], v[240:243], v[28:31], v[180:183]\n"
    "v_mfma_f32_16x16x32_bf16 v[184:187], v[244:247], v[28:31], v[184:187]\n"
    "s_add_u32 s81, s82, 57344\n"
    "buffer_load_dwordx4 v[240:243], v113, s[12:15], s81 offen offset:0 sc1\n"
    "buffer_load_dwordx4 v[244:247], v113, s[12:15], s81 offen offset:256 sc1\n"
    "s_waitcnt vmcnt(0)\n"
    "v_max3_u32 v239, v239, v244, v245\n"
    "v_max3_u32 v239, v239, v246, v247\n"
    "v_max3_u32 v239, v239, v240, v241\n"
    "v_max3_u32 v239, v239, v242, v243\n"
    "v_mfma_f32_16x16x32_bf16 v[156:159], v[240:243], v[32:35], v[156:159]\n"
    "v_mfma_f32_16x16x32_bf16 v[160:163], v[244:247], v[32:35], v[160:163]\n"
    "v_mfma_f32_16x16x32_bf16 v[164:167], v[240:243], v[36:39], v[164:167]\n"
    "v_mfma_f32_16x16x32_bf16 v[168:171], v[244:247], v[36:39], v[168:171]\n"
    "v_mfma_f32_16x16x32_bf16 v[172:175], v[240:243], v[40:43], v[172:175]\n"
    "v_mfma_f32_16x16x32_bf16 v[176:179], v[244:247], v[40:43], v[176:179]\n"
    "v_mfma_f32_16x16x32_bf16 v[180:183], v[240:243], v[44:47], v[180:183]\n"
    "v_mfma_f32_16x16x32_bf16 v[184:187], v[244:247], v[44:47], v[184:187]\n"
    "s_add_u32 s81, s82, 86016\n"
    "buffer_load_dwordx4 v[240:243], v113, s[12:15], s81 offen offset:0 sc1\n"
    "buffer_load_dwordx4 v[244:247], v113, s[12:15], s81 offen offset:256 sc1\n"
    "s_waitcnt vmcnt(0)\n"
    "v_max3_u32 v239, v239, v244, v245\n"
    "v_max3_u32 v239, v239, v246, v247\n"
    "v_max3_u32 v239, v239, v240, v241\n"
    "v_max3_u32 v239, v239, v242, v243\n"
    "v_mfma_f32_16x16x32_bf16 v[156:159], v[240:243], v[48:51], v[156:159]\n"
    "v_mfma_f32_16x16x32_bf16 v[160:163], v[244:247], v[48:51], v[160:163]\n"
    "v_mfma_f32_16x16x32_bf16 v[164:167], v[240:243], v[52:55], v[164:167]\n"
    "v_mfma_f32_16x16x32_bf16 v[168:171], v[244:247], v[52:55], v[168:171]\n"
    "v_mfma_f32_16x16x32_bf16 v[172:175], v[240:243], v[56:59], v[172:175]\n"
    "v_mfma_f32_16x16x32_bf16 v[176:179], v[244:247], v[56:59], v[176:179]\n"
    "v_mfma_f32_16x16x32_bf16 v[180:183], v[240:243], v[60:63], v[180:183]\n"
    "v_mfma_f32_16x16x32_bf16 v[184:187], v[244:247], v[60:63], v[184:187]\n"
    "s_add_u32 s81, s82, 114688\n"
    "buffer_load_dwordx4 v[240:243], v113, s[12:15], s81 offen offset:0 sc1\n"
    "buffer_load_dwordx4 v[244:247], v113, s[12:15], s81 offen offset:256 sc1\n"
    "s_waitcnt vmcnt(0)\n"
    "v_max3_u32 v239, v239, v244, v245\n"
    "v_max3_u32 v239, v239, v246, v247\n"
    "v_max3_u32 v239, v239, v240, v241\n"
    "v_max3_u32 v239, v239, v242, v243\n"
    "v_mfma_f32_16x16x32_bf16 v[156:159], v[240:243], v[64:67], v[156:159]\n"
    "v_mfma_f32_16x16x32_bf16 v[160:163], v[244:247], v[64:67], v[160:163]\n"
    "v_mfma_f32_16x16x32_bf16 v[164:167], v[240:243], v[68:71], v[164:167]\n"
    "v_mfma_f32_16x16x32_bf16 v[168:171], v[244:247], v[68:71], v[168:171]\n"
    "v_mfma_f32_16x16x32_bf16 v[172:175], v[240:243], v[72:75], v[172:175]\n"
    "v_mfma_f32_16x16x32_bf16 v[176:179], v[244:247], v[72:75], v[176:179]\n"
    "v_mfma_f32_16x16x32_bf16 v[180:183], v[240:243], v[76:79], v[180:183]\n"
    "v_mfma_f32_16x16x32_bf16 v[184:187], v[244:247], v[76:79], v[184:187]\n"
    "s_add_u32 s81, s82, 143360\n"
    "buffer_load_dwordx4 v[240:243], v113, s[12:15], s81 offen offset:0 sc1\n"
    "buffer_load_dwordx4 v[244:247], v113, s[12:15], s81 offen offset:256 sc1\n"
    "s_waitcnt vmcnt(0)\n"
    "v_max3_u32 v239, v239, v244, v245\n"
    "v_max3_u32 v239, v239, v246, v247\n"
    "v_max3_u32 v239, v239, v240, v241\n"
    "v_max3_u32 v239, v239, v242, v243\n"
    "v_mfma_f32_16x16x32_bf16 v[156:159], v[240:243], v[80:83], v[156:159]\n"
    "v_mfma_f32_16x16x32_bf16 v[160:163], v[244:247], v[80:83], v[160:163]\n"
    "v_mfma_f32_16x16x32_bf16 v[164:167], v[240:243], v[84:87], v[164:167]\n"
    "v_mfma_f32_16x16x32_bf16 v[168:171], v[244:247], v[84:87], v[168:171]\n"
    "v_mfma_f32_16x16x32_bf16 v[172:175], v[240:243], v[88:91], v[172:175]\n"
    "v_mfma_f32_16x16x32_bf16 v[176:179], v[244:247], v[88:91], v[176:179]\n"
    "v_mfma_f32_16x16x32_bf16 v[180:183], v[240:243], v[92:95], v[180:183]\n"
    "v_mfma_f32_16x16x32_bf16 v[184:187], v[244:247], v[92:95], v[184:187]\n"
    "s_add_u32 s81, s82, 172032\n"
    "buffer_load_dwordx4 v[240:243], v113, s[12:15], s81 offen offset:0 sc1\n"
    "buffer_load_dwordx4 v[244:247], v113, s[12:15], s81 offen offset:256 sc1\n"
    "s_waitcnt vmcnt(0)\n"
    "v_max3_u32 v239, v239, v244, v245\n"
    "v_max3_u32 v239, v239, v246, v247\n"
    "v_max3_u32 v239, v239, v240, v241\n"
    "v_max3_u32 v239, v239, v242, v243\n"
    "ds_read_b64_tr_b16 v[248:249], v115 offset:0\n"
    "ds_read_b64_tr_b16 v[250:251], v115 offset:512\n"
    "s_waitcnt lgkmcnt(0)\n"
    "v_mfma_f32_16x16x32_bf16 v[156:159], v[240:243], v[248:251], v[156:159]\n"
    "v_mfma_f32_16x16x32_bf16 v[160:163], v[244:247], v[248:251], v[160:163]\n"
    "ds_read_b64_tr_b16 v[248:249], v116 offset:0\n"
    "ds_read_b64_tr_b16 v[250:251], v116 offset:512\n"
    "s_waitcnt lgkmcnt(0)\n"
    "v_mfma_f32_16x16x32_bf16 v[164:167], v[240:243], v[248:251], v[164:167]\n"
    "v_mfma_f32_16x16x32_bf16 v[168:171], v[244:247], v[248:251], v[168:171]\n"
    "ds_read_b64_tr_b16 v[248:249], v117 offset:0\n"
    "ds_read_b64_tr_b16 v[250:251], v117 offset:512\n"
    "s_waitcnt lgkmcnt(0)\n"
    "v_mfma_f32_16x16x32_bf16 v[172:175], v[240:243], v[248:251], v[172:175]\n"
    "v_mfma_f32_16x16x32_bf16 v[176:179], v[244:247], v[248:251], v[176:179]\n"
    "ds_read_b64_tr_b16 v[248:249], v118 offset:0\n"
    "ds_read_b64_tr_b16 v[250:251], v118 offset:512\n"
    "s_waitcnt lgkmcnt(0)\n"
    "v_mfma_f32_16x16x32_bf16 v[180:183], v[240:243], v[248:251], v[180:183]\n"
    "v_mfma_f32_16x16x32_bf16 v[184:187], v[244:247], v[248:251], v[184:187]\n"
    "s_add_u32 s81, s82, 200704\n"
    "buffer_load_dwordx4 v[240:243], v113, s[12:15], s81 offen offset:0 sc1\n"
    "buffer_load_dwordx4 v[244:247], v113, s[12:15], s81 offen offset:256 sc1\n"
    "s_waitcnt vmcnt(0)\n"
    "v_max3_u32 v239, v239, v244, v245\n"
    "v_max3_u32 v239, v239, v246, v247\n"
    "v_max3_u32 v239, v239, v240, v241\n"
    "v_max3_u32 v239, v239, v242, v243\n"
    "ds_read_b64_tr_b16 v[248:249], v115 offset:4096\n"
    "ds_read_b64_tr_b16 v[250:251], v115 offset:4608\n"
    "s_waitcnt lgkmcnt(0)\n"
    "v_mfma_f32_16x16x32_bf16 v[156:159], v[240:243], v[248:251], v[156:159]\n"
    "v_mfma_f32_16x16x32_bf16 v[160:163], v[244:247], v[248:251], v[160:163]\n"
    "ds_read_b64_tr_b16 v[248:249], v116 offset:4096\n"
    "ds_read_b64_tr_b16 v[250:251], v116 offset:4608\n"
    "s_waitcnt lgkmcnt(0)\n"
    "v_mfma_f32_16x16x32_bf16 v[164:167], v[240:243], v[248:251], v[164:167]\n"
    "v_mfma_f32_16x16x32_bf16 v[168:171], v[244:247], v[248:251], v[168:171]\n"
    "ds_read_b64_tr_b16 v[248:249], v117 offset:4096\n"
    "ds_read_b64_tr_b16 v[250:251], v117 offset:4608\n"
    "s_waitcnt lgkmcnt(0)\n"
    "v_mfma_f32_16x16x32_bf16 v[172:175], v[240:243], v[248:251], v[172:175]\n"
    "v_mfma_f32_16x16x32_bf16 v[176:179], v[244:247], v[248:251], v[176:179]\n"
    "ds_read_b64_tr_b16 v[248:249], v118 offset:4096\n"
    "ds_read_b64_tr_b16 v[250:251], v118 offset:4608\n"
    "s_waitcnt lgkmcnt(0)\n"
    "v_mfma_f32_16x16x32_bf16 v[180:183], v[240:243], v[248:251], v[180:183]\n"
    "v_mfma_f32_16x16x32_bf16 v[184:187], v[244:247], v[248:251], v[184:187]\n"
    "s_add_u32 s81, s82, 229376\n"
    "buffer_load_dwordx4 v[240:243], v113, s[12:15], s81 offen offset:0 sc1\n"
    "buffer_load_dwordx4 v[244:247], v113, s[12:15], s81 offen offset:256 sc1\n"
    "s_waitcnt vmcnt(0)\n"
    "v_max3_u32 v239, v239, v244, v245\n"
    "v_max3_u32 v239, v239, v246, v247\n"
    "v_max3_u32 v239, v239, v240, v241\n"
    "v_max3_u32 v239, v239, v242, v243\n"
    "ds_read_b64_tr_b16 v[248:249], v115 offset:8192\n"
    "ds_read_b64_tr_b16 v[250:251], v115 offset:8704\n"
    "s_waitcnt lgkmcnt(0)\n"
    "v_mfma_f32_16x16x32_bf16 v[156:159], v[240:243], v[248:251], v[156:159]\n"
    "v_mfma_f32_16x16x32_bf16 v[160:163], v[244:247], v[248:251], v[160:163]\n"
    "ds_read_b64_tr_b16 v[248:249], v116 offset:8192\n"
    "ds_read_b64_tr_b16 v[250:251], v116 offset:8704\n"
    "s_waitcnt lgkmcnt(0)\n"
    "v_mfma_f32_16x16x32_bf16 v[164:167], v[240:243], v[248:251], v[164:167]\n"
    "v_mfma_f32_16x16x32_bf16 v[168:171], v[244:247], v[248:251], v[168:171]\n"
    "ds_read_b64_tr_b16 v[248:249], v117 offset:8192\n"
    "ds_read_b64_tr_b16 v[250:251], v117 offset:8704\n"
    "s_waitcnt lgkmcnt(0)\n"
    "v_mfma_f32_16x16x32_bf16 v[172:175], v[240:243], v[248:251], v[172:175]\n"
    "v_mfma_f32_16x16x32_bf16 v[176:179], v[244:247], v[248:251], v[176:179]\n"
    "ds_read_b64_tr_b16 v[248:249], v118 offset:8192\n"
    "ds_read_b64_tr_b16 v[250:251], v118 offset:8704\n"
    "s_waitcnt lgkmcnt(0)\n"
    "v_mfma_f32_16x16x32_bf16 v[180:183], v[240:243], v[248:251], v[180:183]\n"
    "v_mfma_f32_16x16x32_bf16 v[184:187], v[244:247], v[248:251], v[184:187]\n"
    "s_add_u32 s81, s82, 258048\n"
    "buffer_load_dwordx4 v[240:243], v113, s[12:15], s81 offen offset:0 sc1\n"
    "buffer_load_dwordx4 v[244:247], v113, s[12:15], s81 offen offset:256 sc1\n"
    "s_waitcnt vmcnt(0)\n"
    "v_max3_u32 v239, v239, v244, v245\n"
    "v_max3_u32 v239, v239, v246, v247\n"
    "v_max3_u32 v239, v239, v240, v241\n"
    "v_max3_u32 v239, v239, v242, v243\n"
    "ds_read_b64_tr_b16 v[248:249], v115 offset:12288\n"
    "ds_read_b64_tr_b16 v[250:251], v115 offset:12800\n"
    "s_waitcnt lgkmcnt(0)\n"
    "v_mfma_f32_16x16x32_bf16 v[156:159], v[240:243], v[248:251], v[156:159]\n"
    "v_mfma_f32_16x16x32_bf16 v[160:163], v[244:247], v[248:251], v[160:163]\n"
    "ds_read_b64_tr_b16 v[248:249], v116 offset:12288\n"
    "ds_read_b64_tr_b16 v[250:251], v116 offset:12800\n"
    "s_waitcnt lgkmcnt(0)\n"
    "v_mfma_f32_16x16x32_bf16 v[164:167], v[240:243], v[248:251], v[164:167]\n"
    "v_mfma_f32_16x16x32_bf16 v[168:171], v[244:247], v[248:251], v[168:171]\n"
    "ds_read_b64_tr_b16 v[248:249], v117 offset:12288\n"
    "ds_read_b64_tr_b16 v[250:251], v117 offset:12800\n"
    "s_waitcnt lgkmcnt(0)\n"
    "v_mfma_f32_16x16x32_bf16 v[172:175], v[240:243], v[248:251], v[172:175]\n"
    "v_mfma_f32_16x16x32_bf16 v[176:179], v[244:247], v[248:251], v[176:179]\n"
    "ds_read_b64_tr_b16 v[248:249], v118 offset:12288\n"
    "ds_read_b64_tr_b16 v[250:251], v118 offset:12800\n"
    "s_waitcnt lgkmcnt(0)\n"
    "v_mfma_f32_16x16x32_bf16 v[180:183], v[240:243], v[248:251], v[180:183]\n"
    "v_mfma_f32_16x16x32_bf16 v[184:187], v[244:247], v[248:251], v[184:187]\n"
    "s_add_u32 s81, s82, 286720\n"
    "buffer_load_dwordx4 v[240:243], v113, s[12:15], s81 offen offset:0 sc1\n"
    "buffer_load_dwordx4 v[244:247], v113, s[12:15], s81 offen offset:256 sc1\n"
    "s_waitcnt vmcnt(0)\n"
    "v_max3_u32 v239, v239, v244, v245\n"
    "v_max3_u32 v239, v239, v246, v247\n"
    "v_max3_u32 v239, v239, v240, v241\n"
    "v_max3_u32 v239, v239, v242, v243\n"
    "ds_read_b64_tr_b16 v[248:249], v115 offset:16384\n"
    "ds_read_b64_tr_b16 v[250:251], v115 offset:16896\n"
    "s_waitcnt lgkmcnt(0)\n"
    "v_mfma_f32_16x16x32_bf16 v[156:159], v[240:243], v[248:251], v[156:159]\n"
    "v_mfma_f32_16x16x32_bf16 v[160:163], v[244:247], v[248:251], v[160:163]\n"
    "ds_read_b64_tr_b16 v[248:249], v116 offset:16384\n"
    "ds_read_b64_tr_b16 v[250:251], v116 offset:16896\n"
    "s_waitcnt lgkmcnt(0)\n"
    "v_mfma_f32_16x16x32_bf16 v[164:167], v[240:243], v[248:251], v[164:167]\n"
    "v_mfma_f32_16x16x32_bf16 v[168:171], v[244:247], v[248:251], v[168:171]\n"
    "ds_read_b64_tr_b16 v[248:249], v117 offset:16384\n"
    "ds_read_b64_tr_b16 v[250:251], v117 offset:16896\n"
    "s_waitcnt lgkmcnt(0)\n"
    "v_mfma_f32_16x16x32_bf16 v[172:175], v[240:243], v[248:251], v[172:175]\n"
    "v_mfma_f32_16x16x32_bf16 v[176:179], v[244:247], v[248:251], v[176:179]\n"
    "ds_read_b64_tr_b16 v[248:249], v118 offset:16384\n"
    "ds_read_b64_tr_b16 v[250:251], v118 offset:16896\n"
    "s_waitcnt lgkmcnt(0)\n"
    "v_mfma_f32_16x16x32_bf16 v[180:183], v[240:243], v[248:251], v[180:183]\n"
    "v_mfma_f32_16x16x32_bf16 v[184:187], v[244:247], v[248:251], v[184:187]\n"
    "s_add_u32 s81, s82, 315392\n"
    "buffer_load_dwordx4 v[240:243], v113, s[12:15], s81 offen offset:0 sc1\n"
    "buffer_load_dwordx4 v[244:247], v113, s[12:15], s81 offen offset:256 sc1\n"
    "s_waitcnt vmcnt(0)\n"
    "v_max3_u32 v239, v239, v244, v245\n"
    "v_max3_u32 v239, v239, v246, v247\n"
    "v_max3_u32 v239, v239, v240, v241\n"
    "v_max3_u32 v239, v239, v242, v243\n"
    "ds_read_b64_tr_b16 v[248:249], v115 offset:20480\n"
    "ds_read_b64_tr_b16 v[250:251], v115 offset:20992\n"
    "s_waitcnt lgkmcnt(0)\n"
    "v_mfma_f32_16x16x32_bf16 v[156:159], v[240:243], v[248:251], v[156:159]\n"
    "v_mfma_f32_16x16x32_bf16 v[160:163], v[244:247], v[248:251], v[160:163]\n"
    "ds_read_b64_tr_b16 v[248:249], v116 offset:20480\n"
    "ds_read_b64_tr_b16 v[250:251], v116 offset:20992\n"
    "s_waitcnt lgkmcnt(0)\n"
    "v_mfma_f32_16x16x32_bf16 v[164:167], v[240:243], v[248:251], v[164:167]\n"
    "v_mfma_f32_16x16x32_bf16 v[168:171], v[244:247], v[248:251], v[168:171]\n"
    "ds_read_b64_tr_b16 v[248:249], v117 offset:20480\n"
    "ds_read_b64_tr_b16 v[250:251], v117 offset:20992\n"
    "s_waitcnt lgkmcnt(0)\n"
    "v_mfma_f32_16x16x32_bf16 v[172:175], v[240:243], v[248:251], v[172:175]\n"
    "v_mfma_f32_16x16x32_bf16 v[176:179], v[244:247], v[248:251], v[176:179]\n"
    "ds_read_b64_tr_b16 v[248:249], v118 offset:20480\n"
    "ds_read_b64_tr_b16 v[250:251], v118 offset:20992\n"
    "s_waitcnt lgkmcnt(0)\n"
    "v_mfma_f32_16x16x32_bf16 v[180:183], v[240:243], v[248:251], v[180:183]\n"
    "v_mfma_f32_16x16x32_bf16 v[184:187], v[244:247], v[248:251], v[184:187]\n"
    "s_add_u32 s81, s82, 344064\n"
    "buffer_load_dwordx4 v[240:243], v113, s[12:15], s81 offen offset:0 sc1\n"
    "buffer_load_dwordx4 v[244:247], v113, s[12:15], s81 offen offset:256 sc1\n"
    "s_waitcnt vmcnt(0)\n"
    "v_max3_u32 v239, v239, v244, v245\n"
    "v_max3_u32 v239, v239, v246, v247\n"
    "v_max3_u32 v239, v239, v240, v241\n"
    "v_max3_u32 v239, v239, v242, v243\n"
    "ds_read_b64_tr_b16 v[248:249], v115 offset:24576\n"
    "ds_read_b64_tr_b16 v[250:251], v115 offset:25088\n"
    "s_waitcnt lgkmcnt(0)\n"
    "v_mfma_f32_16x16x32_bf16 v[156:159], v[240:243], v[248:251], v[156:159]\n"
    "v_mfma_f32_16x16x32_bf16 v[160:163], v[244:247], v[248:251], v[160:163]\n"
    "ds_read_b64_tr_b16 v[248:249], v116 offset:24576\n"
    "ds_read_b64_tr_b16 v[250:251], v116 offset:25088\n"
    "s_waitcnt lgkmcnt(0)\n"
    "v_mfma_f32_16x16x32_bf16 v[164:167], v[240:243], v[248:251], v[164:167]\n"
    "v_mfma_f32_16x16x32_bf16 v[168:171], v[244:247], v[248:251], v[168:171]\n"
    "ds_read_b64_tr_b16 v[248:249], v117 offset:24576\n"
    "ds_read_b64_tr_b16 v[250:251], v117 offset:25088\n"
    "s_waitcnt lgkmcnt(0)\n"
    "v_mfma_f32_16x16x32_bf16 v[172:175], v[240:243], v[248:251], v[172:175]\n"
    "v_mfma_f32_16x16x32_bf16 v[176:179], v[244:247], v[248:251], v[176:179]\n"
    "ds_read_b64_tr_b16 v[248:249], v118 offset:24576\n"
    "ds_read_b64_tr_b16 v[250:251], v118 offset:25088\n"
    "s_waitcnt lgkmcnt(0)\n"
    "v_mfma_f32_16x16x32_bf16 v[180:183], v[240:243], v[248:251], v[180:183]\n"
    "v_mfma_f32_16x16x32_bf16 v[184:187], v[244:247], v[248:251], v[184:187]\n"
    "s_add_u32 s81, s82, 372736\n"
    "buffer_load_dwordx4 v[240:243], v113, s[12:15], s81 offen offset:0 sc1\n"
    "buffer_load_dwordx4 v[244:247], v113, s[12:15], s81 offen offset:256 sc1\n"
    "s_waitcnt vmcnt(0)\n"
    "v_max3_u32 v239, v239, v244, v245\n"
    "v_max3_u32 v239, v239, v246, v247\n"
    "v_max3_u32 v239, v239, v240, v241\n"
    "v_max3_u32 v239, v239, v242, v243\n"
    "ds_read_b64_tr_b16 v[248:249], v115 offset:28672\n"
    "ds_read_b64_tr_b16 v[250:251], v115 offset:29184\n"
    "s_waitcnt lgkmcnt(0)\n"
    "v_mfma_f32_16x16x32_bf16 v[156:159], v[240:243], v[248:251], v[156:159]\n"
    "v_mfma_f32_16x16x32_bf16 v[160:163], v[244:247], v[248:251], v[160:163]\n"
    "ds_read_b64_tr_b16 v[248:249], v116 offset:28672\n"
    "ds_read_b64_tr_b16 v[250:251], v116 offset:29184\n"
    "s_waitcnt lgkmcnt(0)\n"
    "v_mfma_f32_16x16x32_bf16 v[164:167], v[240:243], v[248:251], v[164:167]\n"
    "v_mfma_f32_16x16x32_bf16 v[168:171], v[244:247], v[248:251], v[168:171]\n"
    "ds_read_b64_tr_b16 v[248:249], v117 offset:28672\n"
    "ds_read_b64_tr_b16 v[250:251], v117 offset:29184\n"
    "s_waitcnt lgkmcnt(0)\n"
    "v_mfma_f32_16x16x32_bf16 v[172:175], v[240:243], v[248:251], v[172:175]\n"
    "v_mfma_f32_16x16x32_bf16 v[176:179], v[244:247], v[248:251], v[176:179]\n"
    "ds_read_b64_tr_b16 v[248:249], v118 offset:28672\n"
    "ds_read_b64_tr_b16 v[250:251], v118 offset:29184\n"
    "s_waitcnt lgkmcnt(0)\n"
    "v_mfma_f32_16x16x32_bf16 v[180:183], v[240:243], v[248:251], v[180:183]\n"
    "v_mfma_f32_16x16x32_bf16 v[184:187], v[244:247], v[248:251], v[184:187]\n"
    "v_cmp_eq_u32 vcc, -1, v239\n"
    "s_cbranch_vccnz .Lredo_45\n"
    ".Lredook_46:\n"
    "ds_write_b128 v119, v[164:167] offset:0\n"
    "ds_write_b128 v119, v[168:171] offset:1024\n"
    "ds_write_b128 v119, v[172:175] offset:2048\n"
    "ds_write_b128 v119, v[176:179] offset:3072\n"
    "ds_write_b128 v119, v[180:183] offset:4096\n"
    "s_nop 0\n"
    "ds_write_b128 v119, v[184:187] offset:5120\n"
    "s_waitcnt vmcnt(0) lgkmcnt(0)\n"
    "s_branch .Lback_44\n"
    ".Lredo_45:\n"
    "s_waitcnt vmcnt(0) lgkmcnt(0)\n"
    "s_sub_u32 s60, s60, 1\n"
    "s_cbranch_scc0 .Lclean_43\n"
    "s_mov_b32 s60, 0\n"
    "s_branch .Lredook_46\n"
    ".Lredo_47:\n"
    "s_waitcnt vmcnt(0) lgkmcnt(0)\n"
    "s_branch .Lclean_43\n"
    ".Lretry_50:\n"
    "s_sub_u32 s60, s60, 1\n"
    "s_cbranch_scc1 .Lretry_50_giveup\n"
    "buffer_load_dwordx4 v[166:169], v245, s[12:15], s70 offen sc1\n"
    "buffer_load_dwordx4 v[170:173], v245, s[12:15], s71 offen sc1\n"
    "s_waitcnt vmcnt(0)\n"
    "v_max3_u32 v174, v166, v167, v168\n"
    "v_max3_u32 v174, v174, v169, v170\n"
    "v_max3_u32 v174, v174, v171, v172\n"
    "v_max_u32 v174, v174, v173\n"
    "v_cmp_eq_u32 vcc, -1, v174\n"
    "s_cbranch_vccnz .Lretry_50\n"
    "s_branch .Ly_51\n"
    ".Lretry_50_giveup:\n"
    "s_mov_b32 s60, 0\n"
    "s_waitcnt vmcnt(0)\n"
    "s_branch .Ly_51\n"
    ".Lretry_54:\n"
    "s_sub_u32 s60, s60, 1\n"
    "s_cbranch_scc1 .Lretry_54_giveup\n"
    "buffer_load_dwordx4 v[220:223], v114, s[12:15], s78 offen sc1\n"
    "buffer_load_dwordx4 v[224:227], v114, s[12:15], s79 offen sc1\n"
    "s_waitcnt vmcnt(0)\n"
    "v_max3_u32 v236, v220, v221, v222\n"
    "v_max3_u32 v236, v236, v223, v224\n"
    "v_max3_u32 v236, v236, v225, v226\n"
    "v_max_u32 v236, v236, v227\n"
    "v_cmp_eq_u32 vcc, -1, v236\n"
    "s_cbranch_vccnz .Lretry_54\n"
    "s_branch .Lx1_55\n"
    ".Lretry_54_giveup:\n"
    "s_mov_b32 s60, 0\n"
    "s_waitcnt vmcnt(0)\n"
    "s_branch .Lx1_55\n"
    ".Lretry_58:\n"
    "s_sub_u32 s60, s60, 1\n"
    "s_cbranch_scc1 .Lretry_58_giveup\n"
    "buffer_load_dwordx4 v[188:191], v115, s[12:15], s70 offen sc1\n"
    "buffer_load_dwordx4 v[192:195], v123, s[12:15], s71 offen sc1\n"
    "s_waitcnt vmcnt(0)\n"
    "v_max3_u32 v244, v188, v189, v190\n"
    "v_max3_u32 v244, v244, v191, v192\n"
    "v_max3_u32 v244, v244, v193, v194\n"
    "v_max_u32 v244, v244, v195\n"
    "v_cmp_eq_u32 vcc, -1, v244\n"
    "s_cbranch_vccnz .Lretry_58\n"
    "s_branch .Lz_59\n"
    ".Lretry_58_giveup:\n"
    "s_mov_b32 s60, 0\n"
    "s_waitcnt vmcnt(0)\n"
    "s_branch .Lz_59\n"
    ".Lg4retry_63:\n"
    "s_sub_u32 s60, s60, 1\n"
    "s_cbranch_scc1 .Lg4retry_63_giveup\n"
    "buffer_load_dword v124, v125, s[12:15], s71 offen sc1\n"
    "s_waitcnt vmcnt(0)\n"
    "v_cmp_eq_u32 vcc, -1, v124\n"
    "s_cbranch_vccnz .Lg4retry_63\n"
    "s_branch .Lg4ok_64\n"
    ".Lg4retry_63_giveup:\n"
    "s_mov_b32 s60, 0\n"
    "s_waitcnt vmcnt(0)\n"
    "s_branch .Lg4ok_64\n"
    ".Lclean_67:\n"
    "v_mov_b32 v107, 0\n"
    "s_add_u32 s81, s71, 0\n"
    "buffer_load_dwordx4 v[240:243], v113, s[12:15], s81 offen offset:512 sc1\n"
    "buffer_load_dwordx4 v[244:247], v114, s[12:15], s81 offen offset:768 sc1\n"
    "s_waitcnt vmcnt(0)\n"
    "v_max3_u32 v107, v107, v244, v245\n"
    "v_max3_u32 v107, v107, v246, v247\n"
    "v_max3_u32 v107, v107, v240, v241\n"
    "v_max3_u32 v107, v107, v242, v243\n"
    "v_cndmask_b32 v228, 0, v244, s[4:5]\n"
    "v_cndmask_b32 v229, 0, v245, s[4:5]\n"
    "v_cndmask_b32 v230, 0, v246, s[4:5]\n"
    "v_cndmask_b32 v231, 0, v247, s[4:5]\n"
    "v_mfma_f32_16x16x32_bf16 v[124:127], a[0:3], v[240:243], 0\n"
    "s_nop 0\n"
    "v_mfma_f32_16x16x32_bf16 v[128:131], a[0:3], v[228:231], 0\n"
    "v_mfma_f32_16x16x32_bf16 v[132:135], a[4:7], v[240:243], 0\n"
    "v_mfma_f32_16x16x32_bf16 v[136:139], a[4:7], v[228:231], 0\n"
    "v_mfma_f32_16x16x32_bf16 v[140:143], a[8:11], v[240:243], 0\n"
    "v_mfma_f32_16x16x32_bf16 v[144:147], a[8:11], v[228:231], 0\n"
    "v_mfma_f32_16x16x32_bf16 v[148:151], a[12:15], v[240:243], 0\n"
    "v_mfma_f32_16x16x32_bf16 v[152:155], a[12:15], v[228:231], 0\n"
    "s_add_u32 s81, s71, 28672\n"
    "buffer_load_dwordx4 v[240:243], v113, s[12:15], s81 offen offset:512 sc1\n"
    "s_waitcnt vmcnt(0)\n"
    "v_max3_u32 v107, v107, v240, v241\n"
    "v_max3_u32 v107, v107, v242, v243\n"
    "v_cndmask_b32 v228, v244, 0, s[4:5]\n"
    "v_cndmask_b32 v229, v245, 0, s[4:5]\n"
    "v_cndmask_b32 v230, v246, 0, s[4:5]\n"
    "v_cndmask_b32 v231, v247, 0, s[4:5]\n"
    "v_mfma_f32_16x16x32_bf16 v[124:127], a[16:19], v[240:243], v[124:127]\n"
    "s_nop 0\n"
    "v_mfma_f32_16x16x32_bf16 v[128:131], a[16:19], v[228:231], v[128:131]\n"
    "v_mfma_f32_16x16x32_bf16 v[132:135], a[20:23], v[240:243], v[132:135]\n"
    "v_mfma_f32_16x16x32_bf16 v[136:139], a[20:23], v[228:231], v[136:139]\n"
    "v_mfma_f32_16x16x32_bf16 v[140:143], a[24:27], v[240:243], v[140:143]\n"
    "v_mfma_f32_16x16x32_bf16 v[144:147], a[24:27], v[228:231], v[144:147]\n"
    "v_mfma_f32_16x16x32_bf16 v[148:151], a[28:31], v[240:243], v[148:151]\n"
    "v_mfma_f32_16x16x32_bf16 v[152:155], a[28:31], v[228:231], v[152:155]\n"
    "s_add_u32 s81, s71, 57344\n"
    "buffer_load_dwordx4 v[240:243], v113, s[12:15], s81 offen offset:512 sc1\n"
    "buffer_load_dwordx4 v[244:247], v114, s[12:15], s81 offen offset:768 sc1\n"
    "s_waitcnt vmcnt(0)\n"
    "v_max3_u32 v107, v107, v244, v245\n"
    "v_max3_u32 v107, v107, v246, v247\n"
    "v_max3_u32 v107, v107, v240, v241\n"
    "v_max3_u32 v107, v107, v242, v243\n"
    "v_cndmask_b32 v228, 0, v244, s[4:5]\n"
    "v_cndmask_b32 v229, 0, v245, s[4:5]\n"
    "v_cndmask_b32 v230, 0, v246, s[4:5]\n"
    "v_cndmask_b32 v231, 0, v247, s[4:5]\n"
    "v_mfma_f32_16x16x32_bf16 v[124:127], a[32:35], v[240:243], v[124:127]\n"
    "s_nop 0\n"
    "v_mfma_f32_16x16x32_bf16 v[128:131], a[32:35], v[228:231], v[128:131]\n"
    "v_mfma_f32_16x16x32_bf16 v[132:135], a[36:39], v[240:243], v[132:135]\n"
    "v_mfma_f32_16x16x32_bf16 v[136:139], a[36:39], v[228:231], v[136:139]\n"
    "v_mfma_f32_16x16x32_bf16 v[140:143], a[40:43], v[240:243], v[140:143]\n"
    "v_mfma_f32_16x16x32_bf16 v[144:147], a[40:43], v[228:231], v[144:147]\n"
    "v_mfma_f32_16x16x32_bf16 v[148:151], a[44:47], v[240:243], v[148:151]\n"
    "v_mfma_f32_16x16x32_bf16 v[152:155], a[44:47], v[228:231], v[152:155]\n"
    "s_add_u32 s81, s71, 86016\n"
    "buffer_load_dwordx4 v[240:243], v113, s[12:15], s81 offen offset:512 sc1\n"
    "s_waitcnt vmcnt(0)\n"
    "v_max3_u32 v107, v107, v240, v241\n"
    "v_max3_u32 v107, v107, v242, v243\n"
    "v_cndmask_b32 v228, v244, 0, s[4:5]\n"
    "v_cndmask_b32 v229, v245, 0, s[4:5]\n"
    "v_cndmask_b32 v230, v246, 0, s[4:5]\n"
    "v_cndmask_b32 v231, v247, 0, s[4:5]\n"
    "v_mfma_f32_16x16x32_bf16 v[124:127], a[48:51], v[240:243], v[124:127]\n"
    "s_nop 0\n"
    "v_mfma_f32_16x16x32_bf16 v[128:131], a[48:51], v[228:231], v[128:131]\n"
    "v_mfma_f32_16x16x32_bf16 v[132:135], a[52:55], v[240:243], v[132:135]\n"
    "v_mfma_f32_16x16x32_bf16 v[136:139], a[52:55], v[228:231], v[136:139]\n"
    "v_mfma_f32_16x16x32_bf16 v[140:143], a[56:59], v[240:243], v[140:143]\n"
    "v_mfma_f32_16x16x32_bf16 v[144:147], a[56:59], v[228:231], v[144:147]\n"
    "v_mfma_f32_16x16x32_bf16 v[148:151], a[60:63], v[240:243], v[148:151]\n"
    "v_mfma_f32_16x16x32_bf16 v[152:155], a[60:63], v[228:231], v[152:155]\n"
    "s_add_u32 s81, s71, 114688\n"
    "buffer_load_dwordx4 v[240:243], v113, s[12:15], s81 offen offset:512 sc1\n"
    "buffer_load_dwordx4 v[244:247], v114, s[12:15], s81 offen offset:768 sc1\n"
    "s_waitcnt vmcnt(0)\n"
    "v_max3_u32 v107, v107, v244, v245\n"
    "v_max3_u32 v107, v107, v246, v247\n"
    "v_max3_u32 v107, v107, v240, v241\n"
    "v_max3_u32 v107, v107, v242, v243\n"
    "v_cndmask_b32 v228, 0, v244, s[4:5]\n"
    "v_cndmask_b32 v229, 0, v245, s[4:5]\n"
    "v_cndmask_b32 v230, 0, v246, s[4:5]\n"
    "v_cndmask_b32 v231, 0, v247, s[4:5]\n"
    "v_mfma_f32_16x16x32_bf16 v[124:127], a[64:67], v[240:243], v[124:127]\n"
    "s_nop 0\n"
    "v_mfma_f32_16x16x32_bf16 v[128:131], a[64:67], v[228:231], v[128:131]\n"
    "v_mfma_f32_16x16x32_bf16 v[132:135], a[68:71], v[240:243], v[132:135]\n"
    "v_mfma_f32_16x16x32_bf16 v[136:139], a[68:71], v[228:231], v[136:139]\n"
    "v_mfma_f32_16x16x32_bf16 v[140:143], a[72:75], v[240:243], v[140:143]\n"
    "v_mfma_f32_16x16x32_bf16 v[144:147], a[72:75], v[228:231], v[144:147]\n"
    "v_mfma_f32_16x16x32_bf16 v[148:151], a[76:79], v[240:243], v[148:151]\n"
    "v_mfma_f32_16x16x32_bf16 v[152:155], a[76:79], v[228:231], v[152:155]\n"
    "s_add_u32 s81, s71, 143360\n"
    "buffer_load_dwordx4 v[240:243], v113, s[12:15], s81 offen offset:512 sc1\n"
    "s_waitcnt vmcnt(0)\n"
    "v_max3_u32 v107, v107, v240, v241\n"
    "v_max3_u32 v107, v107, v242, v243\n"
    "v_cndmask_b32 v228, v244, 0, s[4:5]\n"
    "v_cndmask_b32 v229, v245, 0, s[4:5]\n"
    "v_cndmask_b32 v230, v246, 0, s[4:5]\n"
    "v_cndmask_b32 v231, v247, 0, s[4:5]\n"
    "v_mfma_f32_16x16x32_bf16 v[124:127], a[80:83], v[240:243], v[124:127]\n"
    "s_nop 0\n"
    "v_mfma_f32_16x16x32_bf16 v[128:131], a[80:83], v[228:231], v[128:131]\n"
    "v_mfma_f32_16x16x32_bf16 v[132:135], a[84:87], v[240:243], v[132:135]\n"
    "v_mfma_f32_16x16x32_bf16 v[136:139], a[84:87], v[228:231], v[136:139]\n"
    "v_mfma_f32_16x16x32_bf16 v[140:143], a[88:91], v[240:243], v[140:143]\n"
    "v_mfma_f32_16x16x32_bf16 v[144:147], a[88:91], v[228:231], v[144:147]\n"
    "v_mfma_f32_16x16x32_bf16 v[148:151], a[92:95], v[240:243], v[148:151]\n"
    "v_mfma_f32_16x16x32_bf16 v[152:155], a[92:95], v[228:231], v[152:155]\n"
    "s_add_u32 s81, s71, 172032\n"
    "buffer_load_dwordx4 v[240:243], v113, s[12:15], s81 offen offset:512 sc1\n"
    "buffer_load_dwordx4 v[244:247], v114, s[12:15], s81 offen offset:768 sc1\n"
    "s_waitcnt vmcnt(0)\n"
    "v_max3_u32 v107, v107, v244, v245\n"
    "v_max3_u32 v107, v107, v246, v247\n"
    "v_max3_u32 v107, v107, v240, v241\n"
    "v_max3_u32 v107, v107, v242, v243\n"
    "v_cndmask_b32 v228, 0, v244, s[4:5]\n"
    "v_cndmask_b32 v229, 0, v245, s[4:5]\n"
    "v_cndmask_b32 v230, 0, v246, s[4:5]\n"
    "v_cndmask_b32 v231, 0, v247, s[4:5]\n"
    "v_mfma_f32_16x16x32_bf16 v[124:127], a[96:99], v[240:243], v[124:127]\n"
    "s_nop 0\n"
    "v_mfma_f32_16x16x32_bf16 v[128:131], a[96:99], v[228:231], v[128:131]\n"
    "v_mfma_f32_16x16x32_bf16 v[132:135], a[100:103], v[240:243], v[132:135]\n"
    "v_mfma_f32_16x16x32_bf16 v[136:139], a[100:103], v[228:231], v[136:139]\n"
    "v_mfma_f32_16x16x32_bf16 v[140:143], a[104:107], v[240:243], v[140:143]\n"
    "v_mfma_f32_16x16x32_bf16 v[144:147], a[104:107], v[228:231], v[144:147]\n"
    "v_mfma_f32_16x16x32_bf16 v[148:151], a[108:111], v[240:243], v[148:151]\n"
    "v_mfma_f32_16x16x32_bf16 v[152:155], a[108:111], v[228:231], v[152:155]\n"
    "s_add_u32 s81, s71, 200704\n"
    "buffer_load_dwordx4 v[240:243], v113, s[12:15], s81 offen offset:512 sc1\n"
    "s_waitcnt vmcnt(0)\n"
    "v_max3_u32 v107, v107, v240, v241\n"
    "v_max3_u32 v107, v107, v242, v243\n"
    "v_cndmask_b32 v228, v244, 0, s[4:5]\n"
    "v_cndmask_b32 v229, v245, 0, s[4:5]\n"
    "v_cndmask_b32 v230, v246, 0, s[4:5]\n"
    "v_cndmask_b32 v231, v247, 0, s[4:5]\n"
    "v_mfma_f32_16x16x32_bf16 v[124:127], a[112:115], v[240:243], v[124:127]\n"
    "s_nop 0\n"
    "v_mfma_f32_16x16x32_bf16 v[128:131], a[112:115], v[228:231], v[128:131]\n"
    "v_mfma_f32_16x16x32_bf16 v[132:135], a[116:119], v[240:243], v[132:135]\n"
    "v_mfma_f32_16x16x32_bf16 v[136:139], a[116:119], v[228:231], v[136:139]\n"
    "v_mfma_f32_16x16x32_bf16 v[140:143], a[120:123], v[240:243], v[140:143]\n"
    "v_mfma_f32_16x16x32_bf16 v[144:147], a[120:123], v[228:231], v[144:147]\n"
    "v_mfma_f32_16x16x32_bf16 v[148:151], a[124:127], v[240:243], v[148:151]\n"
    "v_mfma_f32_16x16x32_bf16 v[152:155], a[124:127], v[228:231], v[152:155]\n"
    "s_add_u32 s81, s71, 229376\n"
    "buffer_load_dwordx4 v[240:243], v113, s[12:15], s81 offen offset:512 sc1\n"
    "buffer_load_dwordx4 v[244:247], v114, s[12:15], s81 offen offset:768 sc1\n"
    "s_waitcnt vmcnt(0)\n"
    "v_max3_u32 v107, v107, v244, v245\n"
    "v_max3_u32 v107, v107, v246, v247\n"
    "v_max3_u32 v107, v107, v240, v241\n"
    "v_max3_u32 v107, v107, v242, v243\n"
    "v_cndmask_b32 v228, 0, v244, s[4:5]\n"
    "v_cndmask_b32 v229, 0, v245, s[4:5]\n"
    "v_cndmask_b32 v230, 0, v246, s[4:5]\n"
    "v_cndmask_b32 v231, 0, v247, s[4:5]\n"
    "v_mfma_f32_16x16x32_bf16 v[124:127], a[128:131], v[240:243], v[124:127]\n"
    "s_nop 0\n"
    "v_mfma_f32_16x16x32_bf16 v[128:131], a[128:131], v[228:231], v[128:131]\n"
    "v_mfma_f32_16x16x32_bf16 v[132:135], a[132:135], v[240:243], v[132:135]\n"
    "v_mfma_f32_16x16x32_bf16 v[136:139], a[132:135], v[228:231], v[136:139]\n"
    "v_mfma_f32_16x16x32_bf16 v[140:143], a[136:139], v[240:243], v[140:143]\n"
    "v_mfma_f32_16x16x32_bf16 v[144:147], a[136:139], v[228:231], v[144:147]\n"
    "v_mfma_f32_16x16x32_bf16 v[148:151], a[140:143], v[240:243], v[148:151]\n"
    "v_mfma_f32_16x16x32_bf16 v[152:155], a[140:143], v[228:231], v[152:155]\n"
    "s_add_u32 s81, s71, 258048\n"
    "buffer_load_dwordx4 v[240:243], v113, s[12:15], s81 offen offset:512 sc1\n"
    "s_waitcnt vmcnt(0)\n"
    "v_max3_u32 v107, v107, v240, v241\n"
    "v_max3_u32 v107, v107, v242, v243\n"
    "v_cndmask_b32 v228, v244, 0, s[4:5]\n"
    "v_cndmask_b32 v229, v245, 0, s[4:5]\n"
    "v_cndmask_b32 v230, v246, 0, s[4:5]\n"
    "v_cndmask_b32 v231, v247, 0, s[4:5]\n"
    "v_mfma_f32_16x16x32_bf16 v[124:127], a[144:147], v[240:243], v[124:127]\n"
    "s_nop 0\n"
    "v_mfma_f32_16x16x32_bf16 v[128:131], a[144:147], v[228:231], v[128:131]\n"
    "v_mfma_f32_16x16x32_bf16 v[132:135], a[148:151], v[240:243], v[132:135]\n"
    "v_mfma_f32_16x16x32_bf16 v[136:139], a[148:151], v[228:231], v[136:139]\n"
    "v_mfma_f32_16x16x32_bf16 v[140:143], a[152:155], v[240:243], v[140:143]\n"
    "v_mfma_f32_16x16x32_bf16 v[144:147], a[152:155], v[228:231], v[144:147]\n"
    "v_mfma_f32_16x16x32_bf16 v[148:151], a[156:159], v[240:243], v[148:151]\n"
    "v_mfma_f32_16x16x32_bf16 v[152:155], a[156:159], v[228:231], v[152:155]\n"
    "s_add_u32 s81, s71, 286720\n"
    "buffer_load_dwordx4 v[240:243], v113, s[12:15], s81 offen offset:512 sc1\n"
    "buffer_load_dwordx4 v[244:247], v114, s[12:15], s81 offen offset:768 sc1\n"
    "s_waitcnt vmcnt(0)\n"
    "v_max3_u32 v107, v107, v244, v245\n"
    "v_max3_u32 v107, v107, v246, v247\n"
    "v_max3_u32 v107, v107, v240, v241\n"
    "v_max3_u32 v107, v107, v242, v243\n"
    "v_cndmask_b32 v228, 0, v244, s[4:5]\n"
    "v_cndmask_b32 v229, 0, v245, s[4:5]\n"
    "v_cndmask_b32 v230, 0, v246, s[4:5]\n"
    "v_cndmask_b32 v231, 0, v247, s[4:5]\n"
    "v_mfma_f32_16x16x32_bf16 v[124:127], a[160:163], v[240:243], v[124:127]\n"
    "s_nop 0\n"
    "v_mfma_f32_16x16x32_bf16 v[128:131], a[160:163], v[228:231], v[128:131]\n"
    "v_mfma_f32_16x16x32_bf16 v[132:135], a[164:167], v[240:243], v[132:135]\n"
    "v_mfma_f32_16x16x32_bf16 v[136:139], a[164:167], v[228:231], v[136:139]\n"
    "v_mfma_f32_16x16x32_bf16 v[140:143], a[168:171], v[240:243], v[140:143]\n"
    "v_mfma_f32_16x16x32_bf16 v[144:147], a[168:171], v[228:231], v[144:147]\n"
    "v_mfma_f32_16x16x32_bf16 v[148:151], a[172:175], v[240:243], v[148:151]\n"
    "v_mfma_f32_16x16x32_bf16 v[152:155], a[172:175], v[228:231], v[152:155]\n"
    "s_add_u32 s81, s71, 315392\n"
    "buffer_load_dwordx4 v[240:243], v113, s[12:15], s81 offen offset:512 sc1\n"
    "s_waitcnt vmcnt(0)\n"
    "v_max3_u32 v107, v107, v240, v241\n"
    "v_max3_u32 v107, v107, v242, v243\n"
    "v_cndmask_b32 v228, v244, 0, s[4:5]\n"
    "v_cndmask_b32 v229, v245, 0, s[4:5]\n"
    "v_cndmask_b32 v230, v246, 0, s[4:5]\n"
    "v_cndmask_b32 v231, v247, 0, s[4:5]\n"
    "v_mfma_f32_16x16x32_bf16 v[124:127], a[176:179], v[240:243], v[124:127]\n"
    "s_nop 0\n"
    "v_mfma_f32_16x16x32_bf16 v[128:131], a[176:179], v[228:231], v[128:131]\n"
    "v_mfma_f32_16x16x32_bf16 v[132:135], a[180:183], v[240:243], v[132:135]\n"
    "v_mfma_f32_16x16x32_bf16 v[136:139], a[180:183], v[228:231], v[136:139]\n"
    "v_mfma_f32_16x16x32_bf16 v[140:143], a[184:187], v[240:243], v[140:143]\n"
    "v_mfma_f32_16x16x32_bf16 v[144:147], a[184:187], v[228:231], v[144:147]\n"
    "v_mfma_f32_16x16x32_bf16 v[148:151], a[188:191], v[240:243], v[148:151]\n"
    "v_mfma_f32_16x16x32_bf16 v[152:155], a[188:191], v[228:231], v[152:155]\n"
    "s_add_u32 s81, s71, 344064\n"
    "buffer_load_dwordx4 v[240:243], v113, s[12:15], s81 offen offset:512 sc1\n"
    "buffer_load_dwordx4 v[244:247], v114, s[12:15], s81 offen offset:768 sc1\n"
    "s_waitcnt vmcnt(0)\n"
    "v_max3_u32 v107, v107, v244, v245\n"
    "v_max3_u32 v107, v107, v246, v247\n"
    "v_max3_u32 v107, v107, v240, v241\n"
    "v_max3_u32 v107, v107, v242, v243\n"
    "v_cndmask_b32 v228, 0, v244, s[4:5]\n"
    "v_cndmask_b32 v229, 0, v245, s[4:5]\n"
    "v_cndmask_b32 v230, 0, v246, s[4:5]\n"
    "v_cndmask_b32 v231, 0, v247, s[4:5]\n"
    "v_mfma_f32_16x16x32_bf16 v[124:127], a[192:195], v[240:243], v[124:127]\n"
    "s_nop 0\n"
    "v_mfma_f32_16x16x32_bf16 v[128:131], a[192:195], v[228:231], v[128:131]\n"
    "v_mfma_f32_16x16x32_bf16 v[132:135], a[196:199], v[240:243], v[132:135]\n"
    "v_mfma_f32_16x16x32_bf16 v[136:139], a[196:199], v[228:231], v[136:139]\n"
    "v_mfma_f32_16x16x32_bf16 v[140:143], a[200:203], v[240:243], v[140:143]\n"
    "v_mfma_f32_16x16x32_bf16 v[144:147], a[200:203], v[228:231], v[144:147]\n"
    "v_mfma_f32_16x16x32_bf16 v[148:151], a[204:207], v[240:243], v[148:151]\n"
    "v_mfma_f32_16x16x32_bf16 v[152:155], a[204:207], v[228:231], v[152:155]\n"
    "s_add_u32 s81, s71, 372736\n"
    "buffer_load_dwordx4 v[240:243], v113, s[12:15], s81 offen offset:512 sc1\n"
    "s_waitcnt vmcnt(0)\n"
    "v_max3_u32 v107, v107, v240, v241\n"
    "v_max3_u32 v107, v107, v242, v243\n"
    "v_cndmask_b32 v228, v244, 0, s[4:5]\n"
    "v_cndmask_b32 v229, v245, 0, s[4:5]\n"
    "v_cndmask_b32 v230, v246, 0, s[4:5]\n"
    "v_cndmask_b32 v231, v247, 0, s[4:5]\n"
    "v_mfma_f32_16x16x32_bf16 v[124:127], a[208:211], v[240:243], v[124:127]\n"
    "s_nop 0\n"
    "v_mfma_f32_16x16x32_bf16 v[128:131], a[208:211], v[228:231], v[128:131]\n"
    "v_mfma_f32_16x16x32_bf16 v[132:135], a[212:215], v[240:243], v[132:135]\n"
    "v_mfma_f32_16x16x32_bf16 v[136:139], a[212:215], v[228:231], v[136:139]\n"
    "v_mfma_f32_16x16x32_bf16 v[140:143], a[216:219], v[240:243], v[140:143]\n"
    "v_mfma_f32_16x16x32_bf16 v[144:147], a[216:219], v[228:231], v[144:147]\n"
    "v_mfma_f32_16x16x32_bf16 v[148:151], a[220:223], v[240:243], v[148:151]\n"
    "v_mfma_f32_16x16x32_bf16 v[152:155], a[220:223], v[228:231], v[152:155]\n"
    "s_add_u32 s81, s71, 401408\n"
    "buffer_load_dwordx4 v[240:243], v113, s[12:15], s81 offen offset:512 sc1\n"
    "buffer_load_dwordx4 v[244:247], v114, s[12:15], s81 offen offset:768 sc1\n"
    "s_waitcnt vmcnt(0)\n"
    "v_max3_u32 v107, v107, v244, v245\n"
    "v_max3_u32 v107, v107, v246, v247\n"
    "v_max3_u32 v107, v107, v240, v241\n"
    "v_max3_u32 v107, v107, v242, v243\n"
    "v_cndmask_b32 v228, 0, v244, s[4:5]\n"
    "v_cndmask_b32 v229, 0, v245, s[4:5]\n"
    "v_cndmask_b32 v230, 0, v246, s[4:5]\n"
    "v_cndmask_b32 v231, 0, v247, s[4:5]\n"
    "v_mfma_f32_16x16x32_bf16 v[124:127], a[224:227], v[240:243], v[124:127]\n"
    "s_nop 0\n"
    "v_mfma_f32_16x16x32_bf16 v[128:131], a[224:227], v[228:231], v[128:131]\n"
    "v_mfma_f32_16x16x32_bf16 v[132:135], a[228:231], v[240:243], v[132:135]\n"
    "v_mfma_f32_16x16x32_bf16 v[136:139], a[228:231], v[228:231], v[136:139]\n"
    "v_mfma_f32_16x16x32_bf16 v[140:143], a[232:235], v[240:243], v[140:143]\n"
    "v_mfma_f32_16x16x32_bf16 v[144:147], a[232:235], v[228:231], v[144:147]\n"
    "v_mfma_f32_16x16x32_bf16 v[148:151], a[236:239], v[240:243], v[148:151]\n"
    "v_mfma_f32_16x16x32_bf16 v[152:155], a[236:239], v[228:231], v[152:155]\n"
    "s_add_u32 s81, s71, 430080\n"
    "buffer_load_dwordx4 v[240:243], v113, s[12:15], s81 offen offset:512 sc1\n"
    "s_waitcnt vmcnt(0)\n"
    "v_max3_u32 v107, v107, v240, v241\n"
    "v_max3_u32 v107, v107, v242, v243\n"
    "v_cndmask_b32 v228, v244, 0, s[4:5]\n"
    "v_cndmask_b32 v229, v245, 0, s[4:5]\n"
    "v_cndmask_b32 v230, v246, 0, s[4:5]\n"
    "v_cndmask_b32 v231, v247, 0, s[4:5]\n"
    "v_mfma_f32_16x16x32_bf16 v[124:127], a[240:243], v[240:243], v[124:127]\n"
    "s_nop 0\n"
    "v_mfma_f32_16x16x32_bf16 v[128:131], a[240:243], v[228:231], v[128:131]\n"
    "v_mfma_f32_16x16x32_bf16 v[132:135], a[244:247], v[240:243], v[132:135]\n"
    "v_mfma_f32_16x16x32_bf16 v[136:139], a[244:247], v[228:231], v[136:139]\n"
    "v_mfma_f32_16x16x32_bf16 v[140:143], a[248:251], v[240:243], v[140:143]\n"
    "v_mfma_f32_16x16x32_bf16 v[144:147], a[248:251], v[228:231], v[144:147]\n"
    "v_mfma_f32_16x16x32_bf16 v[148:151], a[252:255], v[240:243], v[148:151]\n"
    "v_mfma_f32_16x16x32_bf16 v[152:155], a[252:255], v[228:231], v[152:155]\n"
    "v_cmp_eq_u32 vcc, -1, v107\n"
    "s_cbranch_vccnz .Lredo_69\n"
    ".Lredook_70:\n"
    "s_waitcnt vmcnt(0) lgkmcnt(0)\n"
    "s_branch .Lback_68\n"
    ".Lredo_69:\n"
    "s_waitcnt vmcnt(0) lgkmcnt(0)\n"
    "s_sub_u32 s60, s60, 1\n"
    "s_cbranch_scc0 .Lclean_67\n"
    "s_mov_b32 s60, 0\n"
    "s_branch .Lredook_70\n"
    ".Lredo_71:\n"
    "s_waitcnt vmcnt(0) lgkmcnt(0)\n"
    "s_branch .Lclean_67\n"
    ".Lretry_73:\n"
    "s_sub_u32 s60, s60, 1\n"
    "s_cbranch_scc1 .Lretry_73_giveup\n"
    "buffer_load_dwordx4 v[96:99], v104, s[12:15], 0 offen sc1\n"
    "buffer_load_dwordx2 v[100:101], v105, s[12:15], 0 offen offset:64 sc1\n"
    "buffer_load_dwordx2 v[102:103], v106, s[12:15], 0 offen offset:96 sc1\n"
    "s_waitcnt vmcnt(0)\n"
    "v_max3_u32 v132, v96, v97, v98\n"
    "v_max3_u32 v132, v132, v99, v100\n"
    "v_max3_u32 v132, v132, v101, v102\n"
    "v_max_u32 v132, v132, v103\n"
    "v_cmp_eq_u32 vcc, -1, v132\n"
    "s_cbranch_vccnz .Lretry_73\n"
    "s_branch .Lx4_74\n"
    ".Lretry_73_giveup:\n"
    "s_mov_b32 s60, 0\n"
    "s_waitcnt vmcnt(0)\n"
    "s_branch .Lx4_74\n"
    ".Lgretry_75:\n"
    "s_sub_u32 s60, s60, 1\n"
    "s_cbranch_scc1 .Lgretry_75_giveup\n"
    "buffer_load_dword v251, v250, s[12:15], s82 offen sc1\n"
    "s_waitcnt vmcnt(0)\n"
    "v_cmp_eq_u32 vcc, -1, v251\n"
    "s_cbranch_vccnz .Lgretry_75\n"
    "s_add_u32 s81, s82, 0\n"
    "buffer_load_dwordx4 v[200:203], v113, s[12:15], s81 offen offset:512 sc1\n"
    "s_add_u32 s81, s82, 0\n"
    "buffer_load_dwordx4 v[204:207], v114, s[12:15], s81 offen offset:768 sc1\n"
    "s_add_u32 s81, s82, 28672\n"
    "buffer_load_dwordx4 v[220:223], v113, s[12:15], s81 offen offset:512 sc1\n"
    "s_add_u32 s81, s82, 57344\n"
    "buffer_load_dwordx4 v[212:215], v113, s[12:15], s81 offen offset:512 sc1\n"
    "s_add_u32 s81, s82, 57344\n"
    "buffer_load_dwordx4 v[216:219], v114, s[12:15], s81 offen offset:768 sc1\n"
    "s_add_u32 s81, s82, 86016\n"
    "buffer_load_dwordx4 v[232:235], v113, s[12:15], s81 offen offset:512 sc1\n"
    "s_add_u32 s81, s82, 114688\n"
    "buffer_load_dwordx4 v[224:227], v113, s[12:15], s81 offen offset:512 sc1\n"
    "s_add_u32 s81, s82, 114688\n"
    "buffer_load_dwordx4 v[108:111], v114, s[12:15], s81 offen offset:768 sc1\n"
    "s_add_u32 s81, s82, 143360\n"
    "buffer_load_dwordx4 v[196:199], v113, s[12:15], s81 offen offset:512 sc1\n"
    "s_add_u32 s81, s82, 172032\n"
    "buffer_load_dwordx4 v[188:191], v113, s[12:15], s81 offen offset:512 sc1\n"
    "s_add_u32 s81, s82, 172032\n"
    "buffer_load_dwordx4 v[192:195], v114, s[12:15], s81 offen offset:768 sc1\n"
    "s_waitcnt vmcnt(0)\n"
    "s_branch .Lgok_76\n"
    ".Lgretry_75_giveup:\n"
    "s_mov_b32 s60, 0\n"
    "s_waitcnt vmcnt(0)\n"
    "s_branch .Lgok_76\n"
    ".Lclean_78:\n"
    "v_mov_b32 v107, 0\n"
    "s_add_u32 s81, s71, 0\n"
    "buffer_load_dwordx4 v[240:243], v113, s[12:15], s81 offen offset:0 sc1\n"
    "buffer_load_dwordx4 v[244:247], v113, s[12:15], s81 offen offset:256 sc1\n"
    "s_waitcnt vmcnt(0)\n"
    "v_max3_u32 v107, v107, v244, v245\n"
    "v_max3_u32 v107, v107, v246, v247\n"
    "v_max3_u32 v107, v107, v240, v241\n"
    "v_max3_u32 v107, v107, v242, v243\n"
    "v_mfma_f32_16x16x32_bf16 v[156:159], a[0:3], v[240:243], 0\n"
    "v_mfma_f32_16x16x32_bf16 v[160:163], a[0:3], v[244:247], 0\n"
    "v_mfma_f32_16x16x32_bf16 v[164:167], a[4:7], v[240:243], 0\n"
    "v_mfma_f32_16x16x32_bf16 v[168:171], a[4:7], v[244:247], 0\n"
    "v_mfma_f32_16x16x32_bf16 v[172:175], a[8:11], v[240:243], 0\n"
    "v_mfma_f32_16x16x32_bf16 v[176:179], a[8:11], v[244:247], 0\n"
    "v_mfma_f32_16x16x32_bf16 v[180:183], a[12:15], v[240:243], 0\n"
    "v_mfma_f32_16x16x32_bf16 v[184:187], a[12:15], v[244:247], 0\n"
    "s_add_u32 s81, s71, 28672\n"
    "buffer_load_dwordx4 v[240:243], v113, s[12:15], s81 offen offset:0 sc1\n"
    "buffer_load_dwordx4 v[244:247], v113, s[12:15], s81 offen offset:256 sc1\n"
    "s_waitcnt vmcnt(0)\n"
    "v_max3_u32 v107, v107, v244, v245\n"
    "v_max3_u32 v107, v107, v246, v247\n"
    "v_max3_u32 v107, v107, v240, v241\n"
    "v_max3_u32 v107, v107, v242, v243\n"
    "v_mfma_f32_16x16x32_bf16 v[156:159], a[16:19], v[240:243], v[156:159]\n"
    "v_mfma_f32_16x16x32_bf16 v[160:163], a[16:19], v[244:247], v[160:163]\n"
    "v_mfma_f32_16x16x32_bf16 v[164:167], a[20:23], v[240:243], v[164:167]\n"
    "v_mfma_f32_16x16x32_bf16 v[168:171], a[20:23], v[244:247], v[168:171]\n"
    "v_mfma_f32_16x16x32_bf16 v[172:175], a[24:27], v[240:243], v[172:175]\n"
    "v_mfma_f32_16x16x32_bf16 v[176:179], a[24:27], v[244:247], v[176:179]\n"
    "v_mfma_f32_16x16x32_bf16 v[180:183], a[28:31], v[240:243], v[180:183]\n"
    "v_mfma_f32_16x16x32_bf16 v[184:187], a[28:31], v[244:247], v[184:187]\n"
    "s_add_u32 s81, s71, 57344\n"
    "buffer_load_dwordx4 v[240:243], v113, s[12:15], s81 offen offset:0 sc1\n"
    "buffer_load_dwordx4 v[244:247], v113, s[12:15], s81 offen offset:256 sc1\n"
    "s_waitcnt vmcnt(0)\n"
    "v_max3_u32 v107, v107, v244, v245\n"
    "v_max3_u32 v107, v107, v246, v247\n"
    "v_max3_u32 v107, v107, v240, v241\n"
    "v_max3_u32 v107, v107, v242, v243\n"
    "v_mfma_f32_16x16x32_bf16 v[156:159], a[32:35], v[240:243], v[156:159]\n"
    "v_mfma_f32_16x16x32_bf16 v[160:163], a[32:35], v[244:247], v[160:163]\n"
    "v_mfma_f32_16x16x32_bf16 v[164:167], a[36:39], v[240:243], v[164:167]\n"
    "v_mfma_f32_16x16x32_bf16 v[168:171], a[36:39], v[244:247], v[168:171]\n"
    "v_mfma_f32_16x16x32_bf16 v[172:175], a[40:43], v[240:243], v[172:175]\n"
    "v_mfma_f32_16x16x32_bf16 v[176:179], a[40:43], v[244:247], v[176:179]\n"
    "v_mfma_f32_16x16x32_bf16 v[180:183], a[44:47], v[240:243], v[180:183]\n"
    "v_mfma_f32_16x16x32_bf16 v[184:187], a[44:47], v[244:247], v[184:187]\n"
    "s_add_u32 s81, s71, 86016\n"
    "buffer_load_dwordx4 v[240:243], v113, s[12:15], s81 offen offset:0 sc1\n"
    "buffer_load_dwordx4 v[244:247], v113, s[12:15], s81 offen offset:256 sc1\n"
    "s_waitcnt vmcnt(0)\n"
    "v_max3_u32 v107, v107, v244, v245\n"
    "v_max3_u32 v107, v107, v246, v247\n"
    "v_max3_u32 v107, v107, v240, v241\n"
    "v_max3_u32 v107, v107, v242, v243\n"
    "v_mfma_f32_16x16x32_bf16 v[156:159], a[48:51], v[240:243], v[156:159]\n"
    "v_mfma_f32_16x16x32_bf16 v[160:163], a[48:51], v[244:247], v[160:163]\n"
    "v_mfma_f32_16x16x32_bf16 v[164:167], a[52:55], v[240:243], v[164:167]\n"
    "v_mfma_f32_16x16x32_bf16 v[168:171], a[52:55], v[244:247], v[168:171]\n"
    "v_mfma_f32_16x16x32_bf16 v[172:175], a[56:59], v[240:243], v[172:175]\n"
    "v_mfma_f32_16x16x32_bf16 v[176:179], a[56:59], v[244:247], v[176:179]\n"
    "v_mfma_f32_16x16x32_bf16 v[180:183], a[60:63], v[240:243], v[180:183]\n"
    "v_mfma_f32_16x16x32_bf16 v[184:187], a[60:63], v[244:247], v[184:187]\n"
    "s_add_u32 s81, s71, 114688\n"
    "buffer_load_dwordx4 v[240:243], v113, s[12:15], s81 offen offset:0 sc1\n"
    "buffer_load_dwordx4 v[244:247], v113, s[12:15], s81 offen offset:256 sc1\n"
    "s_waitcnt vmcnt(0)\n"
    "v_max3_u32 v107, v107, v244, v245\n"
    "v_max3_u32 v107, v107, v246, v247\n"
    "v_max3_u32 v107, v107, v240, v241\n"
    "v_max3_u32 v107, v107, v242, v243\n"
    "v_mfma_f32_16x16x32_bf16 v[156:159], a[64:67], v[240:243], v[156:159]\n"
    "v_mfma_f32_16x16x32_bf16 v[160:163], a[64:67], v[244:247], v[160:163]\n"
    "v_mfma_f32_16x16x32_bf16 v[164:167], a[68:71], v[240:243], v[164:167]\n"
    "v_mfma_f32_16x16x32_bf16 v[168:171], a[68:71], v[244:247], v[168:171]\n"
    "v_mfma_f32_16x16x32_bf16 v[172:175], a[72:75], v[240:243], v[172:175]\n"
    "v_mfma_f32_16x16x32_bf16 v[176:179], a[72:75], v[244:247], v[176:179]\n"
    "v_mfma_f32_16x16x32_bf16 v[180:183], a[76:79], v[240:243], v[180:183]\n"
    "v_mfma_f32_16x16x32_bf16 v[184:187], a[76:79], v[244:247], v[184:187]\n"
    "s_add_u32 s81, s71, 143360\n"
    "buffer_load_dwordx4 v[240:243], v113, s[12:15], s81 offen offset:0 sc1\n"
    "buffer_load_dwordx4 v[244:247], v113, s[12:15], s81 offen offset:256 sc1\n"
    "s_waitcnt vmcnt(0)\n"
    "v_max3_u32 v107, v107, v244, v245\n"
    "v_max3_u32 v107, v107, v246, v247\n"
    "v_max3_u32 v107, v107, v240, v241\n"
    "v_max3_u32 v107, v107, v242, v243\n"
    "v_mfma_f32_16x16x32_bf16 v[156:159], a[80:83], v[240:243], v[156:159]\n"
    "v_mfma_f32_16x16x32_bf16 v[160:163], a[80:83], v[244:247], v[160:163]\n"
    "v_mfma_f32_16x16x32_bf16 v[164:167], a[84:87], v[240:243], v[164:167]\n"
    "v_mfma_f32_16x16x32_bf16 v[168:171], a[84:87], v[244:247], v[168:171]\n"
    "v_mfma_f32_16x16x32_bf16 v[172:175], a[88:91], v[240:243], v[172:175]\n"
    "v_mfma_f32_16x16x32_bf16 v[176:179], a[88:91], v[244:247], v[176:179]\n"
    "v_mfma_f32_16x16x32_bf16 v[180:183], a[92:95], v[240:243], v[180:183]\n"
    "v_mfma_f32_16x16x32_bf16 v[184:187], a[92:95], v[244:247], v[184:187]\n"
    "s_add_u32 s81, s71, 172032\n"
    "buffer_load_dwordx4 v[240:243], v113, s[12:15], s81 offen offset:0 sc1\n"
    "buffer_load_dwordx4 v[244:247], v113, s[12:15], s81 offen offset:256 sc1\n"
    "s_waitcnt vmcnt(0)\n"
    "v_max3_u32 v107, v107, v244, v245\n"
    "v_max3_u32 v107, v107, v246, v247\n"
    "v_max3_u32 v107, v107, v240, v241\n"
    "v_max3_u32 v107, v107, v242, v243\n"
    "v_mfma_f32_16x16x32_bf16 v[156:159], a[96:99], v[240:243], v[156:159]\n"
    "v_mfma_f32_16x16x32_bf16 v[160:163], a[96:99], v[244:247], v[160:163]\n"
    "v_mfma_f32_16x16x32_bf16 v[164:167], a[100:103], v[240:243], v[164:167]\n"
    "v_mfma_f32_16x16x32_bf16 v[168:171], a[100:103], v[244:247], v[168:171]\n"
    "v_mfma_f32_16x16x32_bf16 v[172:175], a[104:107], v[240:243], v[172:175]\n"
    "v_mfma_f32_16x16x32_bf16 v[176:179], a[104:107], v[244:247], v[176:179]\n"
    "v_mfma_f32_16x16x32_bf16 v[180:183], a[108:111], v[240:243], v[180:183]\n"
    "v_mfma_f32_16x16x32_bf16 v[184:187], a[108:111], v[244:247], v[184:187]\n"
    "s_add_u32 s81, s71, 200704\n"
    "buffer_load_dwordx4 v[240:243], v113, s[12:15], s81 offen offset:0 sc1\n"
    "buffer_load_dwordx4 v[244:247], v113, s[12:15], s81 offen offset:256 sc1\n"
    "s_waitcnt vmcnt(0)\n"
    "v_max3_u32 v107, v107, v244, v245\n"
    "v_max3_u32 v107, v107, v246, v247\n"
    "v_max3_u32 v107, v107, v240, v241\n"
    "v_max3_u32 v107, v107, v242, v243\n"
    "v_mfma_f32_16x16x32_bf16 v[156:159], a[112:115], v[240:243], v[156:159]\n"
    "v_mfma_f32_16x16x32_bf16 v[160:163], a[112:115], v[244:247], v[160:163]\n"
    "v_mfma_f32_16x16x32_bf16 v[164:167], a[116:119], v[240:243], v[164:167]\n"
    "v_mfma_f32_16x16x32_bf16 v[168:171], a[116:119], v[244:247], v[168:171]\n"
    "v_mfma_f32_16x16x32_bf16 v[172:175], a[120:123], v[240:243], v[172:175]\n"
    "v_mfma_f32_16x16x32_bf16 v[176:179], a[120:123], v[244:247], v[176:179]\n"
    "v_mfma_f32_16x16x32_bf16 v[180:183], a[124:127], v[240:243], v[180:183]\n"
    "v_mfma_f32_16x16x32_bf16 v[184:187], a[124:127], v[244:247], v[184:187]\n"
    "s_add_u32 s81, s71, 229376\n"
    "buffer_load_dwordx4 v[240:243], v113, s[12:15], s81 offen offset:0 sc1\n"
    "buffer_load_dwordx4 v[244:247], v113, s[12:15], s81 offen offset:256 sc1\n"
    "s_waitcnt vmcnt(0)\n"
    "v_max3_u32 v107, v107, v244, v245\n"
    "v_max3_u32 v107, v107, v246, v247\n"
    "v_max3_u32 v107, v107, v240, v241\n"
    "v_max3_u32 v107, v107, v242, v243\n"
    "v_mfma_f32_16x16x32_bf16 v[156:159], a[128:131], v[240:243], v[156:159]\n"
    "v_mfma_f32_16x16x32_bf16 v[160:163], a[128:131], v[244:247], v[160:163]\n"
    "v_mfma_f32_16x16x32_bf16 v[164:167], a[132:135], v[240:243], v[164:167]\n"
    "v_mfma_f32_16x16x32_bf16 v[168:171], a[132:135], v[244:247], v[168:171]\n"
    "v_mfma_f32_16x16x32_bf16 v[172:175], a[136:139], v[240:243], v[172:175]\n"
    "v_mfma_f32_16x16x32_bf16 v[176:179], a[136:139], v[244:247], v[176:179]\n"
    "v_mfma_f32_16x16x32_bf16 v[180:183], a[140:143], v[240:243], v[180:183]\n"
    "v_mfma_f32_16x16x32_bf16 v[184:187], a[140:143], v[244:247], v[184:187]\n"
    "s_add_u32 s81, s71, 258048\n"
    "buffer_load_dwordx4 v[240:243], v113, s[12:15], s81 offen offset:0 sc1\n"
    "buffer_load_dwordx4 v[244:247], v113, s[12:15], s81 offen offset:256 sc1\n"
    "s_waitcnt vmcnt(0)\n"
    "v_max3_u32 v107, v107, v244, v245\n"
    "v_max3_u32 v107, v107, v246, v247\n"
    "v_max3_u32 v107, v107, v240, v241\n"
    "v_max3_u32 v107, v107, v242, v243\n"
    "v_mfma_f32_16x16x32_bf16 v[156:159], a[144:147], v[240:243], v[156:159]\n"
    "v_mfma_f32_16x16x32_bf16 v[160:163], a[144:147], v[244:247], v[160:163]\n"
    "v_mfma_f32_16x16x32_bf16 v[164:167], a[148:151], v[240:243], v[164:167]\n"
    "v_mfma_f32_16x16x32_bf16 v[168:171], a[148:151], v[244:247], v[168:171]\n"
    "v_mfma_f32_16x16x32_bf16 v[172:175], a[152:155], v[240:243], v[172:175]\n"
    "v_mfma_f32_16x16x32_bf16 v[176:179], a[152:155], v[244:247], v[176:179]\n"
    "v_mfma_f32_16x16x32_bf16 v[180:183], a[156:159], v[240:243], v[180:183]\n"
    "v_mfma_f32_16x16x32_bf16 v[184:187], a[156:159], v[244:247], v[184:187]\n"
    "s_add_u32 s81, s71, 286720\n"
    "buffer_load_dwordx4 v[240:243], v113, s[12:15], s81 offen offset:0 sc1\n"
    "buffer_load_dwordx4 v[244:247], v113, s[12:15], s81 offen offset:256 sc1\n"
    "s_waitcnt vmcnt(0)\n"
    "v_max3_u32 v107, v107, v244, v245\n"
    "v_max3_u32 v107, v107, v246, v247\n"
    "v_max3_u32 v107, v107, v240, v241\n"
    "v_max3_u32 v107, v107, v242, v243\n"
    "v_mfma_f32_16x16x32_bf16 v[156:159], a[160:163], v[240:243], v[156:159]\n"
    "v_mfma_f32_16x16x32_bf16 v[160:163], a[160:163], v[244:247], v[160:163]\n"
    "v_mfma_f32_16x16x32_bf16 v[164:167], a[164:167], v[240:243], v[164:167]\n"
    "v_mfma_f32_16x16x32_bf16 v[168:171], a[164:167], v[244:247], v[168:171]\n"
    "v_mfma_f32_16x16x32_bf16 v[172:175], a[168:171], v[240:243], v[172:175]\n"
    "v_mfma_f32_16x16x32_bf16 v[176:179], a[168:171], v[244:247], v[176:179]\n"
    "v_mfma_f32_16x16x32_bf16 v[180:183], a[172:175], v[240:243], v[180:183]\n"
    "v_mfma_f32_16x16x32_bf16 v[184:187], a[172:175], v[244:247], v[184:187]\n"
    "s_add_u32 s81, s71, 315392\n"
    "buffer_load_dwordx4 v[240:243], v113, s[12:15], s81 offen offset:0 sc1\n"
    "buffer_load_dwordx4 v[244:247], v113, s[12:15], s81 offen offset:256 sc1\n"
    "s_waitcnt vmcnt(0)\n"
    "v_max3_u32 v107, v107, v244, v245\n"
    "v_max3_u32 v107, v107, v246, v247\n"
    "v_max3_u32 v107, v107, v240, v241\n"
    "v_max3_u32 v107, v107, v242, v243\n"
    "v_mfma_f32_16x16x32_bf16 v[156:159], a[176:179], v[240:243], v[156:159]\n"
    "v_mfma_f32_16x16x32_bf16 v[160:163], a[176:179], v[244:247], v[160:163]\n"
    "v_mfma_f32_16x16x32_bf16 v[164:167], a[180:183], v[240:243], v[164:167]\n"
    "v_mfma_f32_16x16x32_bf16 v[168:171], a[180:183], v[244:247], v[168:171]\n"
    "v_mfma_f32_16x16x32_bf16 v[172:175], a[184:187], v[240:243], v[172:175]\n"
    "v_mfma_f32_16x16x32_bf16 v[176:179], a[184:187], v[244:247], v[176:179]\n"
    "v_mfma_f32_16x16x32_bf16 v[180:183], a[188:191], v[240:243], v[180:183]\n"
    "v_mfma_f32_16x16x32_bf16 v[184:187], a[188:191], v[244:247], v[184:187]\n"
    "s_add_u32 s81, s71, 344064\n"
    "buffer_load_dwordx4 v[240:243], v113, s[12:15], s81 offen offset:0 sc1\n"
    "buffer_load_dwordx4 v[244:247], v113, s[12:15], s81 offen offset:256 sc1\n"
    "s_waitcnt vmcnt(0)\n"
    "v_max3_u32 v107, v107, v244, v245\n"
    "v_max3_u32 v107, v107, v246, v247\n"
    "v_max3_u32 v107, v107, v240, v241\n"
    "v_max3_u32 v107, v107, v242, v243\n"
    "v_mfma_f32_16x16x32_bf16 v[156:159], a[192:195], v[240:243], v[156:159]\n"
    "v_mfma_f32_16x16x32_bf16 v[160:163], a[192:195], v[244:247], v[160:163]\n"
    "v_mfma_f32_16x16x32_bf16 v[164:167], a[196:199], v[240:243], v[164:167]\n"
    "v_mfma_f32_16x16x32_bf16 v[168:171], a[196:199], v[244:247], v[168:171]\n"
    "v_mfma_f32_16x16x32_bf16 v[172:175], a[200:203], v[240:243], v[172:175]\n"
    "v_mfma_f32_16x16x32_bf16 v[176:179], a[200:203], v[244:247], v[176:179]\n"
    "v_mfma_f32_16x16x32_bf16 v[180:183], a[204:207], v[240:243], v[180:183]\n"
    "v_mfma_f32_16x16x32_bf16 v[184:187], a[204:207], v[244:247], v[184:187]\n"
    "s_add_u32 s81, s71, 372736\n"
    "buffer_load_dwordx4 v[240:243], v113, s[12:15], s81 offen offset:0 sc1\n"
    "buffer_load_dwordx4 v[244:247], v113, s[12:15], s81 offen offset:256 sc1\n"
    "s_waitcnt vmcnt(0)\n"
    "v_max3_u32 v107, v107, v244, v245\n"
    "v_max3_u32 v107, v107, v246, v247\n"
    "v_max3_u32 v107, v107, v240, v241\n"
    "v_max3_u32 v107, v107, v242, v243\n"
    "v_mfma_f32_16x16x32_bf16 v[156:159], a[208:211], v[240:243], v[156:159]\n"
    "v_mfma_f32_16x16x32_bf16 v[160:163], a[208:211], v[244:247], v[160:163]\n"
    "v_mfma_f32_16x16x32_bf16 v[164:167], a[212:215], v[240:243], v[164:167]\n"
    "v_mfma_f32_16x16x32_bf16 v[168:171], a[212:215], v[244:247], v[168:171]\n"
    "v_mfma_f32_16x16x32_bf16 v[172:175], a[216:219], v[240:243], v[172:175]\n"
    "v_mfma_f32_16x16x32_bf16 v[176:179], a[216:219], v[244:247], v[176:179]\n"
    "v_mfma_f32_16x16x32_bf16 v[180:183], a[220:223], v[240:243], v[180:183]\n"
    "v_mfma_f32_16x16x32_bf16 v[184:187], a[220:223], v[244:247], v[184:187]\n"
    "s_add_u32 s81, s71, 401408\n"
    "buffer_load_dwordx4 v[240:243], v113, s[12:15], s81 offen offset:0 sc1\n"
    "buffer_load_dwordx4 v[244:247], v113, s[12:15], s81 offen offset:256 sc1\n"
    "s_waitcnt vmcnt(0)\n"
    "v_max3_u32 v107, v107, v244, v245\n"
    "v_max3_u32 v107, v107, v246, v247\n"
    "v_max3_u32 v107, v107, v240, v241\n"
    "v_max3_u32 v107, v107, v242, v243\n"
    "v_mfma_f32_16x16x32_bf16 v[156:159], a[224:227], v[240:243], v[156:159]\n"
    "v_mfma_f32_16x16x32_bf16 v[160:163], a[224:227], v[244:247], v[160:163]\n"
    "v_mfma_f32_16x16x32_bf16 v[164:167], a[228:231], v[240:243], v[164:167]\n"
    "v_mfma_f32_16x16x32_bf16 v[168:171], a[228:231], v[244:247], v[168:171]\n"
    "v_mfma_f32_16x16x32_bf16 v[172:175], a[232:235], v[240:243], v[172:175]\n"
    "v_mfma_f32_16x16x32_bf16 v[176:179], a[232:235], v[244:247], v[176:179]\n"
    "v_mfma_f32_16x16x32_bf16 v[180:183], a[236:239], v[240:243], v[180:183]\n"
    "v_mfma_f32_16x16x32_bf16 v[184:187], a[236:239], v[244:247], v[184:187]\n"
    "s_add_u32 s81, s71, 430080\n"
    "buffer_load_dwordx4 v[240:243], v113, s[12:15], s81 offen offset:0 sc1\n"
    "buffer_load_dwordx4 v[244:247], v113, s[12:15], s81 offen offset:256 sc1\n"
    "s_waitcnt vmcnt(0)\n"
    "v_max3_u32 v107, v107, v244, v245\n"
    "v_max3_u32 v107, v107, v246, v247\n"
    "v_max3_u32 v107, v107, v240, v241\n"
    "v_max3_u32 v107, v107, v242, v243\n"
    "v_mfma_f32_16x16x32_bf16 v[156:159], a[240:243], v[240:243], v[156:159]\n"
    "v_mfma_f32_16x16x32_bf16 v[160:163], a[240:243], v[244:247], v[160:163]\n"
    "v_mfma_f32_16x16x32_bf16 v[164:167], a[244:247], v[240:243], v[164:167]\n"
    "v_mfma_f32_16x16x32_bf16 v[168:171], a[244:247], v[244:247], v[168:171]\n"
    "v_mfma_f32_16x16x32_bf16 v[172:175], a[248:251], v[240:243], v[172:175]\n"
    "v_mfma_f32_16x16x32_bf16 v[176:179], a[248:251], v[244:247], v[176:179]\n"
    "v_mfma_f32_16x16x32_bf16 v[180:183], a[252:255], v[240:243], v[180:183]\n"
    "v_mfma_f32_16x16x32_bf16 v[184:187], a[252:255], v[244:247], v[184:187]\n"
    "v_cmp_eq_u32 vcc, -1, v107\n"
    "s_cbranch_vccnz .Lredo_80\n"
    ".Lredook_81:\n"
    "s_waitcnt vmcnt(0) lgkmcnt(0)\n"
    "s_branch .Lback_79\n"
    ".Lredo_80:\n"
    "s_waitcnt vmcnt(0) lgkmcnt(0)\n"
    "s_sub_u32 s60, s60, 1\n"
    "s_cbranch_scc0 .Lclean_78\n"
    "s_mov_b32 s60, 0\n"
    "s_branch .Lredook_81\n"
    ".Lredo_82:\n"
    "s_waitcnt vmcnt(0) lgkmcnt(0)\n"
    "s_branch .Lclean_78\n"
    ".Lgretry_85:\n"
    "s_sub_u32 s60, s60, 1\n"
    "s_cbranch_scc1 .Lgretry_85_giveup\n"
    "buffer_load_dword v186, v185, s[12:15], s82 offen sc1\n"
    "s_waitcnt vmcnt(0)\n"
    "v_cmp_eq_u32 vcc, -1, v186\n"
    "s_cbranch_vccnz .Lgretry_85\n"
    "s_add_u32 s81, s82, 0\n"
    "buffer_load_dwordx4 v[208:211], v113, s[12:15], s81 offen offset:0 sc1\n"
    "s_add_u32 s81, s82, 0\n"
    "buffer_load_dwordx4 v[192:195], v113, s[12:15], s81 offen offset:256 sc1\n"
    "s_add_u32 s81, s82, 28672\n"
    "buffer_load_dwordx4 v[200:203], v113, s[12:15], s81 offen offset:0 sc1\n"
    "s_add_u32 s81, s82, 28672\n"
    "buffer_load_dwordx4 v[204:207], v113, s[12:15], s81 offen offset:256 sc1\n"
    "s_add_u32 s81, s82, 57344\n"
    "buffer_load_dwordx4 v[220:223], v113, s[12:15], s81 offen offset:0 sc1\n"
    "s_add_u32 s81, s82, 57344\n"
    "buffer_load_dwordx4 v[212:215], v113, s[12:15], s81 offen offset:256 sc1\n"
    "s_add_u32 s81, s82, 86016\n"
    "buffer_load_dwordx4 v[224:227], v113, s[12:15], s81 offen offset:0 sc1\n"
    "s_add_u32 s81, s82, 86016\n"
    "buffer_load_dwordx4 v[216:219], v113, s[12:15], s81 offen offset:256 sc1\n"
    "s_add_u32 s81, s82, 114688\n"
    "buffer_load_dwordx4 v[196:199], v113, s[12:15], s81 offen offset:0 sc1\n"
    "s_add_u32 s81, s82, 114688\n"
    "buffer_load_dwordx4 v[188:191], v113, s[12:15], s81 offen offset:256 sc1\n"
    "s_waitcnt vmcnt(0)\n"
    "s_branch .Lgok_86\n"
    ".Lgretry_85_giveup:\n"
    "s_mov_b32 s60, 0\n"
    "s_waitcnt vmcnt(0)\n"
    "s_branch .Lgok_86\n"
    ".Lclean_87:\n"
    "v_mov_b32 v107, 0\n"
    "v_lshlrev_b32 v124, 16, v100\n"
    "v_and_b32 v125, 0xffff0000, v100\n"
    "v_lshlrev_b32 v126, 16, v101\n"
    "v_and_b32 v127, 0xffff0000, v101\n"
    "v_lshlrev_b32 v128, 16, v102\n"
    "v_and_b32 v129, 0xffff0000, v102\n"
    "v_lshlrev_b32 v130, 16, v103\n"
    "v_and_b32 v131, 0xffff0000, v103\n"
    "v_add_f32 v124, v124, v123\n"
    "v_add_f32 v125, v125, v123\n"
    "v_add_f32 v126, v126, v123\n"
    "v_add_f32 v127, v127, v123\n"
    "v_fma_f32 v128, v123, 0.5, v128\n"
    "v_fma_f32 v129, v123, 0.5, v129\n"
    "v_fma_f32 v130, v123, 0.5, v130\n"
    "v_fma_f32 v131, v123, 0.5, v131\n"
    "s_add_u32 s81, s82, 0\n"
    "buffer_load_dwordx4 v[240:243], v113, s[12:15], s81 offen offset:512 sc1\n"
    "buffer_load_dwordx4 v[244:247], v114, s[12:15], s81 offen offset:768 sc1\n"
    "s_waitcnt vmcnt(0)\n"
    "v_max3_u32 v107, v107, v244, v245\n"
    "v_max3_u32 v107, v107, v246, v247\n"
    "v_max3_u32 v107, v107, v240, v241\n"
    "v_max3_u32 v107, v107, v242, v243\n"
    "v_cndmask_b32 v228, 0, v244, s[4:5]\n"
    "v_cndmask_b32 v229, 0, v245, s[4:5]\n"
    "v_cndmask_b32 v230, 0, v246, s[4:5]\n"
    "v_cndmask_b32 v231, 0, v247, s[4:5]\n"
    "v_mfma_f32_16x16x32_bf16 v[124:127], v[240:243], v[0:3], v[124:127]\n"
    "s_nop 0\n"
    "v_mfma_f32_16x16x32_bf16 v[128:131], v[228:231], v[0:3], v[128:131]\n"
    "v_mfma_f32_16x16x32_bf16 v[132:135], v[240:243], v[4:7], 0\n"
    "v_mfma_f32_16x16x32_bf16 v[136:139], v[228:231], v[4:7], 0\n"
    "v_mfma_f32_16x16x32_bf16 v[140:143], v[240:243], v[8:11], 0\n"
    "v_mfma_f32_16x16x32_bf16 v[144:147], v[228:231], v[8:11], 0\n"
    "v_mfma_f32_16x16x32_bf16 v[148:151], v[240:243], v[12:15], 0\n"
    "v_mfma_f32_16x16x32_bf16 v[152:155], v[228:231], v[12:15], 0\n"
    "s_add_u32 s81, s82, 28672\n"
    "buffer_load_dwordx4 v[240:243], v113, s[12:15], s81 offen offset:512 sc1\n"
    "s_waitcnt vmcnt(0)\n"
    "v_max3_u32 v107, v107, v240, v241\n"
    "v_max3_u32 v107, v107, v242, v243\n"
    "v_cndmask_b32 v228, v244, 0, s[4:5]\n"
    "v_cndmask_b32 v229, v245, 0, s[4:5]\n"
    "v_cndmask_b32 v230, v246, 0, s[4:5]\n"
    "v_cndmask_b32 v231, v247, 0, s[4:5]\n"
    "v_mfma_f32_16x16x32_bf16 v[124:127], v[240:243], v[16:19], v[124:127]\n"
    "s_nop 0\n"
    "v_mfma_f32_16x16x32_bf16 v[128:131], v[228:231], v[16:19], v[128:131]\n"
    "v_mfma_f32_16x16x32_bf16 v[132:135], v[240:243], v[20:23], v[132:135]\n"
    "v_mfma_f32_16x16x32_bf16 v[136:139], v[228:231], v[20:23], v[136:139]\n"
    "v_mfma_f32_16x16x32_bf16 v[140:143], v[240:243], v[24:27], v[140:143]\n"
    "v_mfma_f32_16x16x32_bf16 v[144:147], v[228:231], v[24:27], v[144:147]\n"
    "v_mfma_f32_16x16x32_bf16 v[148:151], v[240:243], v[28:31], v[148:151]\n"
    "v_mfma_f32_16x16x32_bf16 v[152:155], v[228:231], v[28:31], v[152:155]\n"
    "s_add_u32 s81, s82, 57344\n"
    "buffer_load_dwordx4 v[240:243], v113, s[12:15], s81 offen offset:512 sc1\n"
    "buffer_load_dwordx4 v[244:247], v114, s[12:15], s81 offen offset:768 sc1\n"
    "s_waitcnt vmcnt(0)\n"
    "v_max3_u32 v107, v107, v244, v245\n"
    "v_max3_u32 v107, v107, v246, v247\n"
    "v_max3_u32 v107, v107, v240, v241\n"
    "v_max3_u32 v107, v107, v242, v243\n"
    "v_cndmask_b32 v228, 0, v244, s[4:5]\n"
    "v_cndmask_b32 v229, 0, v245, s[4:5]\n"
    "v_cndmask_b32 v230, 0, v246, s[4:5]\n"
    "v_cndmask_b32 v231, 0, v247, s[4:5]\n"
    "v_mfma_f32_16x16x32_bf16 v[124:127], v[240:243], v[32:35], v[124:127]\n"
    "s_nop 0\n"
    "v_mfma_f32_16x16x32_bf16 v[128:131], v[228:231], v[32:35], v[128:131]\n"
    "v_mfma_f32_16x16x32_bf16 v[132:135], v[240:243], v[36:39], v[132:135]\n"
    "v_mfma_f32_16x16x32_bf16 v[136:139], v[228:231], v[36:39], v[136:139]\n"
    "v_mfma_f32_16x16x32_bf16 v[140:143], v[240:243], v[40:43], v[140:143]\n"
    "v_mfma_f32_16x16x32_bf16 v[144:147], v[228:231], v[40:43], v[144:147]\n"
    "v_mfma_f32_16x16x32_bf16 v[148:151], v[240:243], v[44:47], v[148:151]\n"
    "v_mfma_f32_16x16x32_bf16 v[152:155], v[228:231], v[44:47], v[152:155]\n"
    "s_add_u32 s81, s82, 86016\n"
    "buffer_load_dwordx4 v[240:243], v113, s[12:15], s81 offen offset:512 sc1\n"
    "s_waitcnt vmcnt(0)\n"
    "v_max3_u32 v107, v107, v240, v241\n"
    "v_max3_u32 v107, v107, v242, v243\n"
    "v_cndmask_b32 v228, v244, 0, s[4:5]\n"
    "v_cndmask_b32 v229, v245, 0, s[4:5]\n"
    "v_cndmask_b32 v230, v246, 0, s[4:5]\n"
    "v_cndmask_b32 v231, v247, 0, s[4:5]\n"
    "v_mfma_f32_16x16x32_bf16 v[124:127], v[240:243], v[48:51], v[124:127]\n"
    "s_nop 0\n"
    "v_mfma_f32_16x16x32_bf16 v[128:131], v[228:231], v[48:51], v[128:131]\n"
    "v_mfma_f32_16x16x32_bf16 v[132:135], v[240:243], v[52:55], v[132:135]\n"
    "v_mfma_f32_16x16x32_bf16 v[136:139], v[228:231], v[52:55], v[136:139]\n"
    "v_mfma_f32_16x16x32_bf16 v[140:143], v[240:243], v[56:59], v[140:143]\n"
    "v_mfma_f32_16x16x32_bf16 v[144:147], v[228:231], v[56:59], v[144:147]\n"
    "v_mfma_f32_16x16x32_bf16 v[148:151], v[240:243], v[60:63], v[148:151]\n"
    "v_mfma_f32_16x16x32_bf16 v[152:155], v[228:231], v[60:63], v[152:155]\n"
    "s_add_u32 s81, s82, 114688\n"
    "buffer_load_dwordx4 v[240:243], v113, s[12:15], s81 offen offset:512 sc1\n"
    "buffer_load_dwordx4 v[244:247], v114, s[12:15], s81 offen offset:768 sc1\n"
    "s_waitcnt vmcnt(0)\n"
    "v_max3_u32 v107, v107, v244, v245\n"
    "v_max3_u32 v107, v107, v246, v247\n"
    "v_max3_u32 v107, v107, v240, v241\n"
    "v_max3_u32 v107, v107, v242, v243\n"
    "v_cndmask_b32 v228, 0, v244, s[4:5]\n"
    "v_cndmask_b32 v229, 0, v245, s[4:5]\n"
    "v_cndmask_b32 v230, 0, v246, s[4:5]\n"
    "v_cndmask_b32 v231, 0, v247, s[4:5]\n"
    "v_mfma_f32_16x16x32_bf16 v[124:127], v[240:243], v[64:67], v[124:127]\n"
    "s_nop 0\n"
    "v_mfma_f32_16x16x32_bf16 v[128:131], v[228:231], v[64:67], v[128:131]\n"
    "v_mfma_f32_16x16x32_bf16 v[132:135], v[240:243], v[68:71], v[132:135]\n"
    "v_mfma_f32_16x16x32_bf16 v[136:139], v[228:231], v[68:71], v[136:139]\n"
    "v_mfma_f32_16x16x32_bf16 v[140:143], v[240:243], v[72:75], v[140:143]\n"
    "v_mfma_f32_16x16x32_bf16 v[144:147], v[228:231], v[72:75], v[144:147]\n"
    "v_mfma_f32_16x16x32_bf16 v[148:151], v[240:243], v[76:79], v[148:151]\n"
    "v_mfma_f32_16x16x32_bf16 v[152:155], v[228:231], v[76:79], v[152:155]\n"
    "s_add_u32 s81, s82, 143360\n"
    "buffer_load_dwordx4 v[240:243], v113, s[12:15], s81 offen offset:512 sc1\n"
    "s_waitcnt vmcnt(0)\n"
    "v_max3_u32 v107, v107, v240, v241\n"
    "v_max3_u32 v107, v107, v242, v243\n"
    "v_cndmask_b32 v228, v244, 0, s[4:5]\n"
    "v_cndmask_b32 v229, v245, 0, s[4:5]\n"
    "v_cndmask_b32 v230, v246, 0, s[4:5]\n"
    "v_cndmask_b32 v231, v247, 0, s[4:5]\n"
    "v_mfma_f32_16x16x32_bf16 v[124:127], v[240:243], v[80:83], v[124:127]\n"
    "s_nop 0\n"
    "v_mfma_f32_16x16x32_bf16 v[128:131], v[228:231], v[80:83], v[128:131]\n"
    "v_mfma_f32_16x16x32_bf16 v[132:135], v[240:243], v[84:87], v[132:135]\n"
    "v_mfma_f32_16x16x32_bf16 v[136:139], v[228:231], v[84:87], v[136:139]\n"
    "v_mfma_f32_16x16x32_bf16 v[140:143], v[240:243], v[88:91], v[140:143]\n"
    "v_mfma_f32_16x16x32_bf16 v[144:147], v[228:231], v[88:91], v[144:147]\n"
    "v_mfma_f32_16x16x32_bf16 v[148:151], v[240:243], v[92:95], v[148:151]\n"
    "v_mfma_f32_16x16x32_bf16 v[152:155], v[228:231], v[92:95], v[152:155]\n"
    "s_add_u32 s81, s82, 172032\n"
    "buffer_load_dwordx4 v[240:243], v113, s[12:15], s81 offen offset:512 sc1\n"
    "buffer_load_dwordx4 v[244:247], v114, s[12:15], s81 offen offset:768 sc1\n"
    "s_waitcnt vmcnt(0)\n"
    "v_max3_u32 v107, v107, v244, v245\n"
    "v_max3_u32 v107, v107, v246, v247\n"
    "v_max3_u32 v107, v107, v240, v241\n"
    "v_max3_u32 v107, v107, v242, v243\n"
    "v_cndmask_b32 v228, 0, v244, s[4:5]\n"
    "v_cndmask_b32 v229, 0, v245, s[4:5]\n"
    "v_cndmask_b32 v230, 0, v246, s[4:5]\n"
    "v_cndmask_b32 v231, 0, v247, s[4:5]\n"
    "ds_read_b64_tr_b16 v[248:249], v115 offset:0\n"
    "ds_read_b64_tr_b16 v[250:251], v115 offset:512\n"
    "s_waitcnt lgkmcnt(0)\n"
    "v_mfma_f32_16x16x32_bf16 v[124:127], v[240:243], v[248:251], v[124:127]\n"
    "v_mfma_f32_16x16x32_bf16 v[128:131], v[228:231], v[248:251], v[128:131]\n"
    "ds_read_b64_tr_b16 v[248:249], v116 offset:0\n"
    "ds_read_b64_tr_b16 v[250:251], v116 offset:512\n"
    "s_waitcnt lgkmcnt(0)\n"
    "v_mfma_f32_16x16x32_bf16 v[132:135], v[240:243], v[248:251], v[132:135]\n"
    "v_mfma_f32_16x16x32_bf16 v[136:139], v[228:231], v[248:251], v[136:139]\n"
    "ds_read_b64_tr_b16 v[248:249], v117 offset:0\n"
    "ds_read_b64_tr_b16 v[250:251], v117 offset:512\n"
    "s_waitcnt lgkmcnt(0)\n"
    "v_mfma_f32_16x16x32_bf16 v[140:143], v[240:243], v[248:251], v[140:143]\n"
    "v_mfma_f32_16x16x32_bf16 v[144:147], v[228:231], v[248:251], v[144:147]\n"
    "ds_read_b64_tr_b16 v[248:249], v118 offset:0\n"
    "ds_read_b64_tr_b16 v[250:251], v118 offset:512\n"
    "s_waitcnt lgkmcnt(0)\n"
    "v_mfma_f32_16x16x32_bf16 v[148:151], v[240:243], v[248:251], v[148:151]\n"
    "v_mfma_f32_16x16x32_bf16 v[152:155], v[228:231], v[248:251], v[152:155]\n"
    "s_add_u32 s81, s82, 200704\n"
    "buffer_load_dwordx4 v[240:243], v113, s[12:15], s81 offen offset:512 sc1\n"
    "s_waitcnt vmcnt(0)\n"
    "v_max3_u32 v107, v107, v240, v241\n"
    "v_max3_u32 v107, v107, v242, v243\n"
    "v_cndmask_b32 v228, v244, 0, s[4:5]\n"
    "v_cndmask_b32 v229, v245, 0, s[4:5]\n"
    "v_cndmask_b32 v230, v246, 0, s[4:5]\n"
    "v_cndmask_b32 v231, v247, 0, s[4:5]\n"
    "ds_read_b64_tr_b16 v[248:249], v115 offset:4096\n"
    "ds_read_b64_tr_b16 v[250:251], v115 offset:4608\n"
    "s_waitcnt lgkmcnt(0)\n"
    "v_mfma_f32_16x16x32_bf16 v[124:127], v[240:243], v[248:251], v[124:127]\n"
    "v_mfma_f32_16x16x32_bf16 v[128:131], v[228:231], v[248:251], v[128:131]\n"
    "ds_read_b64_tr_b16 v[248:249], v116 offset:4096\n"
    "ds_read_b64_tr_b16 v[250:251], v116 offset:4608\n"
    "s_waitcnt lgkmcnt(0)\n"
    "v_mfma_f32_16x16x32_bf16 v[132:135], v[240:243], v[248:251], v[132:135]\n"
    "v_mfma_f32_16x16x32_bf16 v[136:139], v[228:231], v[248:251], v[136:139]\n"
    "ds_read_b64_tr_b16 v[248:249], v117 offset:4096\n"
    "ds_read_b64_tr_b16 v[250:251], v117 offset:4608\n"
    "s_waitcnt lgkmcnt(0)\n"
    "v_mfma_f32_16x16x32_bf16 v[140:143], v[240:243], v[248:251], v[140:143]\n"
    "v_mfma_f32_16x16x32_bf16 v[144:147], v[228:231], v[248:251], v[144:147]\n"
    "ds_read_b64_tr_b16 v[248:249], v118 offset:4096\n"
    "ds_read_b64_tr_b16 v[250:251], v118 offset:4608\n"
    "s_waitcnt lgkmcnt(0)\n"
    "v_mfma_f32_16x16x32_bf16 v[148:151], v[240:243], v[248:251], v[148:151]\n"
    "v_mfma_f32_16x16x32_bf16 v[152:155], v[228:231], v[248:251], v[152:155]\n"
    "s_add_u32 s81, s82, 229376\n"
    "buffer_load_dwordx4 v[240:243], v113, s[12:15], s81 offen offset:512 sc1\n"
    "buffer_load_dwordx4 v[244:247], v114, s[12:15], s81 offen offset:768 sc1\n"
    "s_waitcnt vmcnt(0)\n"
    "v_max3_u32 v107, v107, v244, v245\n"
    "v_max3_u32 v107, v107, v246, v247\n"
    "v_max3_u32 v107, v107, v240, v241\n"
    "v_max3_u32 v107, v107, v242, v243\n"
    "v_cndmask_b32 v228, 0, v244, s[4:5]\n"
    "v_cndmask_b32 v229, 0, v245, s[4:5]\n"
    "v_cndmask_b32 v230, 0, v246, s[4:5]\n"
    "v_cndmask_b32 v231, 0, v247, s[4:5]\n"
    "ds_read_b64_tr_b16 v[248:249], v115 offset:8192\n"
    "ds_read_b64_tr_b16 v[250:251], v115 offset:8704\n"
    "s_waitcnt lgkmcnt(0)\n"
    "v_mfma_f32_16x16x32_bf16 v[124:127], v[240:243], v[248:251], v[124:127]\n"
    "v_mfma_f32_16x16x32_bf16 v[128:131], v[228:231], v[248:251], v[128:131]\n"
    "ds_read_b64_tr_b16 v[248:249], v116 offset:8192\n"
    "ds_read_b64_tr_b16 v[250:251], v116 offset:8704\n"
    "s_waitcnt lgkmcnt(0)\n"
    "v_mfma_f32_16x16x32_bf16 v[132:135], v[240:243], v[248:251], v[132:135]\n"
    "v_mfma_f32_16x16x32_bf16 v[136:139], v[228:231], v[248:251], v[136:139]\n"
    "ds_read_b64_tr_b16 v[248:249], v117 offset:8192\n"
    "ds_read_b64_tr_b16 v[250:251], v117 offset:8704\n"
    "s_waitcnt lgkmcnt(0)\n"
    "v_mfma_f32_16x16x32_bf16 v[140:143], v[240:243], v[248:251], v[140:143]\n"
    "v_mfma_f32_16x16x32_bf16 v[144:147], v[228:231], v[248:251], v[144:147]\n"
    "ds_read_b64_tr_b16 v[248:249], v118 offset:8192\n"
    "ds_read_b64_tr_b16 v[250:251], v118 offset:8704\n"
    "s_waitcnt lgkmcnt(0)\n"
    "v_mfma_f32_16x16x32_bf16 v[148:151], v[240:243], v[248:251], v[148:151]\n"
    "v_mfma_f32_16x16x32_bf16 v[152:155], v[228:231], v[248:251], v[152:155]\n"
    "s_add_u32 s81, s82, 258048\n"
    "buffer_load_dwordx4 v[240:243], v113, s[12:15], s81 offen offset:512 sc1\n"
    "s_waitcnt vmcnt(0)\n"
    "v_max3_u32 v107, v107, v240, v241\n"
    "v_max3_u32 v107, v107, v242, v243\n"
    "v_cndmask_b32 v228, v244, 0, s[4:5]\n"
    "v_cndmask_b32 v229, v245, 0, s[4:5]\n"
    "v_cndmask_b32 v230, v246, 0, s[4:5]\n"
    "v_cndmask_b32 v231, v247, 0, s[4:5]\n"
    "ds_read_b64_tr_b16 v[248:249], v115 offset:12288\n"
    "ds_read_b64_tr_b16 v[250:251], v115 offset:12800\n"
    "s_waitcnt lgkmcnt(0)\n"
    "v_mfma_f32_16x16x32_bf16 v[124:127], v[240:243], v[248:251], v[124:127]\n"
    "v_mfma_f32_16x16x32_bf16 v[128:131], v[228:231], v[248:251], v[128:131]\n"
    "ds_read_b64_tr_b16 v[248:249], v116 offset:12288\n"
    "ds_read_b64_tr_b16 v[250:251], v116 offset:12800\n"
    "s_waitcnt lgkmcnt(0)\n"
    "v_mfma_f32_16x16x32_bf16 v[132:135], v[240:243], v[248:251], v[132:135]\n"
    "v_mfma_f32_16x16x32_bf16 v[136:139], v[228:231], v[248:251], v[136:139]\n"
    "ds_read_b64_tr_b16 v[248:249], v117 offset:12288\n"
    "ds_read_b64_tr_b16 v[250:251], v117 offset:12800\n"
    "s_waitcnt lgkmcnt(0)\n"
    "v_mfma_f32_16x16x32_bf16 v[140:143], v[240:243], v[248:251], v[140:143]\n"
    "v_mfma_f32_16x16x32_bf16 v[144:147], v[228:231], v[248:251], v[144:147]\n"
    "ds_read_b64_tr_b16 v[248:249], v118 offset:12288\n"
    "ds_read_b64_tr_b16 v[250:251], v118 offset:12800\n"
    "s_waitcnt lgkmcnt(0)\n"
    "v_mfma_f32_16x16x32_bf16 v[148:151], v[240:243], v[248:251], v[148:151]\n"
    "v_mfma_f32_16x16x32_bf16 v[152:155], v[228:231], v[248:251], v[152:155]\n"
    "s_add_u32 s81, s82, 286720\n"
    "buffer_load_dwordx4 v[240:243], v113, s[12:15], s81 offen offset:512 sc1\n"
    "buffer_load_dwordx4 v[244:247], v114, s[12:15], s81 offen offset:768 sc1\n"
    "s_waitcnt vmcnt(0)\n"
    "v_max3_u32 v107, v107, v244, v245\n"
    "v_max3_u32 v107, v107, v246, v247\n"
    "v_max3_u32 v107, v107, v240, v241\n"
    "v_max3_u32 v107, v107, v242, v243\n"
    "v_cndmask_b32 v228, 0, v244, s[4:5]\n"
    "v_cndmask_b32 v229, 0, v245, s[4:5]\n"
    "v_cndmask_b32 v230, 0, v246, s[4:5]\n"
    "v_cndmask_b32 v231, 0, v247, s[4:5]\n"
    "ds_read_b64_tr_b16 v[248:249], v115 offset:16384\n"
    "ds_read_b64_tr_b16 v[250:251], v115 offset:16896\n"
    "s_waitcnt lgkmcnt(0)\n"
    "v_mfma_f32_16x16x32_bf16 v[124:127], v[240:243], v[248:251], v[124:127]\n"
    "v_mfma_f32_16x16x32_bf16 v[128:131], v[228:231], v[248:251], v[128:131]\n"
    "ds_read_b64_tr_b16 v[248:249], v116 offset:16384\n"
    "ds_read_b64_tr_b16 v[250:251], v116 offset:16896\n"
    "s_waitcnt lgkmcnt(0)\n"
    "v_mfma_f32_16x16x32_bf16 v[132:135], v[240:243], v[248:251], v[132:135]\n"
    "v_mfma_f32_16x16x32_bf16 v[136:139], v[228:231], v[248:251], v[136:139]\n"
    "ds_read_b64_tr_b16 v[248:249], v117 offset:16384\n"
    "ds_read_b64_tr_b16 v[250:251], v117 offset:16896\n"
    "s_waitcnt lgkmcnt(0)\n"
    "v_mfma_f32_16x16x32_bf16 v[140:143], v[240:243], v[248:251], v[140:143]\n"
    "v_mfma_f32_16x16x32_bf16 v[144:147], v[228:231], v[248:251], v[144:147]\n"
    "ds_read_b64_tr_b16 v[248:249], v118 offset:16384\n"
    "ds_read_b64_tr_b16 v[250:251], v118 offset:16896\n"
    "s_waitcnt lgkmcnt(0)\n"
    "v_mfma_f32_16x16x32_bf16 v[148:151], v[240:243], v[248:251], v[148:151]\n"
    "v_mfma_f32_16x16x32_bf16 v[152:155], v[228:231], v[248:251], v[152:155]\n"
    "s_add_u32 s81, s82, 315392\n"
    "buffer_load_dwordx4 v[240:243], v113, s[12:15], s81 offen offset:512 sc1\n"
    "s_waitcnt vmcnt(0)\n"
    "v_max3_u32 v107, v107, v240, v241\n"
    "v_max3_u32 v107, v107, v242, v243\n"
    "v_cndmask_b32 v228, v244, 0, s[4:5]\n"
    "v_cndmask_b32 v229, v245, 0, s[4:5]\n"
    "v_cndmask_b32 v230, v246, 0, s[4:5]\n"
    "v_cndmask_b32 v231, v247, 0, s[4:5]\n"
    "ds_read_b64_tr_b16 v[248:249], v115 offset:20480\n"
    "ds_read_b64_tr_b16 v[250:251], v115 offset:20992\n"
    "s_waitcnt lgkmcnt(0)\n"
    "v_mfma_f32_16x16x32_bf16 v[124:127], v[240:243], v[248:251], v[124:127]\n"
    "v_mfma_f32_16x16x32_bf16 v[128:131], v[228:231], v[248:251], v[128:131]\n"
    "ds_read_b64_tr_b16 v[248:249], v116 offset:20480\n"
    "ds_read_b64_tr_b16 v[250:251], v116 offset:20992\n"
    "s_waitcnt lgkmcnt(0)\n"
    "v_mfma_f32_16x16x32_bf16 v[132:135], v[240:243], v[248:251], v[132:135]\n"
    "v_mfma_f32_16x16x32_bf16 v[136:139], v[228:231], v[248:251], v[136:139]\n"
    "ds_read_b64_tr_b16 v[248:249], v117 offset:20480\n"
    "ds_read_b64_tr_b16 v[250:251], v117 offset:20992\n"
    "s_waitcnt lgkmcnt(0)\n"
    "v_mfma_f32_16x16x32_bf16 v[140:143], v[240:243], v[248:251], v[140:143]\n"
    "v_mfma_f32_16x16x32_bf16 v[144:147], v[228:231], v[248:251], v[144:147]\n"
    "ds_read_b64_tr_b16 v[248:249], v118 offset:20480\n"
    "ds_read_b64_tr_b16 v[250:251], v118 offset:20992\n"
    "s_waitcnt lgkmcnt(0)\n"
    "v_mfma_f32_16x16x32_bf16 v[148:151], v[240:243], v[248:251], v[148:151]\n"
    "v_mfma_f32_16x16x32_bf16 v[152:155], v[228:231], v[248:251], v[152:155]\n"
    "s_add_u32 s81, s82, 344064\n"
    "buffer_load_dwordx4 v[240:243], v113, s[12:15], s81 offen offset:512 sc1\n"
    "buffer_load_dwordx4 v[244:247], v114, s[12:15], s81 offen offset:768 sc1\n"
    "s_waitcnt vmcnt(0)\n"
    "v_max3_u32 v107, v107, v244, v245\n"
    "v_max3_u32 v107, v107, v246, v247\n"
    "v_max3_u32 v107, v107, v240, v241\n"
    "v_max3_u32 v107, v107, v242, v243\n"
    "v_cndmask_b32 v228, 0, v244, s[4:5]\n"
    "v_cndmask_b32 v229, 0, v245, s[4:5]\n"
    "v_cndmask_b32 v230, 0, v246, s[4:5]\n"
    "v_cndmask_b32 v231, 0, v247, s[4:5]\n"
    "ds_read_b64_tr_b16 v[248:249], v115 offset:24576\n"
    "ds_read_b64_tr_b16 v[250:251], v115 offset:25088\n"
    "s_waitcnt lgkmcnt(0)\n"
    "v_mfma_f32_16x16x32_bf16 v[124:127], v[240:243], v[248:251], v[124:127]\n"
    "v_mfma_f32_16x16x32_bf16 v[128:131], v[228:231], v[248:251], v[128:131]\n"
    "ds_read_b64_tr_b16 v[248:249], v116 offset:24576\n"
    "ds_read_b64_tr_b16 v[250:251], v116 offset:25088\n"
    "s_waitcnt lgkmcnt(0)\n"
    "v_mfma_f32_16x16x32_bf16 v[132:135], v[240:243], v[248:251], v[132:135]\n"
    "v_mfma_f32_16x16x32_bf16 v[136:139], v[228:231], v[248:251], v[136:139]\n"
    "ds_read_b64_tr_b16 v[248:249], v117 offset:24576\n"
    "ds_read_b64_tr_b16 v[250:251], v117 offset:25088\n"
    "s_waitcnt lgkmcnt(0)\n"
    "v_mfma_f32_16x16x32_bf16 v[140:143], v[240:243], v[248:251], v[140:143]\n"
    "v_mfma_f32_16x16x32_bf16 v[144:147], v[228:231], v[248:251], v[144:147]\n"
    "ds_read_b64_tr_b16 v[248:249], v118 offset:24576\n"
    "ds_read_b64_tr_b16 v[250:251], v118 offset:25088\n"
    "s_waitcnt lgkmcnt(0)\n"
    "v_mfma_f32_16x16x32_bf16 v[148:151], v[240:243], v[248:251], v[148:151]\n"
    "v_mfma_f32_16x16x32_bf16 v[152:155], v[228:231], v[248:251], v[152:155]\n"
    "s_add_u32 s81, s82, 372736\n"
    "buffer_load_dwordx4 v[240:243], v113, s[12:15], s81 offen offset:512 sc1\n"
    "s_waitcnt vmcnt(0)\n"
    "v_max3_u32 v107, v107, v240, v241\n"
    "v_max3_u32 v107, v107, v242, v243\n"
    "v_cndmask_b32 v228, v244, 0, s[4:5]\n"
    "v_cndmask_b32 v229, v245, 0, s[4:5]\n"
    "v_cndmask_b32 v230, v246, 0, s[4:5]\n"
    "v_cndmask_b32 v231, v247, 0, s[4:5]\n"
    "ds_read_b64_tr_b16 v[248:249], v115 offset:28672\n"
    "ds_read_b64_tr_b16 v[250:251], v115 offset:29184\n"
    "s_waitcnt lgkmcnt(0)\n"
    "v_mfma_f32_16x16x32_bf16 v[124:127], v[240:243], v[248:251], v[124:127]\n"
    "v_mfma_f32_16x16x32_bf16 v[128:131], v[228:231], v[248:251], v[128:131]\n"
    "ds_read_b64_tr_b16 v[248:249], v116 offset:28672\n"
    "ds_read_b64_tr_b16 v[250:251], v116 offset:29184\n"
    "s_waitcnt lgkmcnt(0)\n"
    "v_mfma_f32_16x16x32_bf16 v[132:135], v[240:243], v[248:251], v[132:135]\n"
    "v_mfma_f32_16x16x32_bf16 v[136:139], v[228:231], v[248:251], v[136:139]\n"
    "ds_read_b64_tr_b16 v[248:249], v117 offset:28672\n"
    "ds_read_b64_tr_b16 v[250:251], v117 offset:29184\n"
    "s_waitcnt lgkmcnt(0)\n"
    "v_mfma_f32_16x16x32_bf16 v[140:143], v[240:243], v[248:251], v[140:143]\n"
    "v_mfma_f32_16x16x32_bf16 v[144:147], v[228:231], v[248:251], v[144:147]\n"
    "ds_read_b64_tr_b16 v[248:249], v118 offset:28672\n"
    "ds_read_b64_tr_b16 v[250:251], v118 offset:29184\n"
    "s_waitcnt lgkmcnt(0)\n"
    "v_mfma_f32_16x16x32_bf16 v[148:151], v[240:243], v[248:251], v[148:151]\n"
    "v_mfma_f32_16x16x32_bf16 v[152:155], v[228:231], v[248:251], v[152:155]\n"
    "v_cmp_eq_u32 vcc, -1, v107\n"
    "s_cbranch_vccnz .Lredo_89\n"
    ".Lredook_90:\n"
    "s_waitcnt vmcnt(0) lgkmcnt(0)\n"
    "s_branch .Lback_88\n"
    ".Lredo_89:\n"
    "s_waitcnt vmcnt(0) lgkmcnt(0)\n"
    "s_sub_u32 s60, s60, 1\n"
    "s_cbranch_scc0 .Lclean_87\n"
    "s_mov_b32 s60, 0\n"
    "s_branch .Lredook_90\n"
    ".Lredo_91:\n"
    "s_waitcnt vmcnt(0) lgkmcnt(0)\n"
    "s_branch .Lclean_87\n"
    ".Lclean_94:\n"
    "v_mov_b32 v107, 0\n"
    "v_lshlrev_b32 v156, 16, v96\n"
    "v_and_b32 v157, 0xffff0000, v96\n"
    "v_lshlrev_b32 v158, 16, v97\n"
    "v_and_b32 v159, 0xffff0000, v97\n"
    "v_lshlrev_b32 v160, 16, v98\n"
    "v_and_b32 v161, 0xffff0000, v98\n"
    "v_lshlrev_b32 v162, 16, v99\n"
    "v_and_b32 v163, 0xffff0000, v99\n"
    "v_add_f32 v156, v156, v123\n"
    "v_add_f32 v157, v157, v123\n"
    "v_add_f32 v158, v158, v123\n"
    "v_add_f32 v159, v159, v123\n"
    "v_add_f32 v160, v160, v123\n"
    "v_add_f32 v161, v161, v123\n"
    "v_add_f32 v162, v162, v123\n"
    "v_add_f32 v163, v163, v123\n"
    "s_add_u32 s81, s82, 0\n"
    "buffer_load_dwordx4 v[240:243], v113, s[12:15], s81 offen offset:0 sc1\n"
    "buffer_load_dwordx4 v[244:247], v113, s[12:15], s81 offen offset:256 sc1\n"
    "s_waitcnt vmcnt(0)\n"
    "v_max3_u32 v107, v107, v244, v245\n"
    "v_max3_u32 v107, v107, v246, v247\n"
    "v_max3_u32 v107, v107, v240, v241\n"
    "v_max3_u32 v107, v107, v242, v243\n"
    "v_mfma_f32_16x16x32_bf16 v[156:159], v[240:243], v[0:3], v[156:159]\n"
    "v_mfma_f32_16x16x32_bf16 v[160:163], v[244:247], v[0:3], v[160:163]\n"
    "v_mfma_f32_16x16x32_bf16 v[164:167], v[240:243], v[4:7], 0\n"
    "v_mfma_f32_16x16x32_bf16 v[168:171], v[244:247], v[4:7], 0\n"
    "v_mfma_f32_16x16x32_bf16 v[172:175], v[240:243], v[8:11], 0\n"
    "v_mfma_f32_16x16x32_bf16 v[176:179], v[244:247], v[8:11], 0\n"
    "v_mfma_f32_16x16x32_bf16 v[180:183], v[240:243], v[12:15], 0\n"
    "v_mfma_f32_16x16x32_bf16 v[184:187], v[244:247], v[12:15], 0\n"
    "s_add_u32 s81, s82, 28672\n"
    "buffer_load_dwordx4 v[240:243], v113, s[12:15], s81 offen offset:0 sc1\n"
    "buffer_load_dwordx4 v[244:247], v113, s[12:15], s81 offen offset:256 sc1\n"
    "s_waitcnt vmcnt(0)\n"
    "v_max3_u32 v107, v107, v244, v245\n"
    "v_max3_u32 v107, v107, v246, v247\n"
    "v_max3_u32 v107, v107, v240, v241\n"
    "v_max3_u32 v107, v107, v242, v243\n"
    "v_mfma_f32_16x16x32_bf16 v[156:159], v[240:243], v[16:19], v[156:159]\n"
    "v_mfma_f32_16x16x32_bf16 v[160:163], v[244:247], v[16:19], v[160:163]\n"
    "v_mfma_f32_16x16x32_bf16 v[164:167], v[240:243], v[20:23], v[164:167]\n"
    "v_mfma_f32_16x16x32_bf16 v[168:171], v[244:247], v[20:23], v[168:171]\n"
    "v_mfma_f32_16x16x32_bf16 v[172:175], v[240:243], v[24:27], v[172:175]\n"
    "v_mfma_f32_16x16x32_bf16 v[176:179], v[244:247], v[24:27], v[176:179]\n"
    "v_mfma_f32_16x16x32_bf16 v[180:183], v[240:243], v[28:31], v[180:183]\n"
    "v_mfma_f32_16x16x32_bf16 v[184:187], v[244:247], v[28:31], v[184:187]\n"
    "s_add_u32 s81, s82, 57344\n"
    "buffer_load_dwordx4 v[240:243], v113, s[12:15], s81 offen offset:0 sc1\n"
    "buffer_load_dwordx4 v[244:247], v113, s[12:15], s81 offen offset:256 sc1\n"
    "s_waitcnt vmcnt(0)\n"
    "v_max3_u32 v107, v107, v244, v245\n"
    "v_max3_u32 v107, v107, v246, v247\n"
    "v_max3_u32 v107, v107, v240, v241\n"
    "v_max3_u32 v107, v107, v242, v243\n"
    "v_mfma_f32_16x16x32_bf16 v[156:159], v[240:243], v[32:35], v[156:159]\n"
    "v_mfma_f32_16x16x32_bf16 v[160:163], v[244:247], v[32:35], v[160:163]\n"
    "v_mfma_f32_16x16x32_bf16 v[164:167], v[240:243], v[36:39], v[164:167]\n"
    "v_mfma_f32_16x16x32_bf16 v[168:171], v[244:247], v[36:39], v[168:171]\n"
    "v_mfma_f32_16x16x32_bf16 v[172:175], v[240:243], v[40:43], v[172:175]\n"
    "v_mfma_f32_16x16x32_bf16 v[176:179], v[244:247], v[40:43], v[176:179]\n"
    "v_mfma_f32_16x16x32_bf16 v[180:183], v[240:243], v[44:47], v[180:183]\n"
    "v_mfma_f32_16x16x32_bf16 v[184:187], v[244:247], v[44:47], v[184:187]\n"
    "s_add_u32 s81, s82, 86016\n"
    "buffer_load_dwordx4 v[240:243], v113, s[12:15], s81 offen offset:0 sc1\n"
    "buffer_load_dwordx4 v[244:247], v113, s[12:15], s81 offen offset:256 sc1\n"
    "s_waitcnt vmcnt(0)\n"
    "v_max3_u32 v107, v107, v244, v245\n"
    "v_max3_u32 v107, v107, v246, v247\n"
    "v_max3_u32 v107, v107, v240, v241\n"
    "v_max3_u32 v107, v107, v242, v243\n"
    "v_mfma_f32_16x16x32_bf16 v[156:159], v[240:243], v[48:51], v[156:159]\n"
    "v_mfma_f32_16x16x32_bf16 v[160:163], v[244:247], v[48:51], v[160:163]\n"
    "v_mfma_f32_16x16x32_bf16 v[164:167], v[240:243], v[52:55], v[164:167]\n"
    "v_mfma_f32_16x16x32_bf16 v[168:171], v[244:247], v[52:55], v[168:171]\n"
    "v_mfma_f32_16x16x32_bf16 v[172:175], v[240:243], v[56:59], v[172:175]\n"
    "v_mfma_f32_16x16x32_bf16 v[176:179], v[244:247], v[56:59], v[176:179]\n"
    "v_mfma_f32_16x16x32_bf16 v[180:183], v[240:243], v[60:63], v[180:183]\n"
    "v_mfma_f32_16x16x32_bf16 v[184:187], v[244:247], v[60:63], v[184:187]\n"
    "s_add_u32 s81, s82, 114688\n"
    "buffer_load_dwordx4 v[240:243], v113, s[12:15], s81 offen offset:0 sc1\n"
    "buffer_load_dwordx4 v[244:247], v113, s[12:15], s81 offen offset:256 sc1\n"
    "s_waitcnt vmcnt(0)\n"
    "v_max3_u32 v107, v107, v244, v245\n"
    "v_max3_u32 v107, v107, v246, v247\n"
    "v_max3_u32 v107, v107, v240, v241\n"
    "v_max3_u32 v107, v107, v242, v243\n"
    "v_mfma_f32_16x16x32_bf16 v[156:159], v[240:243], v[64:67], v[156:159]\n"
    "v_mfma_f32_16x16x32_bf16 v[160:163], v[244:247], v[64:67], v[160:163]\n"
    "v_mfma_f32_16x16x32_bf16 v[164:167], v[240:243], v[68:71], v[164:167]\n"
    "v_mfma_f32_16x16x32_bf16 v[168:171], v[244:247], v[68:71], v[168:171]\n"
    "v_mfma_f32_16x16x32_bf16 v[172:175], v[240:243], v[72:75], v[172:175]\n"
    "v_mfma_f32_16x16x32_bf16 v[176:179], v[244:247], v[72:75], v[176:179]\n"
    "v_mfma_f32_16x16x32_bf16 v[180:183], v[240:243], v[76:79], v[180:183]\n"
    "v_mfma_f32_16x16x32_bf16 v[184:187], v[244:247], v[76:79], v[184:187]\n"
    "s_add_u32 s81, s82, 143360\n"
    "buffer_load_dwordx4 v[240:243], v113, s[12:15], s81 offen offset:0 sc1\n"
    "buffer_load_dwordx4 v[244:247], v113, s[12:15], s81 offen offset:256 sc1\n"
    "s_waitcnt vmcnt(0)\n"
    "v_max3_u32 v107, v107, v244, v245\n"
    "v_max3_u32 v107, v107, v246, v247\n"
    "v_max3_u32 v107, v107, v240, v241\n"
    "v_max3_u32 v107, v107, v242, v243\n"
    "v_mfma_f32_16x16x32_bf16 v[156:159], v[240:243], v[80:83], v[156:159]\n"
    "v_mfma_f32_16x16x32_bf16 v[160:163], v[244:247], v[80:83], v[160:163]\n"
    "v_mfma_f32_16x16x32_bf16 v[164:167], v[240:243], v[84:87], v[164:167]\n"
    "v_mfma_f32_16x16x32_bf16 v[168:171], v[244:247], v[84:87], v[168:171]\n"
    "v_mfma_f32_16x16x32_bf16 v[172:175], v[240:243], v[88:91], v[172:175]\n"
    "v_mfma_f32_16x16x32_bf16 v[176:179], v[244:247], v[88:91], v[176:179]\n"
    "v_mfma_f32_16x16x32_bf16 v[180:183], v[240:243], v[92:95], v[180:183]\n"
    "v_mfma_f32_16x16x32_bf16 v[184:187], v[244:247], v[92:95], v[184:187]\n"
    "s_add_u32 s81, s82, 172032\n"
    "buffer_load_dwordx4 v[240:243], v113, s[12:15], s81 offen offset:0 sc1\n"
    "buffer_load_dwordx4 v[244:247], v113, s[12:15], s81 offen offset:256 sc1\n"
    "s_waitcnt vmcnt(0)\n"
    "v_max3_u32 v107, v107, v244, v245\n"
    "v_max3_u32 v107, v107, v246, v247\n"
    "v_max3_u32 v107, v107, v240, v241\n"
    "v_max3_u32 v107, v107, v242, v243\n"
    "ds_read_b64_tr_b16 v[248:249], v115 offset:0\n"
    "ds_read_b64_tr_b16 v[250:251], v115 offset:512\n"
    "s_waitcnt lgkmcnt(0)\n"
    "v_mfma_f32_16x16x32_bf16 v[156:159], v[240:243], v[248:251], v[156:159]\n"
    "v_mfma_f32_16x16x32_bf16 v[160:163], v[244:247], v[248:251], v[160:163]\n"
    "ds_read_b64_tr_b16 v[248:249], v116 offset:0\n"
    "ds_read_b64_tr_b16 v[250:251], v116 offset:512\n"
    "s_waitcnt lgkmcnt(0)\n"
    "v_mfma_f32_16x16x32_bf16 v[164:167], v[240:243], v[248:251], v[164:167]\n"
    "v_mfma_f32_16x16x32_bf16 v[168:171], v[244:247], v[248:251], v[168:171]\n"
    "ds_read_b64_tr_b16 v[248:249], v117 offset:0\n"
    "ds_read_b64_tr_b16 v[250:251], v117 offset:512\n"
    "s_waitcnt lgkmcnt(0)\n"
    "v_mfma_f32_16x16x32_bf16 v[172:175], v[240:243], v[248:251], v[172:175]\n"
    "v_mfma_f32_16x16x32_bf16 v[176:179], v[244:247], v[248:251], v[176:179]\n"
    "ds_read_b64_tr_b16 v[248:249], v118 offset:0\n"
    "ds_read_b64_tr_b16 v[250:251], v118 offset:512\n"
    "s_waitcnt lgkmcnt(0)\n"
    "v_mfma_f32_16x16x32_bf16 v[180:183], v[240:243], v[248:251], v[180:183]\n"
    "v_mfma_f32_16x16x32_bf16 v[184:187], v[244:247], v[248:251], v[184:187]\n"
    "s_add_u32 s81, s82, 200704\n"
    "buffer_load_dwordx4 v[240:243], v113, s[12:15], s81 offen offset:0 sc1\n"
    "buffer_load_dwordx4 v[244:247], v113, s[12:15], s81 offen offset:256 sc1\n"
    "s_waitcnt vmcnt(0)\n"
    "v_max3_u32 v107, v107, v244, v245\n"
    "v_max3_u32 v107, v107, v246, v247\n"
    "v_max3_u32 v107, v107, v240, v241\n"
    "v_max3_u32 v107, v107, v242, v243\n"
    "ds_read_b64_tr_b16 v[248:249], v115 offset:4096\n"
    "ds_read_b64_tr_b16 v[250:251], v115 offset:4608\n"
    "s_waitcnt lgkmcnt(0)\n"
    "v_mfma_f32_16x16x32_bf16 v[156:159], v[240:243], v[248:251], v[156:159]\n"
    "v_mfma_f32_16x16x32_bf16 v[160:163], v[244:247], v[248:251], v[160:163]\n"
    "ds_read_b64_tr_b16 v[248:249], v116 offset:4096\n"
    "ds_read_b64_tr_b16 v[250:251], v116 offset:4608\n"
    "s_waitcnt lgkmcnt(0)\n"
    "v_mfma_f32_16x16x32_bf16 v[164:167], v[240:243], v[248:251], v[164:167]\n"
    "v_mfma_f32_16x16x32_bf16 v[168:171], v[244:247], v[248:251], v[168:171]\n"
    "ds_read_b64_tr_b16 v[248:249], v117 offset:4096\n"
    "ds_read_b64_tr_b16 v[250:251], v117 offset:4608\n"
    "s_waitcnt lgkmcnt(0)\n"
    "v_mfma_f32_16x16x32_bf16 v[172:175], v[240:243], v[248:251], v[172:175]\n"
    "v_mfma_f32_16x16x32_bf16 v[176:179], v[244:247], v[248:251], v[176:179]\n"
    "ds_read_b64_tr_b16 v[248:249], v118 offset:4096\n"
    "ds_read_b64_tr_b16 v[250:251], v118 offset:4608\n"
    "s_waitcnt lgkmcnt(0)\n"
    "v_mfma_f32_16x16x32_bf16 v[180:183], v[240:243], v[248:251], v[180:183]\n"
    "v_mfma_f32_16x16x32_bf16 v[184:187], v[244:247], v[248:251], v[184:187]\n"
    "s_add_u32 s81, s82, 229376\n"
    "buffer_load_dwordx4 v[240:243], v113, s[12:15], s81 offen offset:0 sc1\n"
    "buffer_load_dwordx4 v[244:247], v113, s[12:15], s81 offen offset:256 sc1\n"
    "s_waitcnt vmcnt(0)\n"
    "v_max3_u32 v107, v107, v244, v245\n"
    "v_max3_u32 v107, v107, v246, v247\n"
    "v_max3_u32 v107, v107, v240, v241\n"
    "v_max3_u32 v107, v107, v242, v243\n"
    "ds_read_b64_tr_b16 v[248:249], v115 offset:8192\n"
    "ds_read_b64_tr_b16 v[250:251], v115 offset:8704\n"
    "s_waitcnt lgkmcnt(0)\n"
    "v_mfma_f32_16x16x32_bf16 v[156:159], v[240:243], v[248:251], v[156:159]\n"
    "v_mfma_f32_16x16x32_bf16 v[160:163], v[244:247], v[248:251], v[160:163]\n"
    "ds_read_b64_tr_b16 v[248:249], v116 offset:8192\n"
    "ds_read_b64_tr_b16 v[250:251], v116 offset:8704\n"
    "s_waitcnt lgkmcnt(0)\n"
    "v_mfma_f32_16x16x32_bf16 v[164:167], v[240:243], v[248:251], v[164:167]\n"
    "v_mfma_f32_16x16x32_bf16 v[168:171], v[244:247], v[248:251], v[168:171]\n"
    "ds_read_b64_tr_b16 v[248:249], v117 offset:8192\n"
    "ds_read_b64_tr_b16 v[250:251], v117 offset:8704\n"
    "s_waitcnt lgkmcnt(0)\n"
    "v_mfma_f32_16x16x32_bf16 v[172:175], v[240:243], v[248:251], v[172:175]\n"
    "v_mfma_f32_16x16x32_bf16 v[176:179], v[244:247], v[248:251], v[176:179]\n"
    "ds_read_b64_tr_b16 v[248:249], v118 offset:8192\n"
    "ds_read_b64_tr_b16 v[250:251], v118 offset:8704\n"
    "s_waitcnt lgkmcnt(0)\n"
    "v_mfma_f32_16x16x32_bf16 v[180:183], v[240:243], v[248:251], v[180:183]\n"
    "v_mfma_f32_16x16x32_bf16 v[184:187], v[244:247], v[248:251], v[184:187]\n"
    "s_add_u32 s81, s82, 258048\n"
    "buffer_load_dwordx4 v[240:243], v113, s[12:15], s81 offen offset:0 sc1\n"
    "buffer_load_dwordx4 v[244:247], v113, s[12:15], s81 offen offset:256 sc1\n"
    "s_waitcnt vmcnt(0)\n"
    "v_max3_u32 v107, v107, v244, v245\n"
    "v_max3_u32 v107, v107, v246, v247\n"
    "v_max3_u32 v107, v107, v240, v241\n"
    "v_max3_u32 v107, v107, v242, v243\n"
    "ds_read_b64_tr_b16 v[248:249], v115 offset:12288\n"
    "ds_read_b64_tr_b16 v[250:251], v115 offset:12800\n"
    "s_waitcnt lgkmcnt(0)\n"
    "v_mfma_f32_16x16x32_bf16 v[156:159], v[240:243], v[248:251], v[156:159]\n"
    "v_mfma_f32_16x16x32_bf16 v[160:163], v[244:247], v[248:251], v[160:163]\n"
    "ds_read_b64_tr_b16 v[248:249], v116 offset:12288\n"
    "ds_read_b64_tr_b16 v[250:251], v116 offset:12800\n"
    "s_waitcnt lgkmcnt(0)\n"
    "v_mfma_f32_16x16x32_bf16 v[164:167], v[240:243], v[248:251], v[164:167]\n"
    "v_mfma_f32_16x16x32_bf16 v[168:171], v[244:247], v[248:251], v[168:171]\n"
    "ds_read_b64_tr_b16 v[248:249], v117 offset:12288\n"
    "ds_read_b64_tr_b16 v[250:251], v117 offset:12800\n"
    "s_waitcnt lgkmcnt(0)\n"
    "v_mfma_f32_16x16x32_bf16 v[172:175], v[240:243], v[248:251], v[172:175]\n"
    "v_mfma_f32_16x16x32_bf16 v[176:179], v[244:247], v[248:251], v[176:179]\n"
    "ds_read_b64_tr_b16 v[248:249], v118 offset:12288\n"
    "ds_read_b64_tr_b16 v[250:251], v118 offset:12800\n"
    "s_waitcnt lgkmcnt(0)\n"
    "v_mfma_f32_16x16x32_bf16 v[180:183], v[240:243], v[248:251], v[180:183]\n"
    "v_mfma_f32_16x16x32_bf16 v[184:187], v[244:247], v[248:251], v[184:187]\n"
    "s_add_u32 s81, s82, 286720\n"
    "buffer_load_dwordx4 v[240:243], v113, s[12:15], s81 offen offset:0 sc1\n"
    "buffer_load_dwordx4 v[244:247], v113, s[12:15], s81 offen offset:256 sc1\n"
    "s_waitcnt vmcnt(0)\n"
    "v_max3_u32 v107, v107, v244, v245\n"
    "v_max3_u32 v107, v107, v246, v247\n"
    "v_max3_u32 v107, v107, v240, v241\n"
    "v_max3_u32 v107, v107, v242, v243\n"
    "ds_read_b64_tr_b16 v[248:249], v115 offset:16384\n"
    "ds_read_b64_tr_b16 v[250:251], v115 offset:16896\n"
    "s_waitcnt lgkmcnt(0)\n"
    "v_mfma_f32_16x16x32_bf16 v[156:159], v[240:243], v[248:251], v[156:159]\n"
    "v_mfma_f32_16x16x32_bf16 v[160:163], v[244:247], v[248:251], v[160:163]\n"
    "ds_read_b64_tr_b16 v[248:249], v116 offset:16384\n"
    "ds_read_b64_tr_b16 v[250:251], v116 offset:16896\n"
    "s_waitcnt lgkmcnt(0)\n"
    "v_mfma_f32_16x16x32_bf16 v[164:167], v[240:243], v[248:251], v[164:167]\n"
    "v_mfma_f32_16x16x32_bf16 v[168:171], v[244:247], v[248:251], v[168:171]\n"
    "ds_read_b64_tr_b16 v[248:249], v117 offset:16384\n"
    "ds_read_b64_tr_b16 v[250:251], v117 offset:16896\n"
    "s_waitcnt lgkmcnt(0)\n"
    "v_mfma_f32_16x16x32_bf16 v[172:175], v[240:243], v[248:251], v[172:175]\n"
    "v_mfma_f32_16x16x32_bf16 v[176:179], v[244:247], v[248:251], v[176:179]\n"
    "ds_read_b64_tr_b16 v[248:249], v118 offset:16384\n"
    "ds_read_b64_tr_b16 v[250:251], v118 offset:16896\n"
    "s_waitcnt lgkmcnt(0)\n"
    "v_mfma_f32_16x16x32_bf16 v[180:183], v[240:243], v[248:251], v[180:183]\n"
    "v_mfma_f32_16x16x32_bf16 v[184:187], v[244:247], v[248:251], v[184:187]\n"
    "s_add_u32 s81, s82, 315392\n"
    "buffer_load_dwordx4 v[240:243], v113, s[12:15], s81 offen offset:0 sc1\n"
    "buffer_load_dwordx4 v[244:247], v113, s[12:15], s81 offen offset:256 sc1\n"
    "s_waitcnt vmcnt(0)\n"
    "v_max3_u32 v107, v107, v244, v245\n"
    "v_max3_u32 v107, v107, v246, v247\n"
    "v_max3_u32 v107, v107, v240, v241\n"
    "v_max3_u32 v107, v107, v242, v243\n"
    "ds_read_b64_tr_b16 v[248:249], v115 offset:20480\n"
    "ds_read_b64_tr_b16 v[250:251], v115 offset:20992\n"
    "s_waitcnt lgkmcnt(0)\n"
    "v_mfma_f32_16x16x32_bf16 v[156:159], v[240:243], v[248:251], v[156:159]\n"
    "v_mfma_f32_16x16x32_bf16 v[160:163], v[244:247], v[248:251], v[160:163]\n"
    "ds_read_b64_tr_b16 v[248:249], v116 offset:20480\n"
    "ds_read_b64_tr_b16 v[250:251], v116 offset:20992\n"
    "s_waitcnt lgkmcnt(0)\n"
    "v_mfma_f32_16x16x32_bf16 v[164:167], v[240:243], v[248:251], v[164:167]\n"
    "v_mfma_f32_16x16x32_bf16 v[168:171], v[244:247], v[248:251], v[168:171]\n"
    "ds_read_b64_tr_b16 v[248:249], v117 offset:20480\n"
    "ds_read_b64_tr_b16 v[250:251], v117 offset:20992\n"
    "s_waitcnt lgkmcnt(0)\n"
    "v_mfma_f32_16x16x32_bf16 v[172:175], v[240:243], v[248:251], v[172:175]\n"
    "v_mfma_f32_16x16x32_bf16 v[176:179], v[244:247], v[248:251], v[176:179]\n"
    "ds_read_b64_tr_b16 v[248:249], v118 offset:20480\n"
    "ds_read_b64_tr_b16 v[250:251], v118 offset:20992\n"
    "s_waitcnt lgkmcnt(0)\n"
    "v_mfma_f32_16x16x32_bf16 v[180:183], v[240:243], v[248:251], v[180:183]\n"
    "v_mfma_f32_16x16x32_bf16 v[184:187], v[244:247], v[248:251], v[184:187]\n"
    "s_add_u32 s81, s82, 344064\n"
    "buffer_load_dwordx4 v[240:243], v113, s[12:15], s81 offen offset:0 sc1\n"
    "buffer_load_dwordx4 v[244:247], v113, s[12:15], s81 offen offset:256 sc1\n"
    "s_waitcnt vmcnt(0)\n"
    "v_max3_u32 v107, v107, v244, v245\n"
    "v_max3_u32 v107, v107, v246, v247\n"
    "v_max3_u32 v107, v107, v240, v241\n"
    "v_max3_u32 v107, v107, v242, v243\n"
    "ds_read_b64_tr_b16 v[248:249], v115 offset:24576\n"
    "ds_read_b64_tr_b16 v[250:251], v115 offset:25088\n"
    "s_waitcnt lgkmcnt(0)\n"
    "v_mfma_f32_16x16x32_bf16 v[156:159], v[240:243], v[248:251], v[156:159]\n"
    "v_mfma_f32_16x16x32_bf16 v[160:163], v[244:247], v[248:251], v[160:163]\n"
    "ds_read_b64_tr_b16 v[248:249], v116 offset:24576\n"
    "ds_read_b64_tr_b16 v[250:251], v116 offset:25088\n"
    "s_waitcnt lgkmcnt(0)\n"
    "v_mfma_f32_16x16x32_bf16 v[164:167], v[240:243], v[248:251], v[164:167]\n"
    "v_mfma_f32_16x16x32_bf16 v[168:171], v[244:247], v[248:251], v[168:171]\n"
    "ds_read_b64_tr_b16 v[248:249], v117 offset:24576\n"
    "ds_read_b64_tr_b16 v[250:251], v117 offset:25088\n"
    "s_waitcnt lgkmcnt(0)\n"
    "v_mfma_f32_16x16x32_bf16 v[172:175], v[240:243], v[248:251], v[172:175]\n"
    "v_mfma_f32_16x16x32_bf16 v[176:179], v[244:247], v[248:251], v[176:179]\n"
    "ds_read_b64_tr_b16 v[248:249], v118 offset:24576\n"
    "ds_read_b64_tr_b16 v[250:251], v118 offset:25088\n"
    "s_waitcnt lgkmcnt(0)\n"
    "v_mfma_f32_16x16x32_bf16 v[180:183], v[240:243], v[248:251], v[180:183]\n"
    "v_mfma_f32_16x16x32_bf16 v[184:187], v[244:247], v[248:251], v[184:187]\n"
    "s_add_u32 s81, s82, 372736\n"
    "buffer_load_dwordx4 v[240:243], v113, s[12:15], s81 offen offset:0 sc1\n"
    "buffer_load_dwordx4 v[244:247], v113, s[12:15], s81 offen offset:256 sc1\n"
    "s_waitcnt vmcnt(0)\n"
    "v_max3_u32 v107, v107, v244, v245\n"
    "v_max3_u32 v107, v107, v246, v247\n"
    "v_max3_u32 v107, v107, v240, v241\n"
    "v_max3_u32 v107, v107, v242, v243\n"
    "ds_read_b64_tr_b16 v[248:249], v115 offset:28672\n"
    "ds_read_b64_tr_b16 v[250:251], v115 offset:29184\n"
    "s_waitcnt lgkmcnt(0)\n"
    "v_mfma_f32_16x16x32_bf16 v[156:159], v[240:243], v[248:251], v[156:159]\n"
    "v_mfma_f32_16x16x32_bf16 v[160:163], v[244:247], v[248:251], v[160:163]\n"
    "ds_read_b64_tr_b16 v[248:249], v116 offset:28672\n"
    "ds_read_b64_tr_b16 v[250:251], v116 offset:29184\n"
    "s_waitcnt lgkmcnt(0)\n"
    "v_mfma_f32_16x16x32_bf16 v[164:167], v[240:243], v[248:251], v[164:167]\n"
    "v_mfma_f32_16x16x32_bf16 v[168:171], v[244:247], v[248:251], v[168:171]\n"
    "ds_read_b64_tr_b16 v[248:249], v117 offset:28672\n"
    "ds_read_b64_tr_b16 v[250:251], v117 offset:29184\n"
    "s_waitcnt lgkmcnt(0)\n"
    "v_mfma_f32_16x16x32_bf16 v[172:175], v[240:243], v[248:251], v[172:175]\n"
    "v_mfma_f32_16x16x32_bf16 v[176:179], v[244:247], v[248:251], v[176:179]\n"
    "ds_read_b64_tr_b16 v[248:249], v118 offset:28672\n"
    "ds_read_b64_tr_b16 v[250:251], v118 offset:29184\n"
    "s_waitcnt lgkmcnt(0)\n"
    "v_mfma_f32_16x16x32_bf16 v[180:183], v[240:243], v[248:251], v[180:183]\n"
    "v_mfma_f32_16x16x32_bf16 v[184:187], v[244:247], v[248:251], v[184:187]\n"
    "v_cmp_eq_u32 vcc, -1, v107\n"
    "s_cbranch_vccnz .Lredo_96\n"
    ".Lredook_97:\n"
    "ds_write_b128 v119, v[164:167] offset:0\n"
    "ds_write_b128 v119, v[168:171] offset:1024\n"
    "ds_write_b128 v119, v[172:175] offset:2048\n"
    "ds_write_b128 v119, v[176:179] offset:3072\n"
    "ds_write_b128 v119, v[180:183] offset:4096\n"
    "s_nop 0\n"
    "ds_write_b128 v119, v[184:187] offset:5120\n"
    "s_waitcnt vmcnt(0) lgkmcnt(0)\n"
    "s_branch .Lback_95\n"
    ".Lredo_96:\n"
    "s_waitcnt vmcnt(0) lgkmcnt(0)\n"
    "s_sub_u32 s60, s60, 1\n"
    "s_cbranch_scc0 .Lclean_94\n"
    "s_mov_b32 s60, 0\n"
    "s_branch .Lredook_97\n"
    ".Lredo_98:\n"
    "s_waitcnt vmcnt(0) lgkmcnt(0)\n"
    "s_branch .Lclean_94\n"
    ".Lcode_end:\n"
    : : "s"(__builtin_amdgcn_kernarg_segment_ptr()), "s"(blockIdx.x), "v"(tid)
    : "v1", "v2", "v3", "v4", "v5", "v6", "v7", "v8", "v9", "v10", "v11", "v12", "v13", "v14", "v15", "v16", "v17", "v18", "v19", "v20", "v21", "v22", "v23", "v24", "v25", "v26", "v27", "v28", "v29", "v30", "v31", "v32", "v33", "v34", "v35", "v36", "v37", "v38", "v39", "v40", "v41", "v42", "v43", "v44", "v45", "v46", "v47", "v48", "v49", "v50", "v51", "v52", "v53", "v54", "v55", "v56", "v57", "v58", "v59", "v60", "v61", "v62", "v63", "v64", "v65", "v66", "v67", "v68", "v69", "v70", "v71", "v72", "v73", "v74", "v75", "v76", "v77", "v78", "v79", "v80", "v81", "v82", "v83", "v84", "v85", "v86", "v87", "v88", "v89", "v90", "v91", "v92", "v93", "v94", "v95", "v96", "v97", "v98", "v99", "v100", "v101", "v102", "v103", "v104", "v105", "v106", "v107", "v108", "v109", "v110", "v111", "v112", "v113", "v114", "v115", "v116", "v117", "v118", "v119", "v120", "v121", "v122", "v123", "v124", "v125", "v126", "v127", "v128", "v129", "v130", "v131", "v132", "v133", "v134", "v135", "v136", "v137", "v138", "v139", "v140", "v141", "v142", "v143", "v144", "v145", "v146", "v147", "v148", "v149", "v150", "v151", "v152", "v153", "v154", "v155", "v156", "v157", "v158", "v159", "v160", "v161", "v162", "v163", "v164", "v165", "v166", "v167", "v168", "v169", "v170", "v171", "v172", "v173", "v174", "v175", "v176", "v177", "v178", "v179", "v180", "v181", "v182", "v183", "v184", "v185", "v186", "v187", "v188", "v189", "v190", "v191", "v192", "v193", "v194", "v195", "v196", "v197", "v198", "v199", "v200", "v201", "v202", "v203", "v204", "v205", "v206", "v207", "v208", "v209", "v210", "v211", "v212", "v213", "v214", "v215", "v216", "v217", "v218", "v219", "v220", "v221", "v222", "v223", "v224", "v225", "v226", "v227", "v228", "v229", "v230", "v231", "v232", "v233", "v234", "v235", "v236", "v237", "v238", "v239", "v240", "v241", "v242", "v243", "v244", "v245", "v246", "v247", "v248", "v249", "v250", "v251", "v252", "v253", "v254", "v255", "a0", "a1", "a2", "a3", "a4", "a5", "a6", "a7", "a8", "a9", "a10", "a11", "a12", "a13", "a14", "a15", "a16", "a17", "a18", "a19", "a20", "a21", "a22", "a23", "a24", "a25", "a26", "a27", "a28", "a29", "a30", "a31", "a32", "a33", "a34", "a35", "a36", "a37", "a38", "a39", "a40", "a41", "a42", "a43", "a44", "a45", "a46", "a47", "a48", "a49", "a50", "a51", "a52", "a53", "a54", "a55", "a56", "a57", "a58", "a59", "a60", "a61", "a62", "a63", "a64", "a65", "a66", "a67", "a68", "a69", "a70", "a71", "a72", "a73", "a74", "a75", "a76", "a77", "a78", "a79", "a80", "a81", "a82", "a83", "a84", "a85", "a86", "a87", "a88", "a89", "a90", "a91", "a92", "a93", "a94", "a95", "a96", "a97", "a98", "a99", "a100", "a101", "a102", "a103", "a104", "a105", "a106", "a107", "a108", "a109", "a110", "a111", "a112", "a113", "a114", "a115", "a116", "a117", "a118", "a119", "a120", "a121", "a122", "a123", "a124", "a125", "a126", "a127", "a128", "a129", "a130", "a131", "a132", "a133", "a134", "a135", "a136", "a137", "a138", "a139", "a140", "a141", "a142", "a143", "a144", "a145", "a146", "a147", "a148", "a149", "a150", "a151", "a152", "a153", "a154", "a155", "a156", "a157", "a158", "a159", "a160", "a161", "a162", "a163", "a164", "a165", "a166", "a167", "a168", "a169", "a170", "a171", "a172", "a173", "a174", "a175", "a176", "a177", "a178", "a179", "a180", "a181", "a182", "a183", "a184", "a185", "a186", "a187", "a188", "a189", "a190", "a191", "a192", "a193", "a194", "a195", "a196", "a197", "a198", "a199", "a200", "a201", "a202", "a203", "a204", "a205", "a206", "a207", "a208", "a209", "a210", "a211", "a212", "a213", "a214", "a215", "a216", "a217", "a218", "a219", "a220", "a221", "a222", "a223", "a224", "a225", "a226", "a227", "a228", "a229", "a230", "a231", "a232", "a233", "a234", "a235", "a236", "a237", "a238", "a239", "a240", "a241", "a242", "a243", "a244", "a245", "a246", "a247", "a248", "a249", "a250", "a251", "a252", "a253", "a254", "a255", "s3", "s4", "s5", "s6", "s7", "s8", "s9", "s10", "s11", "s12", "s13", "s14", "s15", "s16", "s17", "s18", "s19", "s20", "s21", "s22", "s23", "s24", "s25", "s26", "s27", "s28", "s29", "s30", "s31", "s33", "s34", "s35", "s36", "s37", "s38", "s39", "s40", "s41", "s42", "s43", "s44", "s45", "s46", "s47", "s48", "s49", "s50", "s51", "s52", "s53", "s54", "s55", "s56", "s57", "s58", "s59", "s60", "s61", "s62", "s63", "s64", "s65", "s66", "s67", "s68", "s69", "s70", "s71", "s72", "s73", "s74", "s75", "s76", "s77", "s78", "s79", "s80", "s81", "s82", "s83", "s84", "s85", "s86", "s87", "s88", "s89", "s90", "s91", "s92", "s93", "s94", "s95", "s96", "s97", "s98", "s99", "vcc", "scc", "memory");
}

extern "C" void kernel_launch(void* const* d_in, const int* in_sizes, int n_in, void* d_out, int out_size, void* d_ws,
                              size_t ws_size, hipStream_t stream) {
  Params p;
  p.x = d_in[0]; p.wr = d_in[1]; p.wi = d_in[2]; p.w1 = d_in[4]; p.b1 = d_in[5]; p.w2 = d_in[6]; p.b2 = d_in[7];
  p.out = d_out; p.ws = d_ws;
  (void)hipFuncSetAttribute((const void*)interleave_layers__spectral_gfnet__mixer_token_mlp__prompt_kernel, hipFuncAttributeMaxDynamicSharedMemorySize, 163840);
  interleave_layers__spectral_gfnet__mixer_token_mlp__prompt_kernel<<<256, 256, 163840, stream>>>(p);
}
```
